```python
import torch

def _softmax(x, axis=-1):
    return torch.softmax(x, dim=axis)
def _gelu(x):
    return torch.nn.functional.gelu(x, approximate="tanh")
def _silu(x):
    return torch.nn.functional.silu(x)
def _sigmoid(x):
    return torch.sigmoid(x)
def _layernorm(x, axis=-1, eps=1e-5):
    m = x.mean(dim=axis, keepdim=True)
    v = x.var(dim=axis, keepdim=True, unbiased=False)
    return (x - m) / torch.sqrt(v + eps)
def _scatter_add(tgt, idx, src, axis=0):
    return tgt.clone().index_add_(axis, idx, src)
def _segment_sum(src, seg, n):
    out = torch.zeros((n,) + src.shape[1:], dtype=src.dtype)
    return out.index_add_(0, seg, src)
def _pad(x, pw):
    flat = tuple(v for pair in reversed(pw) for v in pair)
    return torch.nn.functional.pad(x, flat)

kernel_name = 'recurrent_scan__softmax_full_mha__mixer_s_mlp__prompt'

def setup_inputs():

    _w = (lambda a: (lambda t: t.to(torch.bfloat16) if t.is_floating_point() else t)(torch.as_tensor(a)))
    def _A_setup():
        B, S, D = 1, 2048, 576
        return {
            "x": _w((lambda *s: torch.randn(*s, dtype=torch.bfloat16) * 0.05)(B, S, D)),
            "wqkv": _w((lambda *s: torch.randn(*s, dtype=torch.bfloat16) * 0.05)(D, 3 * D)),
            "wo": _w((lambda *s: torch.randn(*s, dtype=torch.bfloat16) * 0.05)(D, D)),
        }
    _A_in = _A_setup()

    def _B_setup():
        B, S, D, F = 1, 2048, 576, 4608
        return {
            "x":  _w((lambda *s: torch.randn(*s, dtype=torch.bfloat16) * 0.05)(B, S, D)),
            "w1": _w((lambda *s: torch.randn(*s, dtype=torch.bfloat16) * 3.2)(D, D) * (D ** -0.5)),
            "w2": _w((lambda *s: torch.randn(*s, dtype=torch.bfloat16) * 3.2)(D, D) * (D ** -0.5)),
            "w3": _w((lambda *s: torch.randn(*s, dtype=torch.bfloat16) * 3.2)(D, F) * (D ** -0.5)),
            "w4": _w((lambda *s: torch.randn(*s, dtype=torch.bfloat16) * 3.2)(F, D) * (F ** -0.5)),
        }
    _B_in = _B_setup()

    return {
        "A_x": _A_in["x"],
        "A_wqkv": _A_in["wqkv"],
        "A_wo": _A_in["wo"],
        "B_x": _B_in["x"],
        "B_w1": _B_in["w1"],
        "B_w2": _B_in["w2"],
        "B_w3": _B_in["w3"],
        "B_w4": _B_in["w4"],
    }

def reference(A_x, A_wqkv, A_wo, B_x, B_w1, B_w2, B_w3, B_w4):
    def _A_ref(x, wqkv, wo):
        B, S, D = x.shape
        H = 32; Dh = D // H
        qkv = (lambda x, *d: x.permute(*d))((x @ wqkv).reshape(B, S, 3, H, Dh), 2, 0, 3, 1, 4)
        q, k, v = qkv[0], qkv[1], qkv[2]
        scale = Dh ** -0.5
        scores = torch.einsum("bhqd,bhkd->bhqk", q, k) * scale
        p = _softmax(scores, axis=-1)
        o = torch.einsum("bhqk,bhkd->bhqd", p, v)
        return (lambda x, *d: x.permute(*d))(o, 0, 2, 1, 3).reshape(B, S, D) @ wo

    def _B_ref(x, w1, w2, w3, w4):
        B, S, D = x.shape
        h = _gelu(_layernorm(x, axis=-1) @ w1)
        c = D // 4
        g0 = _pad(h[:, :, 0*c:1*c], ((0, 0), (1, 0), (0, 0)))[:, :S, :]
        g1 = _pad(h[:, :, 1*c:2*c], ((0, 0), (0, 1), (0, 0)))[:, 1:, :]
        g2 = _pad(h[:, :, 2*c:3*c], ((0, 0), (2, 0), (0, 0)))[:, :S, :]
        g3 = _pad(h[:, :, 3*c:   ], ((0, 0), (0, 2), (0, 0)))[:, 2:, :]
        hs = torch.concatenate([g0, g1, g2, g3], axis=-1) @ w2
        x1 = x + hs
        h2 = _gelu(_layernorm(x1, axis=-1) @ w3) @ w4
        return x1 + h2

    x = A_x
    xn = _layernorm(x, axis=-1)
    s = _A_ref(xn, A_wqkv, A_wo)
    g = _silu(_B_ref(xn, B_w1, B_w2, B_w3, B_w4))
    return x + s * g
```

```cpp
#include <hip/hip_runtime.h>
#include <hip/hip_bf16.h>
typedef __hip_bfloat16 bf16;
constexpr int S=2048, D=576, H=32, DH=18, F=4608;
__device__ inline float b2f(bf16 x){ return __bfloat162float(x); }
__device__ inline bf16 f2b(float x){ return __float2bfloat16(x); }
__device__ inline float gelu(float x){ float u=0.7978845608028654f*(x+0.044715f*x*x*x); return 0.5f*x*(1.f+tanhf(u)); }

__global__ void k_ln(const bf16* x, bf16* y){
  int r=blockIdx.x; float v[9]; float s=0;
  for(int i=0;i<9;i++){ v[i]=b2f(x[r*D+threadIdx.x+64*i]); s+=v[i]; }
  for(int o=32;o;o>>=1) s+=__shfl_xor(s,o);
  float m=s/D; float q=0; for(int i=0;i<9;i++){ float d=v[i]-m; q+=d*d; }
  for(int o=32;o;o>>=1) q+=__shfl_xor(q,o);
  float rs=rsqrtf(q/D+1e-5f);
  for(int i=0;i<9;i++) y[r*D+threadIdx.x+64*i]=f2b((v[i]-m)*rs);
}
template<int MODE>
__global__ void k_gemm(const bf16* A, const bf16* B, bf16* C, int M, int N, int K, const bf16* R){
  __shared__ float As[16][17], Bs[16][17];
  int tx=threadIdx.x, ty=threadIdx.y; int row=blockIdx.y*16+ty, col=blockIdx.x*16+tx; float acc=0;
  for(int k0=0;k0<K;k0+=16){
    if (MODE==2) {
      int k=k0+tx; int g=k/144; int d = g==0?-1: g==1?1: g==2?-2:2; int rr=row+d;
      As[ty][tx] = (rr>=0 && rr<M)? b2f(A[rr*K+k]) : 0.f;
    } else As[ty][tx]=b2f(A[row*K+k0+tx]);
    Bs[ty][tx]=b2f(B[(k0+ty)*N+col]);
    __syncthreads();
    for(int k=0;k<16;k++) acc+=As[ty][k]*Bs[k][tx];
    __syncthreads();
  }
  if (MODE==1) acc=gelu(acc);
  if (MODE==2 || MODE==3) acc+=b2f(R[row*N+col]);
  C[row*N+col]=f2b(acc);
}
__global__ void k_attn(const bf16* qkv, bf16* o){
  int h=blockIdx.y; int q=blockIdx.x*64+threadIdx.x;
  float qv[DH]; for(int d=0;d<DH;d++) qv[d]=b2f(qkv[q*3*D+h*DH+d])*0.23570226039551584f;
  float m=-1e30f,l=0,acc[DH]; for(int d=0;d<DH;d++) acc[d]=0;
  for(int j=0;j<S;j++){
    float s=0; for(int d=0;d<DH;d++) s+=qv[d]*b2f(qkv[j*3*D+D+h*DH+d]);
    float mn=fmaxf(m,s); float a=__expf(m-mn), p=__expf(s-mn); l=l*a+p;
    for(int d=0;d<DH;d++) acc[d]=acc[d]*a+p*b2f(qkv[j*3*D+2*D+h*DH+d]);
    m=mn;
  }
  for(int d=0;d<DH;d++) o[q*D+h*DH+d]=f2b(acc[d]/l);
}
__global__ void k_final(const bf16* x, const bf16* s, const bf16* y, bf16* out){
  int i=blockIdx.x*256+threadIdx.x; float yy=b2f(y[i]); float g=yy/(1.f+__expf(-yy));
  out[i]=f2b(b2f(x[i])+b2f(s[i])*g);
}
extern "C" void kernel_launch(void* const* d_in, const int* in_sizes, int n_in, void* d_out, int out_size, void* d_ws, size_t ws_size, hipStream_t st){
  const bf16* x=(const bf16*)d_in[0]; const bf16* wqkv=(const bf16*)d_in[1]; const bf16* wo=(const bf16*)d_in[2];
  const bf16* w1=(const bf16*)d_in[4]; const bf16* w2=(const bf16*)d_in[5]; const bf16* w3=(const bf16*)d_in[6]; const bf16* w4=(const bf16*)d_in[7];
  bf16* ws=(bf16*)d_ws;
  bf16* xn=ws; bf16* xn2=xn+S*D; bf16* qkv=xn2+S*D; bf16* o=qkv+S*3*D; bf16* s=o+S*D; bf16* h=s+S*D; bf16* x1=h+S*D; bf16* x1n=x1+S*D; bf16* h2=x1n+S*D; bf16* y=h2+S*F;
  k_ln<<<S,64,0,st>>>(x,xn); k_ln<<<S,64,0,st>>>(xn,xn2);
  dim3 b(16,16);
  k_gemm<0><<<dim3(3*D/16,S/16),b,0,st>>>(xn,wqkv,qkv,S,3*D,D,nullptr);
  k_attn<<<dim3(S/64,H),64,0,st>>>(qkv,o);
  k_gemm<0><<<dim3(D/16,S/16),b,0,st>>>(o,wo,s,S,D,D,nullptr);
  k_gemm<1><<<dim3(D/16,S/16),b,0,st>>>(xn2,w1,h,S,D,D,nullptr);
  k_gemm<2><<<dim3(D/16,S/16),b,0,st>>>(h,w2,x1,S,D,D,xn);
  k_ln<<<S,64,0,st>>>(x1,x1n);
  k_gemm<1><<<dim3(F/16,S/16),b,0,st>>>(x1n,w3,h2,S,F,D,nullptr);
  k_gemm<3><<<dim3(D/16,S/16),b,0,st>>>(h2,w4,y,S,D,F,x1);
  k_final<<<S*D/256,256,0,st>>>(x,s,y,(bf16*)d_out);
}
```

```cpp
#include <hip/hip_runtime.h>
#include <hip/hip_bf16.h>
#include <stdint.h>
#include <utility>
#pragma clang diagnostic ignored "-Winline-asm"

#define DEV __device__ __forceinline__
typedef short bf16x8 __attribute__((ext_vector_type(8)));
typedef short s16x4 __attribute__((ext_vector_type(4)));
typedef float f32x16 __attribute__((ext_vector_type(16)));
typedef float f32x4 __attribute__((ext_vector_type(4)));
typedef float f32x2 __attribute__((ext_vector_type(2)));
typedef unsigned u32x4 __attribute__((ext_vector_type(4)));
typedef unsigned u32x2 __attribute__((ext_vector_type(2)));
typedef __bf16 bf16x2_t __attribute__((ext_vector_type(2)));
typedef short v4i16_t __attribute__((ext_vector_type(4)));
typedef __attribute__((address_space(3))) const char* lds_cptr;
typedef __attribute__((address_space(3))) char* lds_ptr;
#define LDSP(T) __attribute__((address_space(3))) T*

constexpr int S = 2048, D = 576, NH = 32, FF = 4608;
constexpr float QSCALE = 0.23570226039551584f * 1.4426950408889634f;

constexpr size_t WS_QF = 0;
constexpr size_t WS_QX = WS_QF + 32 * 64 * 1024;
constexpr size_t WS_KF = WS_QX + 32 * 2048 * 4;
constexpr size_t WS_KX = WS_KF + 32 * 64 * 1024;
constexpr size_t WS_KN = WS_KX + 32 * 2048 * 4;
constexpr size_t WS_VF = WS_KN + 32 * 2048 * 4;
constexpr size_t WS_HS = WS_VF + 32 * 128 * 576;
constexpr size_t WS_W2F = WS_HS + 64 * 36 * 1024;
constexpr size_t WS_WOF = WS_W2F + 18 * 36 * 1024;
constexpr size_t WS_W3F = WS_WOF + 18 * 36 * 1024;
constexpr size_t WS_W4F = WS_W3F + 144 * 36 * 1024;
constexpr size_t WS_CS3 = WS_W4F + 144 * 36 * 1024;
constexpr size_t WS_ST0 = WS_CS3 + 6 * 4608 * 4;
constexpr size_t WS_X1F = WS_ST0 + 2048 * 8;
constexpr size_t WS_ST1 = WS_X1F + 64 * 36 * 1024;
constexpr size_t WS_PART = WS_ST1 + 64 * 18 * 32 * 8;
constexpr size_t WS_OF = WS_PART + (size_t)8 * 64 * 18 * 2048;
constexpr size_t WS_END = WS_OF + 64 * 36 * 1024;

DEV f32x16 mfma(bf16x8 a, bf16x8 b, f32x16 c) { return __builtin_amdgcn_mfma_f32_32x32x16_bf16(a, b, c, 0, 0, 0); }
DEV unsigned cvtpk(float lo, float hi) { const bf16x2_t v = __builtin_convertvector(f32x2{lo, hi}, bf16x2_t); return __builtin_bit_cast(unsigned, v); }
DEV float dot2(unsigned a, unsigned b, float acc) { return __builtin_amdgcn_fdot2_f32_bf16(__builtin_bit_cast(bf16x2_t, a), __builtin_bit_cast(bf16x2_t, b), acc, false); }
DEV float bflo(unsigned u) { return __uint_as_float(u << 16); }
DEV float bfhi(unsigned u) { return __uint_as_float(u & 0xffff0000u); }
DEV float bfround(float x) { return bflo(cvtpk(x, x)); }
DEV float xhalf_sum(float x) {
  auto r = __builtin_amdgcn_permlane32_swap(__float_as_uint(x), __float_as_uint(x), false, false);
  return __uint_as_float(r[0]) + __uint_as_float(r[1]);
}
DEV float xhalf_max(float x) {
  auto r = __builtin_amdgcn_permlane32_swap(__float_as_uint(x), __float_as_uint(x), false, false);
  return fmaxf(__uint_as_float(r[0]), __uint_as_float(r[1]));
}
DEV float lo_bcast(float x) {
  auto r = __builtin_amdgcn_permlane32_swap(__float_as_uint(x), __float_as_uint(x), false, false);
  return __uint_as_float(r[0]);
}
DEV bf16x8 as_bf(u32x4 v) { return __builtin_bit_cast(bf16x8, v); }
DEV u32x4 as_u(bf16x8 v) { return __builtin_bit_cast(u32x4, v); }
DEV void dma16(unsigned voff, const void* sbase, unsigned ldsaddr) {
  asm volatile("s_mov_b32 m0, %2\n\ts_nop 0\n\tglobal_load_lds_dwordx4 %0, %1" ::"v"(voff), "s"(sbase), "s"(ldsaddr) : "memory", "m0");
}
#define WAIT_VM(n) asm volatile("s_waitcnt vmcnt(" #n ")" ::: "memory")
#define BARRIER() asm volatile("s_waitcnt lgkmcnt(0)\n\ts_barrier" ::: "memory")
DEV float gelu_f(float x) {
  constexpr float C0 = -2.f * 0.7978845608028654f * 1.4426950408889634f, C1 = C0 * 0.044715f;
  float z = x * __builtin_fmaf(x * x, C1, C0);
  return x * __builtin_amdgcn_rcpf(1.f + __builtin_amdgcn_exp2f(z));
}
DEV bf16x8 lds_b128(lds_cptr p) { return *(const LDSP(bf16x8))p; }
DEV s16x4 lds_tr(lds_cptr p) { return __builtin_bit_cast(s16x4, __builtin_amdgcn_ds_read_tr16_b64_v4i16((LDSP(v4i16_t))p)); }

template <int... I, class F> DEV void static_for_impl(std::integer_sequence<int, I...>, F&& f) { (f(std::integral_constant<int, I>{}), ...); }
template <int N, class F> DEV void static_for(F&& f) { static_for_impl(std::make_integer_sequence<int, N>{}, f); }

constexpr int dw_feature(int di, int hh) { return 32 * (di >> 3) + 8 * ((di & 7) >> 1) + 4 * hh + 2 * (di & 1); }
constexpr int head_dw(int hl, int hh, int p) {
  int cnt = 0;
  for (int di = 0; di < 40; di++) { int n = dw_feature(di, hh); if (n >= 18 * hl && n < 18 * hl + 18 && n < 144) { if (cnt == p) return di; cnt++; } }
  return -1;
}

constexpr int P1_CH = 36864;
constexpr int P1_LDS = 4 * P1_CH + 4096;

template <bool VSEC>
DEV void p1_main(const char* lds, unsigned ldsb, const char* xsrc, const char* wsrc, size_t wstep, const unsigned (&xoff)[4], const unsigned (&woff)[5],
                 int w, int lane, f32x16 (&acc)[5], float& sum, float& sq, float (&cs)[5]) {
  const int c = lane & 31, hh = lane >> 5;
  unsigned boff[4];
#pragma unroll
  for (int ks = 0; ks < 4; ks++) boff[ks] = (32 * w + c) * 128 + (((2 * ks + hh) ^ ((c >> 1) & 7)) << 4);
  const unsigned aoff = 16384 + (8 * hh + ((lane & 15) >> 2)) * 320 + (16 * ((lane >> 4) & 1) + 4 * (lane & 3)) * 2;
  auto issue = [&](int kc) {
    const unsigned slot = ldsb + (kc & 3) * P1_CH;
#pragma unroll
    for (int j = 0; j < 4; j++) dma16(xoff[j], xsrc + kc * 128, slot + (4 * w + j) * 1024);
#pragma unroll
    for (int j = 0; j < 5; j++) dma16(woff[j], wsrc + (size_t)(4 * kc + w) * wstep, slot + 16384 + w * 5120 + j * 1024);
  };
  issue(0); issue(1); issue(2);
#pragma unroll 1
  for (int kc = 0; kc < 9; kc++) {
    if (kc <= 6) WAIT_VM(18); else if (kc == 7) WAIT_VM(9); else WAIT_VM(0);
    BARRIER();
    if (kc + 3 < 9) issue(kc + 3);
    lds_cptr sl = (lds_cptr)lds + (kc & 3) * P1_CH;
#pragma unroll
    for (int ks = 0; ks < 4; ks++) {
      bf16x8 bf = lds_b128(sl + boff[ks]);
      bf16x8 af[5];
#pragma unroll
      for (int b = 0; b < 5; b++) {
        s16x4 lo = lds_tr(sl + aoff + ks * 5120 + b * 64), hi = lds_tr(sl + aoff + ks * 5120 + 1280 + b * 64);
        af[b] = bf16x8{lo[0], lo[1], lo[2], lo[3], hi[0], hi[1], hi[2], hi[3]};
      }
#pragma unroll
      for (int b = 0; b < 5; b++) acc[b] = VSEC ? mfma(bf, af[b], acc[b]) : mfma(af[b], bf, acc[b]);
      u32x4 bu = as_u(bf);
#pragma unroll
      for (int i = 0; i < 4; i++) { sum = dot2(bu[i], 0x3f803f80u, sum); sq = dot2(bu[i], bu[i], sq); }
      if (ks == w) {
#pragma unroll
        for (int b = 0; b < 5; b++) { u32x4 au = as_u(af[b]);
#pragma unroll
          for (int i = 0; i < 4; i++) cs[b] = dot2(au[i], 0x3f803f80u, cs[b]); }
      }
    }
  }
}

template <class ROWF>
DEV void prep_task(const unsigned short* src, int stride, int col0, int s0, char* dst0, size_t dst_cb_stride, ROWF rowf, int lane, float* csout) {
  const int cp = lane & 31, hh = lane >> 5;
  unsigned v[6][8];
#pragma unroll
  for (int u = 0; u < 6; u++)
#pragma unroll
    for (int j = 0; j < 8; j++) v[u][j] = *(const unsigned*)(src + (size_t)rowf(s0 + u, hh, j) * stride + col0 + 2 * cp);
  float lo = 0.f, hi = 0.f;
#pragma unroll
  for (int u = 0; u < 6; u++) {
    u32x4 a, b;
#pragma unroll
    for (int i = 0; i < 4; i++) {
      a[i] = __builtin_amdgcn_perm(v[u][2 * i + 1], v[u][2 * i], 0x05040100u);
      b[i] = __builtin_amdgcn_perm(v[u][2 * i + 1], v[u][2 * i], 0x07060302u);
    }
    char* d = dst0 + (size_t)(cp >> 4) * dst_cb_stride + (size_t)(s0 + u) * 1024 + (hh * 32 + ((2 * cp) & 31)) * 16;
    *(u32x4*)d = a; *(u32x4*)(d + 16) = b;
    if (csout) {
#pragma unroll
      for (int j = 0; j < 8; j++) { lo += bflo(v[u][j]); hi += bfhi(v[u][j]); }
    }
  }
  if (csout) { lo = xhalf_sum(lo); hi = xhalf_sum(hi); if (hh == 0) *(f32x2*)(csout + col0 + 2 * cp) = f32x2{lo, hi}; }
}
DEV int row_nat(int s, int hh, int j) { return 16 * s + 8 * (j >> 2) + 4 * hh + (j & 3); }
DEV int row_wo(int s, int hh, int j) { return s < 32 ? 18 * s + 8 * (j >> 2) + 4 * hh + (j & 3) : 18 * (8 * (s - 32) + 4 * hh + (j >> 1)) + 16 + (j & 1); }

DEV void prep_all(int task, int lane, const unsigned short* wo, const unsigned short* w2, const unsigned short* w3, const unsigned short* w4, char* ws) {
  if (task < 432) {
    const int cg = task / 6, kg = task % 6;
    prep_task(w3, FF, cg * 64, kg * 6, ws + WS_W3F + (size_t)(2 * cg) * 36864, 36864, [](int s, int hh, int j) { return row_nat(s, hh, j); }, lane, (float*)(ws + WS_CS3) + kg * FF);
  } else if (task < 864) {
    const int t = task - 432, cg = t % 9, kg = t / 9, sl = kg / 6;
    prep_task(w4 + (size_t)sl * 576 * D, D, cg * 64, (kg % 6) * 6, ws + WS_W4F + ((size_t)sl * 18 + 2 * cg) * 36864, 36864, [](int s, int hh, int j) { return row_nat(s, hh, j); }, lane, nullptr);
  } else if (task < 918) {
    const int t = task - 864, cg = t % 9, kg = t / 9;
    prep_task(w2, D, cg * 64, kg * 6, ws + WS_W2F + (size_t)(2 * cg) * 36864, 36864, [](int s, int hh, int j) { return row_nat(s, hh, j); }, lane, nullptr);
  } else if (task < 972) {
    const int t = task - 918, cg = t % 9, kg = t / 9;
    prep_task(wo, D, cg * 64, kg * 6, ws + WS_WOF + (size_t)(2 * cg) * 36864, 36864, [](int s, int hh, int j) { return row_wo(s, hh, j); }, lane, nullptr);
  }
}

__global__ __launch_bounds__(256, 1) void k_p1(const unsigned short* __restrict__ x, const unsigned short* __restrict__ wqkv, const unsigned short* __restrict__ wo,
                                               const unsigned short* __restrict__ w1, const unsigned short* __restrict__ w2, const unsigned short* __restrict__ w3,
                                               const unsigned short* __restrict__ w4, char* __restrict__ ws) {
  extern __shared__ __attribute__((aligned(16))) char lds[];
  const int tid = threadIdx.x, lane = tid & 63, w = __builtin_amdgcn_readfirstlane(tid >> 6);
  const int c = lane & 31, hh = lane >> 5;
  const int b = blockIdx.x, xcd = b & 7, i = b >> 3;
  const int tt = (xcd & 1) * 8 + (i & 7), ct = (xcd >> 1) * 4 + (i >> 3), sec = ct >> 2, sub = ct & 3;
  const int wstride = sec < 3 ? 3 * D : D;
  const char* wsrc = sec < 3 ? (const char*)(wqkv + sec * D + sub * 144) : (const char*)(w1 + sub * 144);
  const char* xsrc = (const char*)(x + (size_t)tt * 128 * D);
  unsigned xoff[4], woff[5];
#pragma unroll
  for (int j = 0; j < 4; j++) { const int row = 32 * w + 8 * j + (lane >> 3); xoff[j] = row * (D * 2) + (((lane & 7) ^ ((row >> 1) & 7)) << 4); }
#pragma unroll
  for (int j = 0; j < 5; j++) { const int q = 64 * j + lane, row = q / 20; int ch = q % 20; if (ch >= 18) ch -= 2; woff[j] = row * wstride * 2 + ch * 16; }
  f32x16 acc[5];
#pragma unroll
  for (int k = 0; k < 5; k++) acc[k] = f32x16{};
  float sum = 0.f, sq = 0.f, cs[5] = {0.f, 0.f, 0.f, 0.f, 0.f};
  const unsigned ldsb = (unsigned)(uintptr_t)lds;
  if (sec == 2) p1_main<true>(lds, ldsb, xsrc, wsrc, (size_t)16 * wstride * 2, xoff, woff, w, lane, acc, sum, sq, cs);
  else p1_main<false>(lds, ldsb, xsrc, wsrc, (size_t)16 * wstride * 2, xoff, woff, w, lane, acc, sum, sq, cs);

  sum = xhalf_sum(sum); sq = xhalf_sum(sq);
  const float mu = sum * (1.f / D);
  const float var = fmaxf(sq * (1.f / D) - mu * mu, 0.f);
  const float mu_b = bfround(mu);
  const float sd_b = bfround(__builtin_sqrtf(bfround(bfround(var) + 1e-5f)));
  float mean, rstd;
  if (sec < 3) { mean = mu_b; rstd = 1.f / sd_b; } else { mean = mu; rstd = 1.f / __builtin_sqrtf(var + 1e-5f * sd_b * sd_b); }
  const int tb = tt * 4 + w, tok = tb * 32 + c;
  if (sec == 0 && sub == 0 && hh == 0) *(f32x2*)(ws + WS_ST0 + (size_t)tok * 8) = f32x2{mu_b, 1.f / sd_b};
  float* csl = (float*)(lds + 4 * P1_CH);
  float* stl = csl + 640 + w * 64;
  BARRIER();
#pragma unroll
  for (int k = 0; k < 5; k++) { const float t = xhalf_sum(cs[k]); if (hh == 0) csl[w * 160 + 32 * k + c] = t; }
  if (hh == 0) { stl[c] = mean; stl[32 + c] = rstd; }
  BARRIER();
  if (tid < 160) csl[tid] = csl[tid] + csl[160 + tid] + csl[320 + tid] + csl[480 + tid];
  BARRIER();

  if (sec == 2) {
    float mn[16], rs[16];
#pragma unroll
    for (int g = 0; g < 4; g++) {
      const f32x4 m4 = *(const f32x4*)(stl + 8 * g + 4 * hh), r4 = *(const f32x4*)(stl + 32 + 8 * g + 4 * hh);
#pragma unroll
      for (int e = 0; e < 4; e++) { mn[4 * g + e] = m4[e]; rs[4 * g + e] = r4[e]; }
    }
#pragma unroll
    for (int k = 0; k < 5; k++) {
      const int n = 32 * k + c;
      const float csn = csl[n];
      unsigned p[8];
#pragma unroll
      for (int e = 0; e < 8; e++) p[e] = cvtpk(rs[2 * e] * (acc[k][2 * e] - mn[2 * e] * csn), rs[2 * e + 1] * (acc[k][2 * e + 1] - mn[2 * e + 1] * csn));
      if (n < 144) {
        const int hl = n / 18, d = n % 18, head = sub * 8 + hl;
        char* dst = ws + WS_VF + ((size_t)(head * 128 + 2 * tb) * 2 + hh) * 288 + d * 16;
        *(u32x4*)dst = u32x4{p[0], p[1], p[2], p[3]};
        *(u32x4*)(dst + 576) = u32x4{p[4], p[5], p[6], p[7]};
      }
    }
  } else {
    unsigned P[40];
    const float sc = sec == 0 ? rstd * QSCALE : rstd;
#pragma unroll
    for (int k = 0; k < 5; k++)
#pragma unroll
      for (int g = 0; g < (k == 4 ? 2 : 4); g++) {
        const f32x4 c4 = *(const f32x4*)(csl + 32 * k + 8 * g + 4 * hh);
        float y[4];
#pragma unroll
        for (int e = 0; e < 4; e++) { y[e] = sc * (acc[k][4 * g + e] - mean * c4[e]); if (sec == 3) y[e] = gelu_f(y[e]); }
        P[8 * k + 2 * g] = cvtpk(y[0], y[1]); P[8 * k + 2 * g + 1] = cvtpk(y[2], y[3]);
      }
    if (sec == 3) {
      const int dl = sub == 0 ? -1 : sub == 1 ? 1 : sub == 2 ? -2 : 2;
      const int t = tok - dl, tw = t & (S - 1);
      const bool wrap = t != tw;
      char* dst = ws + WS_HS + ((size_t)(tw >> 5) * 36 + 9 * sub) * 1024 + ((tw & 31) + 32 * hh) * 16;
#pragma unroll
      for (int ks = 0; ks < 9; ks++) {
        u32x4 v = u32x4{P[4 * ks], P[4 * ks + 1], P[4 * ks + 2], P[4 * ks + 3]};
        if (wrap) v = u32x4{0, 0, 0, 0};
        *(u32x4*)(dst + ks * 1024) = v;
      }
    } else {
      char* fdst = ws + (sec == 0 ? WS_QF : WS_KF);
      unsigned* xdst = (unsigned*)(ws + (sec == 0 ? WS_QX : WS_KX));
      static_for<8>([&](auto HL) {
        constexpr int hl = HL;
        unsigned v[5];
        static_for<5>([&](auto PP) {
          constexpr int p = PP, d0 = head_dw(hl, 0, p), d1 = head_dw(hl, 1, p);
          const unsigned a0 = d0 >= 0 ? P[d0 >= 0 ? d0 : 0] : 0u, a1 = d1 >= 0 ? P[d1 >= 0 ? d1 : 0] : 0u;
          v[p] = hh ? a1 : a0;
        });
        const int head = sub * 8 + hl;
        *(u32x4*)(fdst + ((size_t)head * 64 + tb) * 1024 + lane * 16) = u32x4{v[0], v[1], v[2], v[3]};
        constexpr int xh = head_dw(hl, 0, 4) >= 0 ? 0 : 1;
        if (hh == xh) xdst[head * S + tok] = v[4];
        if (sec == 1) {
          float nr = 0.f;
#pragma unroll
          for (int p = 0; p < 5; p++) nr = dot2(v[p], v[p], nr);
          nr = xhalf_sum(nr);
          if (hh == 0) ((float*)(ws + WS_KN))[head * S + tok] = nr;
        }
      });
    }
  }
  prep_all(b * 4 + w, lane, wo, w2, w3, w4, ws);
}

__global__ __launch_bounds__(256) void k_p2(const unsigned short* __restrict__ x, char* __restrict__ ws) {
  const int tid = threadIdx.x, lane = tid & 63, w = tid >> 6, c = lane & 31, hh = lane >> 5;
  const int u = blockIdx.x * 4 + w, db = u % 18, tb = u / 18;
  const bf16x8* A = (const bf16x8*)(ws + WS_W2F + (size_t)db * 36864) + lane;
  const bf16x8* B = (const bf16x8*)(ws + WS_HS + (size_t)tb * 36864) + lane;
  f32x16 acc = {};
#pragma unroll 6
  for (int s = 0; s < 36; s++) acc = mfma(A[s * 64], B[s * 64], acc);
  const int tok = tb * 32 + c;
  const f32x2 st = *(const f32x2*)(ws + WS_ST0 + (size_t)tok * 8);
  unsigned P[8];
#pragma unroll
  for (int g = 0; g < 4; g++) {
    const u32x2 xv = *(const u32x2*)(x + (size_t)tok * D + 32 * db + 8 * g + 4 * hh);
    const float a0 = (bflo(xv[0]) - st[0]) * st[1] + acc[4 * g], a1 = (bfhi(xv[0]) - st[0]) * st[1] + acc[4 * g + 1];
    const float a2 = (bflo(xv[1]) - st[0]) * st[1] + acc[4 * g + 2], a3 = (bfhi(xv[1]) - st[0]) * st[1] + acc[4 * g + 3];
    P[2 * g] = cvtpk(a0, a1); P[2 * g + 1] = cvtpk(a2, a3);
  }
  char* dst = ws + WS_X1F + ((size_t)tb * 36 + 2 * db) * 1024 + lane * 16;
  *(u32x4*)dst = u32x4{P[0], P[1], P[2], P[3]};
  *(u32x4*)(dst + 1024) = u32x4{P[4], P[5], P[6], P[7]};
  float sm = 0.f, sq = 0.f;
#pragma unroll
  for (int e = 0; e < 8; e++) { sm = dot2(P[e], 0x3f803f80u, sm); sq = dot2(P[e], P[e], sq); }
  sm = xhalf_sum(sm); sq = xhalf_sum(sq);
  if (hh == 0) *(f32x2*)(ws + WS_ST1 + (((size_t)tb * 18 + db) * 32 + c) * 8) = f32x2{sm, sq};
}

constexpr int AT_K = 0, AT_KX = 65536, AT_V = 73728, AT_C = 147456, AT_RED = AT_C + 640, AT_LDS = AT_RED + 64;

template <bool BIAS>
DEV void attn_tile(lds_cptr kp, lds_cptr kxp, lds_cptr vp, int vstride, const bf16x8 (&qm)[4], const bf16x8 (&qe)[4], const float (&nb)[4], f32x16 (&o)[4], int kt0, int kt1) {
#pragma unroll 1
  for (int kt = kt0; kt < kt1; kt++) {
    const bf16x8 km = lds_b128(kp + kt * 1024);
    const unsigned kx = *(const LDSP(unsigned))(kxp + kt * 128);
    const bf16x8 ke = as_bf(u32x4{kx, 0u, 0u, 0u});
    lds_cptr v = vp + kt * vstride;
    const bf16x8 v0 = lds_b128(v), v1 = lds_b128(v + 576);
#pragma unroll
    for (int q = 0; q < 4; q++) {
      f32x16 s;
      if (BIAS) {
#pragma unroll
        for (int r = 0; r < 16; r++) s[r] = nb[q];
      } else s = f32x16{};
      s = mfma(km, qm[q], s);
      s = mfma(ke, qe[q], s);
      unsigned p[8];
#pragma unroll
      for (int e = 0; e < 8; e++) p[e] = cvtpk(__builtin_amdgcn_exp2f(s[2 * e]), __builtin_amdgcn_exp2f(s[2 * e + 1]));
      o[q] = mfma(v0, as_bf(u32x4{p[0], p[1], p[2], p[3]}), o[q]);
      o[q] = mfma(v1, as_bf(u32x4{p[4], p[5], p[6], p[7]}), o[q]);
    }
  }
}

DEV void attn_block(char* lds, char* ws, int head, int qr) {
  const int tid = threadIdx.x, lane = tid & 63, w = __builtin_amdgcn_readfirstlane(tid >> 6), c = lane & 31, hh = lane >> 5;
  const unsigned ldsb = (unsigned)(uintptr_t)lds;
  const char* kf = ws + WS_KF + (size_t)head * 65536;
  const char* kx = ws + WS_KX + (size_t)head * 8192;
  const char* vf = ws + WS_VF + (size_t)head * 73728;
#pragma unroll
  for (int ch = 0; ch < 4; ch++) {
#pragma unroll
    for (int j = 0; j < 4; j++) dma16(lane * 16, kf + ch * 16384 + (4 * w + j) * 1024, ldsb + AT_K + ch * 16384 + (4 * w + j) * 1024);
#pragma unroll
    for (int j = 0; j < 5; j++) {
      const int p = 5 * w + j;
      if (p < 18) dma16(lane * 16, vf + ch * 18432 + p * 1024, ldsb + AT_V + ch * 18432 + p * 1024);
      else dma16(lane * 16, kx + ch * 2048 + (p - 18) * 1024, ldsb + AT_KX + ch * 2048 + (p - 18) * 1024);
    }
  }
  if (tid < 8) { const unsigned v = (tid & 4) ? 0u : 0x3f803f80u; *(unsigned*)(lds + AT_C + tid * 4) = v; *(unsigned*)(lds + AT_C + 576 + tid * 4) = v; }
  const int xh = (head >> 1) & 1;
  bf16x8 qm[4], qe[4];
  float qn = 0.f;
#pragma unroll
  for (int q = 0; q < 4; q++) {
    const int tb = qr * 16 + w * 4 + q;
    const u32x4 m = *(const u32x4*)(ws + WS_QF + ((size_t)head * 64 + tb) * 1024 + lane * 16);
    unsigned e = ((const unsigned*)(ws + WS_QX))[head * S + tb * 32 + c];
    if (hh != xh) e = 0u;
    qm[q] = as_bf(m); qe[q] = as_bf(u32x4{e, 0u, 0u, 0u});
    float n2 = dot2(e, e, 0.f);
#pragma unroll
    for (int i = 0; i < 4; i++) n2 = dot2(m[i], m[i], n2);
    qn = fmaxf(qn, xhalf_sum(n2));
  }
  {
    const f32x4* kn = (const f32x4*)(ws + WS_KN + (size_t)head * 8192);
    const f32x4 a = kn[tid], b2 = kn[256 + tid];
    float m = fmaxf(fmaxf(fmaxf(a[0], a[1]), fmaxf(a[2], a[3])), fmaxf(fmaxf(b2[0], b2[1]), fmaxf(b2[2], b2[3])));
#pragma unroll
    for (int o2 = 1; o2 < 64; o2 <<= 1) { m = fmaxf(m, __shfl_xor(m, o2)); qn = fmaxf(qn, __shfl_xor(qn, o2)); }
    if (lane == 0) ((float*)(lds + AT_RED))[w] = m;
  }
  lds_cptr kp = (lds_cptr)lds + AT_K + lane * 16;
  lds_cptr kxp = (lds_cptr)lds + AT_KX + c * 4;
  const int vstride = c < 18 ? 1152 : 0;
  lds_cptr vp = (lds_cptr)lds + (c < 18 ? AT_V + hh * 288 + c * 16 : c == 18 ? AT_C : AT_C + 16);
  f32x16 o[4];
  float nb[4] = {0.f, 0.f, 0.f, 0.f};
#pragma unroll
  for (int q = 0; q < 4; q++) o[q] = f32x16{};
  WAIT_VM(27); BARRIER();
  const f32x4 km4 = *(const f32x4*)(lds + AT_RED);
  const float kmax2 = fmaxf(fmaxf(km4[0], km4[1]), fmaxf(km4[2], km4[3]));
  const bool safe = qn * kmax2 * 1.02f <= 3600.f;
  if (safe) {
    attn_tile<false>(kp, kxp, vp, vstride, qm, qe, nb, o, 0, 16);
    WAIT_VM(18); BARRIER();
    attn_tile<false>(kp, kxp, vp, vstride, qm, qe, nb, o, 16, 32);
    WAIT_VM(9); BARRIER();
    attn_tile<false>(kp, kxp, vp, vstride, qm, qe, nb, o, 32, 48);
    WAIT_VM(0); BARRIER();
    attn_tile<false>(kp, kxp, vp, vstride, qm, qe, nb, o, 48, 64);
  } else { WAIT_VM(0); BARRIER(); BARRIER(); BARRIER(); }
  bool redo = !safe;
  if (safe) {
    float t = 0.f;
#pragma unroll
    for (int q = 0; q < 4; q++)
#pragma unroll
      for (int r = 0; r < 11; r++) t += fabsf(o[q][r]);
    redo = __any(!(t < 3.0e38f));
  }
  if (redo) {
    float mx[4] = {-3.0e38f, -3.0e38f, -3.0e38f, -3.0e38f};
#pragma unroll 1
    for (int kt = 0; kt < 64; kt++) {
      const bf16x8 km = lds_b128(kp + kt * 1024);
      const unsigned kxv = *(const LDSP(unsigned))(kxp + kt * 128);
      const bf16x8 ke = as_bf(u32x4{kxv, 0u, 0u, 0u});
#pragma unroll
      for (int q = 0; q < 4; q++) {
        f32x16 s = mfma(km, qm[q], f32x16{});
        s = mfma(ke, qe[q], s);
#pragma unroll
        for (int r = 0; r < 16; r++) mx[q] = fmaxf(mx[q], s[r]);
      }
    }
#pragma unroll
    for (int q = 0; q < 4; q++) { nb[q] = -xhalf_max(mx[q]); o[q] = f32x16{}; }
    attn_tile<true>(kp, kxp, vp, vstride, qm, qe, nb, o, 0, 64);
  }
#pragma unroll
  for (int q = 0; q < 4; q++) {
    const int tb = qr * 16 + w * 4 + q;
    const float inv = 1.f / lo_bcast(o[q][10]);
    unsigned p[5];
#pragma unroll
    for (int e = 0; e < 5; e++) p[e] = cvtpk(o[q][2 * e] * inv, o[q][2 * e + 1] * inv);
    char* dst = ws + WS_OF + (size_t)tb * 36864;
    *(u32x4*)(dst + head * 1024 + lane * 16) = u32x4{p[0], p[1], p[2], p[3]};
    if (hh == 0) *(unsigned*)(dst + (32 + (head >> 3)) * 1024 + (c + 32 * ((head >> 2) & 1)) * 16 + (head & 3) * 4) = p[4];
  }
}

constexpr int ML_ST = 36864, ML_CS = 4 * ML_ST, ML_LDS = ML_CS + 576 * 4;

DEV void mlp_block(char* lds, char* ws, int tt, int c8) {
  const int tid = threadIdx.x, lane = tid & 63, w = __builtin_amdgcn_readfirstlane(tid >> 6), c = lane & 31, hh = lane >> 5;
  const unsigned ldsb = (unsigned)(uintptr_t)lds;
  const char* w3 = ws + WS_W3F + (size_t)c8 * 18 * 36864;
  const char* w4 = ws + WS_W4F + (size_t)c8 * 18 * 36864;
  auto issue = [&](int u) {
    const char* src = (u < 18 ? w3 + (size_t)u * 36864 : w4 + (size_t)(u - 18) * 36864) + w * 9216;
    const unsigned dst = ldsb + (u & 3) * ML_ST + w * 9216;
#pragma unroll
    for (int j = 0; j < 9; j++) dma16(lane * 16, src + j * 1024, dst + j * 1024);
  };
  issue(0); issue(1); issue(2);
  const int tb = tt * 4 + w;
  bf16x8 X[36];
#pragma unroll
  for (int s = 0; s < 36; s++) X[s] = *(const bf16x8*)(ws + WS_X1F + ((size_t)tb * 36 + s) * 1024 + lane * 16);
  float sm = 0.f, sq = 0.f;
#pragma unroll
  for (int db = 0; db < 18; db++) { const f32x2 t = *(const f32x2*)(ws + WS_ST1 + (((size_t)tb * 18 + db) * 32 + c) * 8); sm += t[0]; sq += t[1]; }
  const float mean = sm * (1.f / D), rstd = 1.f / __builtin_sqrtf(fmaxf(sq * (1.f / D) - mean * mean, 0.f) + 1e-5f);
  float* csl = (float*)(lds + ML_CS);
  for (int f = tid; f < 576; f += 256) {
    const float* p = (const float*)(ws + WS_CS3) + c8 * 576 + f;
    csl[f] = ((p[0] + p[FF]) + (p[2 * FF] + p[3 * FF])) + (p[4 * FF] + p[5 * FF]);
  }
  bf16x8 H[36];
#pragma unroll
  for (int u = 0; u < 18; u++) {
    WAIT_VM(18); BARRIER();
    issue(u + 3);
    lds_cptr sl = (lds_cptr)lds + (u & 3) * ML_ST + lane * 16;
    f32x16 acc;
#pragma unroll
    for (int g = 0; g < 4; g++) {
      const f32x4 c4 = *(const f32x4*)(csl + 32 * u + 8 * g + 4 * hh);
#pragma unroll
      for (int e = 0; e < 4; e++) acc[4 * g + e] = -mean * c4[e];
    }
#pragma unroll
    for (int s = 0; s < 36; s++) acc = mfma(lds_b128(sl + s * 1024), X[s], acc);
    unsigned p[8];
#pragma unroll
    for (int e = 0; e < 8; e++) p[e] = cvtpk(gelu_f(acc[2 * e] * rstd), gelu_f(acc[2 * e + 1] * rstd));
    H[2 * u] = as_bf(u32x4{p[0], p[1], p[2], p[3]}); H[2 * u + 1] = as_bf(u32x4{p[4], p[5], p[6], p[7]});
  }
#pragma unroll 1
  for (int u = 18; u < 36; u++) {
    if (u < 34) WAIT_VM(18); else if (u == 34) WAIT_VM(9); else WAIT_VM(0);
    BARRIER();
    if (u + 3 < 36) issue(u + 3);
    lds_cptr sl = (lds_cptr)lds + (u & 3) * ML_ST + lane * 16;
    f32x16 acc = {};
#pragma unroll
    for (int s = 0; s < 36; s++) acc = mfma(lds_b128(sl + s * 1024), H[s], acc);
    char* dst = ws + WS_PART + (((size_t)c8 * 64 + tb) * 18 + (u - 18)) * 2048 + lane * 16;
    *(u32x4*)dst = u32x4{cvtpk(acc[0], acc[1]), cvtpk(acc[2], acc[3]), cvtpk(acc[4], acc[5]), cvtpk(acc[6], acc[7])};
    *(u32x4*)(dst + 1024) = u32x4{cvtpk(acc[8], acc[9]), cvtpk(acc[10], acc[11]), cvtpk(acc[12], acc[13]), cvtpk(acc[14], acc[15])};
  }
}

constexpr int MID_LDS = ML_LDS > AT_LDS ? ML_LDS : AT_LDS;
__global__ __launch_bounds__(256, 1) void k_mid(char* __restrict__ ws) {
  extern __shared__ __attribute__((aligned(16))) char lds[];
  const int b = blockIdx.x, xcd = b & 7, i = b >> 3;
  if (i < 16) attn_block(lds, ws, xcd * 4 + (i >> 2), i & 3);
  else mlp_block(lds, ws, i - 16, xcd);
}

__global__ __launch_bounds__(256) void k_fin(const unsigned short* __restrict__ x, const char* __restrict__ ws, unsigned short* __restrict__ out) {
  const int tid = threadIdx.x, lane = tid & 63, w = tid >> 6, c = lane & 31, hh = lane >> 5;
  const int u = blockIdx.x * 4 + w, db = u % 18, tb = u / 18;
  const bf16x8* A = (const bf16x8*)(ws + WS_WOF + (size_t)db * 36864) + lane;
  const bf16x8* B = (const bf16x8*)(ws + WS_OF + (size_t)tb * 36864) + lane;
  float y[16];
  {
    const u32x4 a = *(const u32x4*)(ws + WS_X1F + ((size_t)tb * 36 + 2 * db) * 1024 + lane * 16);
    const u32x4 b = *(const u32x4*)(ws + WS_X1F + ((size_t)tb * 36 + 2 * db + 1) * 1024 + lane * 16);
#pragma unroll
    for (int e = 0; e < 4; e++) { y[2 * e] = bflo(a[e]); y[2 * e + 1] = bfhi(a[e]); y[8 + 2 * e] = bflo(b[e]); y[8 + 2 * e + 1] = bfhi(b[e]); }
  }
#pragma unroll
  for (int p = 0; p < 8; p++) {
    const char* src = ws + WS_PART + (((size_t)p * 64 + tb) * 18 + db) * 2048 + lane * 16;
    const u32x4 a = *(const u32x4*)src, b = *(const u32x4*)(src + 1024);
#pragma unroll
    for (int e = 0; e < 4; e++) { y[2 * e] += bflo(a[e]); y[2 * e + 1] += bfhi(a[e]); y[8 + 2 * e] += bflo(b[e]); y[8 + 2 * e + 1] += bfhi(b[e]); }
  }
  f32x16 acc = {};
#pragma unroll 6
  for (int s = 0; s < 36; s++) acc = mfma(A[s * 64], B[s * 64], acc);
  const int tok = tb * 32 + c;
#pragma unroll
  for (int g = 0; g < 4; g++) {
    const size_t off = (size_t)tok * D + 32 * db + 8 * g + 4 * hh;
    const u32x2 xv = *(const u32x2*)(x + off);
    float r[4];
    const float xf[4] = {bflo(xv[0]), bfhi(xv[0]), bflo(xv[1]), bfhi(xv[1])};
#pragma unroll
    for (int e = 0; e < 4; e++) {
      const float yy = y[4 * g + e];
      const float gt = yy * __builtin_amdgcn_rcpf(1.f + __builtin_amdgcn_exp2f(-1.4426950408889634f * yy));
      r[e] = xf[e] + acc[4 * g + e] * gt;
    }
    *(u32x2*)(out + off) = u32x2{cvtpk(r[0], r[1]), cvtpk(r[2], r[3])};
  }
}

extern "C" void kernel_launch(void* const* d_in, const int* in_sizes, int n_in, void* d_out, int out_size, void* d_ws, size_t ws_size, hipStream_t stream) {
  const unsigned short* x = (const unsigned short*)d_in[0];
  const unsigned short* wqkv = (const unsigned short*)d_in[1];
  const unsigned short* wo = (const unsigned short*)d_in[2];
  const unsigned short* w1 = (const unsigned short*)d_in[4];
  const unsigned short* w2 = (const unsigned short*)d_in[5];
  const unsigned short* w3 = (const unsigned short*)d_in[6];
  const unsigned short* w4 = (const unsigned short*)d_in[7];
  char* ws = (char*)d_ws;
  (void)hipFuncSetAttribute((const void*)k_p1, hipFuncAttributeMaxDynamicSharedMemorySize, P1_LDS);
  (void)hipFuncSetAttribute((const void*)k_mid, hipFuncAttributeMaxDynamicSharedMemorySize, MID_LDS);
  k_p1<<<256, 256, P1_LDS, stream>>>(x, wqkv, wo, w1, w2, w3, w4, ws);
  k_p2<<<288, 256, 0, stream>>>(x, ws);
  k_mid<<<256, 256, MID_LDS, stream>>>(ws);
  k_fin<<<288, 256, 0, stream>>>(x, ws, (unsigned short*)d_out);
}
```

```cpp
#include <hip/hip_runtime.h>
#include <hip/hip_bf16.h>
#include <stdint.h>
#include <utility>
#pragma clang diagnostic ignored "-Winline-asm"

#define HLD "sc1"
#define HAUX 16
#define DEV __device__ __forceinline__
typedef short bf16x8 __attribute__((ext_vector_type(8)));
typedef short s16x4 __attribute__((ext_vector_type(4)));
typedef float f32x16 __attribute__((ext_vector_type(16)));
typedef float f32x4 __attribute__((ext_vector_type(4)));
typedef float f32x2 __attribute__((ext_vector_type(2)));
typedef unsigned u32x4 __attribute__((ext_vector_type(4)));
typedef unsigned u32x2 __attribute__((ext_vector_type(2)));
typedef __bf16 bf16x2_t __attribute__((ext_vector_type(2)));
typedef short v4i16_t __attribute__((ext_vector_type(4)));
typedef __attribute__((address_space(3))) const char* lds_cptr;
#define LDSP(T) __attribute__((address_space(3))) T*
typedef __amdgpu_buffer_rsrc_t srd_t;

constexpr int S = 2048, D = 576, NH = 32, FF = 4608;
constexpr float QSCALE = 0.23570226039551584f * 1.4426950408889634f;

constexpr unsigned WS_FLAG = 0;
constexpr unsigned WS_QF = 16384;
constexpr unsigned WS_QX = WS_QF + 32 * 64 * 1024;
constexpr unsigned WS_KF = WS_QX + 32 * 2048 * 4;
constexpr unsigned WS_KX = WS_KF + 32 * 64 * 1024;
constexpr unsigned WS_KN = WS_KX + 32 * 2048 * 4;
constexpr unsigned WS_VF = WS_KN + 32 * 2048 * 4;
constexpr unsigned WS_HS = WS_VF + 32 * 128 * 576;
constexpr unsigned WS_CS3 = WS_HS + 64 * 36 * 1024;
constexpr unsigned WS_RS1 = WS_CS3 + 4608 * 8;
constexpr unsigned WS_X1F = WS_RS1 + 2048 * 4;
constexpr unsigned WS_ST1 = WS_X1F + 64 * 36 * 1024;
constexpr unsigned WS_PART = WS_ST1 + 64 * 18 * 32 * 8;
constexpr unsigned WS_OF = WS_PART + 8 * 64 * 18 * 2048;
constexpr unsigned WS_END = WS_OF + 64 * 36 * 1024;
constexpr int FL_EPOCH = 0, FL_P1 = 384, FL_X1 = 704, FL_O = 896, FL_PART = 2048;

DEV f32x16 mfma(bf16x8 a, bf16x8 b, f32x16 c) { return __builtin_amdgcn_mfma_f32_32x32x16_bf16(a, b, c, 0, 0, 0); }
DEV unsigned cvtpk(float lo, float hi) { const bf16x2_t v = __builtin_convertvector(f32x2{lo, hi}, bf16x2_t); return __builtin_bit_cast(unsigned, v); }
DEV float dot2(unsigned a, unsigned b, float acc) { return __builtin_amdgcn_fdot2_f32_bf16(__builtin_bit_cast(bf16x2_t, a), __builtin_bit_cast(bf16x2_t, b), acc, false); }
DEV float bflo(unsigned u) { return __uint_as_float(u << 16); }
DEV float bfhi(unsigned u) { return __uint_as_float(u & 0xffff0000u); }
DEV float bfround(float x) { return bflo(cvtpk(x, x)); }
DEV float xhalf_sum(float x) { auto r = __builtin_amdgcn_permlane32_swap(__float_as_uint(x), __float_as_uint(x), false, false); return __uint_as_float(r[0]) + __uint_as_float(r[1]); }
DEV float xhalf_max(float x) { auto r = __builtin_amdgcn_permlane32_swap(__float_as_uint(x), __float_as_uint(x), false, false); return fmaxf(__uint_as_float(r[0]), __uint_as_float(r[1])); }
DEV float lo_bcast(float x) { auto r = __builtin_amdgcn_permlane32_swap(__float_as_uint(x), __float_as_uint(x), false, false); return __uint_as_float(r[0]); }
template <int CTRL> DEV unsigned dpp_u(unsigned x) { return (unsigned)__builtin_amdgcn_mov_dpp((int)x, CTRL, 0xf, 0xf, true); }
DEV unsigned wave_umax(unsigned x) {
  x = max(x, dpp_u<0xB1>(x)); x = max(x, dpp_u<0x4E>(x)); x = max(x, dpp_u<0x141>(x)); x = max(x, dpp_u<0x128>(x));
  auto a = __builtin_amdgcn_permlane16_swap(x, x, false, false); x = max(a[0], a[1]);
  auto b = __builtin_amdgcn_permlane32_swap(x, x, false, false); return max(b[0], b[1]);
}
DEV bf16x8 as_bf(u32x4 v) { return __builtin_bit_cast(bf16x8, v); }
DEV u32x4 as_u(bf16x8 v) { return __builtin_bit_cast(u32x4, v); }
DEV srd_t mk_srd(const void* p, unsigned bytes) { return __builtin_amdgcn_make_buffer_rsrc((void*)p, (short)0, (int)bytes, 0x00020000); }
DEV void dma(unsigned voff, srd_t srd, unsigned soff, unsigned ldsaddr) {
  asm volatile("s_mov_b32 m0, %3\n\ts_nop 0\n\tbuffer_load_dwordx4 %0, %1, %2 offen lds" ::"v"(voff), "s"(srd), "s"(soff), "s"(ldsaddr) : "memory", "m0");
}
DEV void dma_dev(unsigned voff, srd_t srd, unsigned soff, unsigned ldsaddr) {
  asm volatile("s_mov_b32 m0, %3\n\ts_nop 0\n\tbuffer_load_dwordx4 %0, %1, %2 offen " HLD " lds" ::"v"(voff), "s"(srd), "s"(soff), "s"(ldsaddr) : "memory", "m0");
}
DEV void touch(unsigned voff, srd_t srd, unsigned ldsaddr) {
  asm volatile("s_mov_b32 m0, %2\n\ts_nop 0\n\tbuffer_load_dword %0, %1, 0 offen lds" ::"v"(voff), "s"(srd), "s"(ldsaddr) : "memory", "m0");
}
DEV void st128(srd_t ws, unsigned off, u32x4 v) { __builtin_amdgcn_raw_buffer_store_b128(v, ws, off, 0, 16); }
DEV void st64(srd_t ws, unsigned off, u32x2 v) { __builtin_amdgcn_raw_buffer_store_b64(v, ws, off, 0, 16); }
DEV void st32(srd_t ws, unsigned off, unsigned v) { __builtin_amdgcn_raw_buffer_store_b32(v, ws, off, 0, 16); }
DEV u32x4 ld128(srd_t ws, unsigned off) { return __builtin_amdgcn_raw_buffer_load_b128(ws, off, 0, 16); }
DEV u32x2 ld64(srd_t ws, unsigned off) { return __builtin_amdgcn_raw_buffer_load_b64(ws, off, 0, 16); }
DEV unsigned ld32(srd_t ws, unsigned off) { return __builtin_amdgcn_raw_buffer_load_b32(ws, off, 0, 16); }
DEV void st128s(srd_t ws, unsigned voff, unsigned soff, u32x4 v) { asm volatile("buffer_store_dwordx4 %0, %1, %2, %3 offen sc1\n\ts_nop 1" ::"v"(v), "v"(voff), "s"(ws), "s"(soff) : "memory"); }
DEV void st32s(srd_t ws, unsigned voff, unsigned soff, unsigned v) { __builtin_amdgcn_raw_buffer_store_b32(v, ws, voff, soff, 16); }
DEV u32x4 ld128s(srd_t ws, unsigned voff, unsigned soff) { return __builtin_amdgcn_raw_buffer_load_b128(ws, voff, soff, HAUX); }
DEV void ld32_raw(unsigned& d, srd_t r, unsigned voff, unsigned soff) { asm volatile("buffer_load_dword %0, %1, %2, %3 offen" : "=v"(d) : "v"(voff), "s"(r), "s"(soff)); }
constexpr unsigned OOB = 0x7ffffff0u;
DEV float frcp(float x) { return __builtin_amdgcn_rcpf(x); }
DEV float fsqrt(float x) { return __builtin_amdgcn_sqrtf(x); }

#define STAMP(i)
#define WAIT_VM(n) asm volatile("s_waitcnt vmcnt(" #n ")" ::: "memory")
template <int N> DEV void wait_vm() { asm volatile("s_waitcnt vmcnt(%0)" ::"i"(N) : "memory"); }
#define BARRIER() asm volatile("s_waitcnt lgkmcnt(0)\n\ts_barrier" ::: "memory")
#define SB() __builtin_amdgcn_sched_barrier(0)
DEV float gelu_f(float x) {
  constexpr float C0 = -2.f * 0.7978845608028654f * 1.4426950408889634f, C1 = C0 * 0.044715f;
  float z = x * __builtin_fmaf(x * x, C1, C0);
  return x * __builtin_amdgcn_rcpf(1.f + __builtin_amdgcn_exp2f(z));
}
DEV bf16x8 lds_b128(unsigned a) { return *(const LDSP(bf16x8))(lds_cptr)(uintptr_t)a; }
DEV s16x4 lds_tr(unsigned a) { return __builtin_bit_cast(s16x4, __builtin_amdgcn_ds_read_tr16_b64_v4i16((LDSP(v4i16_t))(lds_cptr)(uintptr_t)a)); }
DEV bf16x8 join(s16x4 lo, s16x4 hi) { return bf16x8{lo[0], lo[1], lo[2], lo[3], hi[0], hi[1], hi[2], hi[3]}; }
template <class T> DEV void pin_v(T& x) { asm volatile("" : "+v"(x)); }
template <class T> DEV void pin_a(T& x) { asm volatile("" : "+a"(x)); }

template <int... I, class F> DEV void static_for_impl(std::integer_sequence<int, I...>, F&& f) { (f(std::integral_constant<int, I>{}), ...); }
template <int N, class F> DEV void static_for(F&& f) { static_for_impl(std::make_integer_sequence<int, N>{}, f); }

DEV int lane_id() { int l; asm volatile("v_mbcnt_lo_u32_b32 %0, -1, 0\n\tv_mbcnt_hi_u32_b32 %0, -1, %0" : "=v"(l)); return l; }

struct Params {
  const unsigned short *x, *wqkv, *wo, *w1, *w2, *w3, *w4;
  char* ws;
  unsigned short* out;
};

constexpr int LDS_CTRL = 155648, LDS_POLL = LDS_CTRL + 256, LDS_TOTAL = LDS_POLL + 1024;
DEV void publish(unsigned* flag, unsigned tag, int w) {
  WAIT_VM(0);
  BARRIER();
  if (w == 0 && lane_id() == 0) __hip_atomic_store(flag, tag, __ATOMIC_RELAXED, __HIP_MEMORY_SCOPE_AGENT);
}
DEV void await(const unsigned* flags, int idx, bool active, unsigned tag) {
  const unsigned long long t0 = __builtin_amdgcn_s_memrealtime();
  for (;;) {
    const unsigned v = active ? __hip_atomic_load(flags + idx, __ATOMIC_RELAXED, __HIP_MEMORY_SCOPE_AGENT) : tag;
    if (__all(v == tag)) break;
    __builtin_amdgcn_s_sleep(2);
    if (__builtin_amdgcn_s_memrealtime() - t0 > 2000000ull) break;
  }
  asm volatile("" ::: "memory");
}

DEV void await_wg(srd_t ws, unsigned ldsb, int idx, bool active, unsigned tag, int w) {
  if (w == 0) {
    const unsigned long long mask = __ballot(active);
    const unsigned voff = active ? WS_FLAG + idx * 4 : OOB, scr = ldsb + LDS_POLL, la = scr + lane_id() * 4;
    unsigned t, n;
#define POLL_ISSUE(i) "s_add_u32 m0, %[scr], " #i "*256\n\ts_nop 0\n\tbuffer_load_dword %[vo], %[ws], 0 offen sc1 lds\n\t"
#define POLL_CHECK(i) "s_waitcnt vmcnt(3)\n\tds_read_b32 %[t], %[la] offset:" #i "*256\n\ts_waitcnt lgkmcnt(0)\n\tv_cmp_ne_u32 vcc, %[tag], %[t]\n\ts_and_b64 vcc, vcc, %[mask]\n\ts_cbranch_scc0 .Lpd_%=\n\t" POLL_ISSUE(i)
    asm volatile("s_mov_b32 %[n], 0x10000\n\t"
                 POLL_ISSUE(0) "s_sleep 6\n\t" POLL_ISSUE(1) "s_sleep 6\n\t" POLL_ISSUE(2) "s_sleep 6\n\t" POLL_ISSUE(3)
                 ".Lpl_%=:\n\t" POLL_CHECK(0) POLL_CHECK(1) POLL_CHECK(2) POLL_CHECK(3)
                 "s_sub_u32 %[n], %[n], 1\n\ts_cmp_lg_u32 %[n], 0\n\ts_cbranch_scc1 .Lpl_%=\n"
                 ".Lpd_%=:"
                 : [t] "=&v"(t), [n] "=&s"(n) : [scr] "s"(scr), [vo] "v"(voff), [ws] "s"(ws), [la] "v"(la), [tag] "s"(tag), [mask] "s"(mask) : "memory", "scc", "vcc", "m0");
  }
  BARRIER();
}

constexpr int dw_feature(int di, int hh) { return 32 * (di >> 3) + 8 * ((di & 7) >> 1) + 4 * hh + 2 * (di & 1); }
constexpr int head_dw(int hl, int hh, int p) {
  int cnt = 0;
  for (int di = 0; di < 40; di++) { int n = dw_feature(di, hh); if (n >= 18 * hl && n < 18 * hl + 18 && n < 144) { if (cnt == p) return di; cnt++; } }
  return -1;
}

constexpr int P1_CH = 36864;
constexpr int P1_AUX = 4 * P1_CH;

typedef float f32x8 __attribute__((ext_vector_type(8)));
typedef unsigned u32x8 __attribute__((ext_vector_type(8)));
#define P1_DBG_OUT
#define P1_OPERANDS \
      : "={v[128:143]}"(acc[0]), "={v[144:159]}"(acc[1]), "={v[160:175]}"(acc[2]), "={v[176:191]}"(acc[3]), "={v[192:207]}"(acc[4]), \
        "+{v[108:111]}"(bo), "+{v[112:115]}"(ao), "+{v[116:123]}"(st) P1_DBG_OUT \
      : "{v[96:99]}"(xoff), "{v[100:107]}"(woff), [xs] "s"(xs), [ws] "s"(wsr), [xso] "s"(xso), [wso] "s"(wso), [wst4] "s"(wst4), [m0x] "s"(m0x), [m0w] "s"(m0w) \
      : "memory", "scc", "m0", "s64", "s65", "s66", "s67", "s68", "s69", "s70", "s71", "s72", "s73", "v208", "v209", "v210", "v211", "v212", "v213", "v214", "v215", "v216", "v217", "v218", "v219", "v220", "v221", "v222", "v223", "v224", "v225", "v226", "v227", "v228", "v229", "v230", "v231", "v232", "v233", "v234", "v235", "v236", "v237", "v238", "v239", "v240", "v241", "v242", "v243", "v244", "v245", "v246", "v247", "v248", "v249", "v250", "v251", "v252", "v253", "v254", "v255"
template <bool VSEC>
DEV void p1_main(srd_t xs, unsigned xso, srd_t wsr, unsigned wso, unsigned wst4, unsigned m0x, unsigned m0w, u32x4 xoff, u32x8 woff, u32x4 bo, u32x4 ao, f32x16 (&acc)[5], f32x8& st, unsigned long long (&dbg)[4]) {
  if constexpr (VSEC) asm volatile(
    "s_mov_b32 s71, 0xfffe5000\n\t"
    "s_mov_b32 s70, 0x3f803f80\n\t"
    "s_mov_b32 s64, 0\n\t"
    "s_mov_b32 s66, 0\n\t"
    "s_mov_b32 s67, %[xso]\n\t"
    "s_mov_b32 s68, %[wso]\n\t"
    ".Lp1a_%=:\n"
    "s_add_u32 m0, s66, %[m0x]\n\t"
    "s_nop 0\n\t"
    "buffer_load_dwordx4 v96, %[xs], s67 offen lds\n\t"
    "s_add_u32 m0, m0, 0x400\n\t"
    "s_nop 0\n\t"
    "buffer_load_dwordx4 v97, %[xs], s67 offen lds\n\t"
    "s_add_u32 m0, m0, 0x400\n\t"
    "s_nop 0\n\t"
    "buffer_load_dwordx4 v98, %[xs], s67 offen lds\n\t"
    "s_add_u32 m0, m0, 0x400\n\t"
    "s_nop 0\n\t"
    "buffer_load_dwordx4 v99, %[xs], s67 offen lds\n\t"
    "s_add_u32 m0, s66, %[m0w]\n\t"
    "s_nop 0\n\t"
    "buffer_load_dwordx4 v100, %[ws], s68 offen lds\n\t"
    "s_add_u32 m0, m0, 0x400\n\t"
    "s_nop 0\n\t"
    "buffer_load_dwordx4 v101, %[ws], s68 offen lds\n\t"
    "s_add_u32 m0, m0, 0x400\n\t"
    "s_nop 0\n\t"
    "buffer_load_dwordx4 v102, %[ws], s68 offen lds\n\t"
    "s_add_u32 m0, m0, 0x400\n\t"
    "s_nop 0\n\t"
    "buffer_load_dwordx4 v103, %[ws], s68 offen lds\n\t"
    "s_add_u32 m0, m0, 0x400\n\t"
    "s_nop 0\n\t"
    "buffer_load_dwordx4 v104, %[ws], s68 offen lds\n\t"
    "s_add_u32 s67, s67, 0x80\n\t"
    "s_add_u32 s68, s68, %[wst4]\n\t"
    "s_add_u32 s66, s66, 0x9000\n\t"
    "s_add_u32 s64, s64, 1\n\t"
    "s_cmp_lt_u32 s64, 2\n\t"
    "s_cbranch_scc1 .Lp1a_%=\n\t"
    "s_add_u32 m0, s66, %[m0x]\n\t"
    "s_nop 0\n\t"
    "buffer_load_dwordx4 v96, %[xs], s67 offen lds\n\t"
    "s_add_u32 m0, m0, 0x400\n\t"
    "s_nop 0\n\t"
    "buffer_load_dwordx4 v97, %[xs], s67 offen lds\n\t"
    "s_add_u32 m0, m0, 0x400\n\t"
    "s_nop 0\n\t"
    "buffer_load_dwordx4 v98, %[xs], s67 offen lds\n\t"
    "s_add_u32 m0, m0, 0x400\n\t"
    "s_nop 0\n\t"
    "buffer_load_dwordx4 v99, %[xs], s67 offen lds\n\t"
    "s_add_u32 m0, s66, %[m0w]\n\t"
    "s_nop 0\n\t"
    "buffer_load_dwordx4 v100, %[ws], s68 offen lds\n\t"
    "s_add_u32 s72, s66, %[m0x]\n\t"
    "s_add_u32 s73, s66, %[m0w]\n\t"
    "v_mov_b32 v128, 0\n\t"
    "v_mov_b32 v129, 0\n\t"
    "v_mov_b32 v130, 0\n\t"
    "v_mov_b32 v131, 0\n\t"
    "v_mov_b32 v132, 0\n\t"
    "v_mov_b32 v133, 0\n\t"
    "v_mov_b32 v134, 0\n\t"
    "v_mov_b32 v135, 0\n\t"
    "v_mov_b32 v136, 0\n\t"
    "v_mov_b32 v137, 0\n\t"
    "v_mov_b32 v138, 0\n\t"
    "v_mov_b32 v139, 0\n\t"
    "v_mov_b32 v140, 0\n\t"
    "v_mov_b32 v141, 0\n\t"
    "v_mov_b32 v142, 0\n\t"
    "v_mov_b32 v143, 0\n\t"
    "v_mov_b32 v144, 0\n\t"
    "v_mov_b32 v145, 0\n\t"
    "v_mov_b32 v146, 0\n\t"
    "v_mov_b32 v147, 0\n\t"
    "v_mov_b32 v148, 0\n\t"
    "v_mov_b32 v149, 0\n\t"
    "v_mov_b32 v150, 0\n\t"
    "v_mov_b32 v151, 0\n\t"
    "v_mov_b32 v152, 0\n\t"
    "v_mov_b32 v153, 0\n\t"
    "v_mov_b32 v154, 0\n\t"
    "v_mov_b32 v155, 0\n\t"
    "v_mov_b32 v156, 0\n\t"
    "v_mov_b32 v157, 0\n\t"
    "v_mov_b32 v158, 0\n\t"
    "v_mov_b32 v159, 0\n\t"
    "v_mov_b32 v160, 0\n\t"
    "v_mov_b32 v161, 0\n\t"
    "v_mov_b32 v162, 0\n\t"
    "v_mov_b32 v163, 0\n\t"
    "v_mov_b32 v164, 0\n\t"
    "v_mov_b32 v165, 0\n\t"
    "v_mov_b32 v166, 0\n\t"
    "v_mov_b32 v167, 0\n\t"
    "v_mov_b32 v168, 0\n\t"
    "v_mov_b32 v169, 0\n\t"
    "v_mov_b32 v170, 0\n\t"
    "v_mov_b32 v171, 0\n\t"
    "v_mov_b32 v172, 0\n\t"
    "v_mov_b32 v173, 0\n\t"
    "v_mov_b32 v174, 0\n\t"
    "v_mov_b32 v175, 0\n\t"
    "v_mov_b32 v176, 0\n\t"
    "v_mov_b32 v177, 0\n\t"
    "v_mov_b32 v178, 0\n\t"
    "v_mov_b32 v179, 0\n\t"
    "v_mov_b32 v180, 0\n\t"
    "v_mov_b32 v181, 0\n\t"
    "v_mov_b32 v182, 0\n\t"
    "v_mov_b32 v183, 0\n\t"
    "v_mov_b32 v184, 0\n\t"
    "v_mov_b32 v185, 0\n\t"
    "v_mov_b32 v186, 0\n\t"
    "v_mov_b32 v187, 0\n\t"
    "v_mov_b32 v188, 0\n\t"
    "v_mov_b32 v189, 0\n\t"
    "v_mov_b32 v190, 0\n\t"
    "v_mov_b32 v191, 0\n\t"
    "v_mov_b32 v192, 0\n\t"
    "v_mov_b32 v193, 0\n\t"
    "v_mov_b32 v194, 0\n\t"
    "v_mov_b32 v195, 0\n\t"
    "v_mov_b32 v196, 0\n\t"
    "v_mov_b32 v197, 0\n\t"
    "v_mov_b32 v198, 0\n\t"
    "v_mov_b32 v199, 0\n\t"
    "v_mov_b32 v200, 0\n\t"
    "v_mov_b32 v201, 0\n\t"
    "v_mov_b32 v202, 0\n\t"
    "v_mov_b32 v203, 0\n\t"
    "v_mov_b32 v204, 0\n\t"
    "v_mov_b32 v205, 0\n\t"
    "v_mov_b32 v206, 0\n\t"
    "v_mov_b32 v207, 0\n\t"
    "s_mov_b32 s65, 0x9000\n\t"
    "s_mov_b32 s64, 0\n\t"
    "s_waitcnt vmcnt(14)\n\t"
    "s_barrier\n\t"
    "ds_read_b128 v[208:211], v108\n\t"
    "ds_read_b64_tr_b16 v[212:213], v112 offset:0\n\t"
    "ds_read_b64_tr_b16 v[214:215], v112 offset:1280\n\t"
    "ds_read_b64_tr_b16 v[216:217], v112 offset:64\n\t"
    "ds_read_b64_tr_b16 v[218:219], v112 offset:1344\n\t"
    "ds_read_b64_tr_b16 v[220:221], v112 offset:128\n\t"
    "ds_read_b64_tr_b16 v[222:223], v112 offset:1408\n\t"
    "ds_read_b64_tr_b16 v[224:225], v112 offset:192\n\t"
    "ds_read_b64_tr_b16 v[226:227], v112 offset:1472\n\t"
    "ds_read_b64_tr_b16 v[228:229], v112 offset:256\n\t"
    "ds_read_b64_tr_b16 v[230:231], v112 offset:1536\n\t"
    "v_add_u32 v108, s65, v108\n\t"
    "v_add_u32 v112, s65, v112\n\t"
    ".Lp1b_%=:\n"
    "s_waitcnt lgkmcnt(0)\n\t"
    "v_mfma_f32_32x32x16_bf16 v[128:143], v[208:211], v[212:215], v[128:143]\n\t"
    "ds_read_b128 v[232:235], v109\n\t"
    "ds_read_b64_tr_b16 v[236:237], v113 offset:0\n\t"
    "ds_read_b64_tr_b16 v[238:239], v113 offset:1280\n\t"
    "ds_read_b64_tr_b16 v[240:241], v113 offset:64\n\t"
    "ds_read_b64_tr_b16 v[242:243], v113 offset:1344\n\t"
    "v_mfma_f32_32x32x16_bf16 v[144:159], v[208:211], v[216:219], v[144:159]\n\t"
    "ds_read_b64_tr_b16 v[244:245], v113 offset:128\n\t"
    "ds_read_b64_tr_b16 v[246:247], v113 offset:1408\n\t"
    "ds_read_b64_tr_b16 v[248:249], v113 offset:192\n\t"
    "ds_read_b64_tr_b16 v[250:251], v113 offset:1472\n\t"
    "ds_read_b64_tr_b16 v[252:253], v113 offset:256\n\t"
    "ds_read_b64_tr_b16 v[254:255], v113 offset:1536\n\t"
    "v_mfma_f32_32x32x16_bf16 v[160:175], v[208:211], v[220:223], v[160:175]\n\t"
    "v_add_u32 v109, s65, v109\n\t"
    "v_add_u32 v113, s65, v113\n\t"
    "v_mfma_f32_32x32x16_bf16 v[176:191], v[208:211], v[224:227], v[176:191]\n\t"
    "s_add_u32 m0, s73, 1024\n\t"
    "s_nop 0\n\t"
    "buffer_load_dwordx4 v101, %[ws], s68 offen lds\n\t"
    "v_mfma_f32_32x32x16_bf16 v[192:207], v[208:211], v[228:231], v[192:207]\n\t"
    "s_add_u32 m0, s73, 2048\n\t"
    "s_nop 0\n\t"
    "buffer_load_dwordx4 v102, %[ws], s68 offen lds\n\t"
    "v_dot2c_f32_bf16 v116, s70, v208\n\t"
    "v_dot2c_f32_bf16 v117, v208, v208\n\t"
    "v_dot2c_f32_bf16 v116, s70, v209\n\t"
    "v_dot2c_f32_bf16 v117, v209, v209\n\t"
    "v_dot2c_f32_bf16 v116, s70, v210\n\t"
    "v_dot2c_f32_bf16 v117, v210, v210\n\t"
    "v_dot2c_f32_bf16 v116, s70, v211\n\t"
    "v_dot2c_f32_bf16 v117, v211, v211\n\t"
    "v_dot2c_f32_bf16 v118, s70, v212\n\t"
    "v_dot2c_f32_bf16 v119, s70, v216\n\t"
    "v_dot2c_f32_bf16 v120, s70, v220\n\t"
    "v_dot2c_f32_bf16 v121, s70, v224\n\t"
    "v_dot2c_f32_bf16 v122, s70, v228\n\t"
    "v_dot2c_f32_bf16 v118, s70, v213\n\t"
    "v_dot2c_f32_bf16 v119, s70, v217\n\t"
    "v_dot2c_f32_bf16 v120, s70, v221\n\t"
    "v_dot2c_f32_bf16 v121, s70, v225\n\t"
    "v_dot2c_f32_bf16 v122, s70, v229\n\t"
    "v_dot2c_f32_bf16 v118, s70, v214\n\t"
    "v_dot2c_f32_bf16 v119, s70, v218\n\t"
    "v_dot2c_f32_bf16 v120, s70, v222\n\t"
    "v_dot2c_f32_bf16 v121, s70, v226\n\t"
    "v_dot2c_f32_bf16 v122, s70, v230\n\t"
    "v_dot2c_f32_bf16 v118, s70, v215\n\t"
    "v_dot2c_f32_bf16 v119, s70, v219\n\t"
    "v_dot2c_f32_bf16 v120, s70, v223\n\t"
    "v_dot2c_f32_bf16 v121, s70, v227\n\t"
    "v_dot2c_f32_bf16 v122, s70, v231\n\t"
    "s_waitcnt lgkmcnt(0)\n\t"
    "v_mfma_f32_32x32x16_bf16 v[128:143], v[232:235], v[236:239], v[128:143]\n\t"
    "ds_read_b128 v[208:211], v110\n\t"
    "ds_read_b64_tr_b16 v[212:213], v114 offset:0\n\t"
    "ds_read_b64_tr_b16 v[214:215], v114 offset:1280\n\t"
    "ds_read_b64_tr_b16 v[216:217], v114 offset:64\n\t"
    "ds_read_b64_tr_b16 v[218:219], v114 offset:1344\n\t"
    "v_mfma_f32_32x32x16_bf16 v[144:159], v[232:235], v[240:243], v[144:159]\n\t"
    "ds_read_b64_tr_b16 v[220:221], v114 offset:128\n\t"
    "ds_read_b64_tr_b16 v[222:223], v114 offset:1408\n\t"
    "ds_read_b64_tr_b16 v[224:225], v114 offset:192\n\t"
    "ds_read_b64_tr_b16 v[226:227], v114 offset:1472\n\t"
    "ds_read_b64_tr_b16 v[228:229], v114 offset:256\n\t"
    "ds_read_b64_tr_b16 v[230:231], v114 offset:1536\n\t"
    "v_mfma_f32_32x32x16_bf16 v[160:175], v[232:235], v[244:247], v[160:175]\n\t"
    "v_add_u32 v110, s65, v110\n\t"
    "v_add_u32 v114, s65, v114\n\t"
    "v_mfma_f32_32x32x16_bf16 v[176:191], v[232:235], v[248:251], v[176:191]\n\t"
    "s_add_u32 m0, s73, 3072\n\t"
    "s_nop 0\n\t"
    "buffer_load_dwordx4 v103, %[ws], s68 offen lds\n\t"
    "v_mfma_f32_32x32x16_bf16 v[192:207], v[232:235], v[252:255], v[192:207]\n\t"
    "s_add_u32 m0, s73, 4096\n\t"
    "s_nop 0\n\t"
    "buffer_load_dwordx4 v104, %[ws], s68 offen lds\n\t"
    "s_add_u32 s66, s66, 0x9000\n\t"
    "s_cmp_eq_u32 s66, 0x24000\n\t"
    "s_cselect_b32 s66, 0, s66\n\t"
    "s_add_u32 s72, s66, %[m0x]\n\t"
    "s_add_u32 s73, s66, %[m0w]\n\t"
    "s_add_u32 s67, s67, 0x80\n\t"
    "s_cmp_ge_u32 s64, 6\n\t"
    "s_cselect_b32 s67, 0x7ff00000, s67\n\t"
    "s_add_u32 s68, s68, %[wst4]\n\t"
    "v_dot2c_f32_bf16 v116, s70, v232\n\t"
    "v_dot2c_f32_bf16 v117, v232, v232\n\t"
    "v_dot2c_f32_bf16 v116, s70, v233\n\t"
    "v_dot2c_f32_bf16 v117, v233, v233\n\t"
    "v_dot2c_f32_bf16 v116, s70, v234\n\t"
    "v_dot2c_f32_bf16 v117, v234, v234\n\t"
    "v_dot2c_f32_bf16 v116, s70, v235\n\t"
    "v_dot2c_f32_bf16 v117, v235, v235\n\t"
    "s_waitcnt vmcnt(9)\n\t"
    "s_barrier\n\t"
    "s_waitcnt lgkmcnt(0)\n\t"
    "v_mfma_f32_32x32x16_bf16 v[128:143], v[208:211], v[212:215], v[128:143]\n\t"
    "ds_read_b128 v[232:235], v111\n\t"
    "ds_read_b64_tr_b16 v[236:237], v115 offset:0\n\t"
    "ds_read_b64_tr_b16 v[238:239], v115 offset:1280\n\t"
    "ds_read_b64_tr_b16 v[240:241], v115 offset:64\n\t"
    "ds_read_b64_tr_b16 v[242:243], v115 offset:1344\n\t"
    "v_mfma_f32_32x32x16_bf16 v[144:159], v[208:211], v[216:219], v[144:159]\n\t"
    "ds_read_b64_tr_b16 v[244:245], v115 offset:128\n\t"
    "ds_read_b64_tr_b16 v[246:247], v115 offset:1408\n\t"
    "ds_read_b64_tr_b16 v[248:249], v115 offset:192\n\t"
    "ds_read_b64_tr_b16 v[250:251], v115 offset:1472\n\t"
    "ds_read_b64_tr_b16 v[252:253], v115 offset:256\n\t"
    "ds_read_b64_tr_b16 v[254:255], v115 offset:1536\n\t"
    "v_mfma_f32_32x32x16_bf16 v[160:175], v[208:211], v[220:223], v[160:175]\n\t"
    "v_add_u32 v111, s65, v111\n\t"
    "v_add_u32 v115, s65, v115\n\t"
    "s_add_u32 m0, s72, 0\n\t"
    "s_nop 0\n\t"
    "buffer_load_dwordx4 v96, %[xs], s67 offen lds\n\t"
    "v_mfma_f32_32x32x16_bf16 v[176:191], v[208:211], v[224:227], v[176:191]\n\t"
    "s_add_u32 m0, s72, 1024\n\t"
    "s_nop 0\n\t"
    "buffer_load_dwordx4 v97, %[xs], s67 offen lds\n\t"
    "v_mfma_f32_32x32x16_bf16 v[192:207], v[208:211], v[228:231], v[192:207]\n\t"
    "s_add_u32 m0, s72, 2048\n\t"
    "s_nop 0\n\t"
    "buffer_load_dwordx4 v98, %[xs], s67 offen lds\n\t"
    "v_dot2c_f32_bf16 v116, s70, v208\n\t"
    "v_dot2c_f32_bf16 v117, v208, v208\n\t"
    "v_dot2c_f32_bf16 v116, s70, v209\n\t"
    "v_dot2c_f32_bf16 v117, v209, v209\n\t"
    "v_dot2c_f32_bf16 v116, s70, v210\n\t"
    "v_dot2c_f32_bf16 v117, v210, v210\n\t"
    "v_dot2c_f32_bf16 v116, s70, v211\n\t"
    "v_dot2c_f32_bf16 v117, v211, v211\n\t"
    "s_add_u32 s64, s64, 1\n\t"
    "s_and_b32 s69, s64, 3\n\t"
    "s_cmp_eq_u32 s69, 3\n\t"
    "s_mov_b32 s65, 0x9000\n\t"
    "s_cselect_b32 s65, s71, s65\n\t"
    "s_waitcnt lgkmcnt(0)\n\t"
    "v_mfma_f32_32x32x16_bf16 v[128:143], v[232:235], v[236:239], v[128:143]\n\t"
    "ds_read_b128 v[208:211], v108\n\t"
    "ds_read_b64_tr_b16 v[212:213], v112 offset:0\n\t"
    "ds_read_b64_tr_b16 v[214:215], v112 offset:1280\n\t"
    "ds_read_b64_tr_b16 v[216:217], v112 offset:64\n\t"
    "ds_read_b64_tr_b16 v[218:219], v112 offset:1344\n\t"
    "v_mfma_f32_32x32x16_bf16 v[144:159], v[232:235], v[240:243], v[144:159]\n\t"
    "ds_read_b64_tr_b16 v[220:221], v112 offset:128\n\t"
    "ds_read_b64_tr_b16 v[222:223], v112 offset:1408\n\t"
    "ds_read_b64_tr_b16 v[224:225], v112 offset:192\n\t"
    "ds_read_b64_tr_b16 v[226:227], v112 offset:1472\n\t"
    "ds_read_b64_tr_b16 v[228:229], v112 offset:256\n\t"
    "ds_read_b64_tr_b16 v[230:231], v112 offset:1536\n\t"
    "v_mfma_f32_32x32x16_bf16 v[160:175], v[232:235], v[244:247], v[160:175]\n\t"
    "v_add_u32 v108, s65, v108\n\t"
    "v_add_u32 v112, s65, v112\n\t"
    "v_mfma_f32_32x32x16_bf16 v[176:191], v[232:235], v[248:251], v[176:191]\n\t"
    "s_add_u32 m0, s72, 3072\n\t"
    "s_nop 0\n\t"
    "buffer_load_dwordx4 v99, %[xs], s67 offen lds\n\t"
    "v_mfma_f32_32x32x16_bf16 v[192:207], v[232:235], v[252:255], v[192:207]\n\t"
    "s_add_u32 m0, s73, 0\n\t"
    "s_nop 0\n\t"
    "buffer_load_dwordx4 v100, %[ws], s68 offen lds\n\t"
    "v_dot2c_f32_bf16 v116, s70, v232\n\t"
    "v_dot2c_f32_bf16 v117, v232, v232\n\t"
    "v_dot2c_f32_bf16 v116, s70, v233\n\t"
    "v_dot2c_f32_bf16 v117, v233, v233\n\t"
    "v_dot2c_f32_bf16 v116, s70, v234\n\t"
    "v_dot2c_f32_bf16 v117, v234, v234\n\t"
    "v_dot2c_f32_bf16 v116, s70, v235\n\t"
    "v_dot2c_f32_bf16 v117, v235, v235\n\t"
    "s_cmp_lt_u32 s64, 9\n\t"
    "s_cbranch_scc1 .Lp1b_%=\n\t"
    "s_waitcnt vmcnt(0) lgkmcnt(0)\n\t"
    "s_nop 15\n\t"
    "s_nop 3\n\t"
      P1_OPERANDS);
  else asm volatile(
    "s_mov_b32 s71, 0xfffe5000\n\t"
    "s_mov_b32 s70, 0x3f803f80\n\t"
    "s_mov_b32 s64, 0\n\t"
    "s_mov_b32 s66, 0\n\t"
    "s_mov_b32 s67, %[xso]\n\t"
    "s_mov_b32 s68, %[wso]\n\t"
    ".Lp1a_%=:\n"
    "s_add_u32 m0, s66, %[m0x]\n\t"
    "s_nop 0\n\t"
    "buffer_load_dwordx4 v96, %[xs], s67 offen lds\n\t"
    "s_add_u32 m0, m0, 0x400\n\t"
    "s_nop 0\n\t"
    "buffer_load_dwordx4 v97, %[xs], s67 offen lds\n\t"
    "s_add_u32 m0, m0, 0x400\n\t"
    "s_nop 0\n\t"
    "buffer_load_dwordx4 v98, %[xs], s67 offen lds\n\t"
    "s_add_u32 m0, m0, 0x400\n\t"
    "s_nop 0\n\t"
    "buffer_load_dwordx4 v99, %[xs], s67 offen lds\n\t"
    "s_add_u32 m0, s66, %[m0w]\n\t"
    "s_nop 0\n\t"
    "buffer_load_dwordx4 v100, %[ws], s68 offen lds\n\t"
    "s_add_u32 m0, m0, 0x400\n\t"
    "s_nop 0\n\t"
    "buffer_load_dwordx4 v101, %[ws], s68 offen lds\n\t"
    "s_add_u32 m0, m0, 0x400\n\t"
    "s_nop 0\n\t"
    "buffer_load_dwordx4 v102, %[ws], s68 offen lds\n\t"
    "s_add_u32 m0, m0, 0x400\n\t"
    "s_nop 0\n\t"
    "buffer_load_dwordx4 v103, %[ws], s68 offen lds\n\t"
    "s_add_u32 m0, m0, 0x400\n\t"
    "s_nop 0\n\t"
    "buffer_load_dwordx4 v104, %[ws], s68 offen lds\n\t"
    "s_add_u32 s67, s67, 0x80\n\t"
    "s_add_u32 s68, s68, %[wst4]\n\t"
    "s_add_u32 s66, s66, 0x9000\n\t"
    "s_add_u32 s64, s64, 1\n\t"
    "s_cmp_lt_u32 s64, 2\n\t"
    "s_cbranch_scc1 .Lp1a_%=\n\t"
    "s_add_u32 m0, s66, %[m0x]\n\t"
    "s_nop 0\n\t"
    "buffer_load_dwordx4 v96, %[xs], s67 offen lds\n\t"
    "s_add_u32 m0, m0, 0x400\n\t"
    "s_nop 0\n\t"
    "buffer_load_dwordx4 v97, %[xs], s67 offen lds\n\t"
    "s_add_u32 m0, m0, 0x400\n\t"
    "s_nop 0\n\t"
    "buffer_load_dwordx4 v98, %[xs], s67 offen lds\n\t"
    "s_add_u32 m0, m0, 0x400\n\t"
    "s_nop 0\n\t"
    "buffer_load_dwordx4 v99, %[xs], s67 offen lds\n\t"
    "s_add_u32 m0, s66, %[m0w]\n\t"
    "s_nop 0\n\t"
    "buffer_load_dwordx4 v100, %[ws], s68 offen lds\n\t"
    "s_add_u32 s72, s66, %[m0x]\n\t"
    "s_add_u32 s73, s66, %[m0w]\n\t"
    "v_mov_b32 v128, 0\n\t"
    "v_mov_b32 v129, 0\n\t"
    "v_mov_b32 v130, 0\n\t"
    "v_mov_b32 v131, 0\n\t"
    "v_mov_b32 v132, 0\n\t"
    "v_mov_b32 v133, 0\n\t"
    "v_mov_b32 v134, 0\n\t"
    "v_mov_b32 v135, 0\n\t"
    "v_mov_b32 v136, 0\n\t"
    "v_mov_b32 v137, 0\n\t"
    "v_mov_b32 v138, 0\n\t"
    "v_mov_b32 v139, 0\n\t"
    "v_mov_b32 v140, 0\n\t"
    "v_mov_b32 v141, 0\n\t"
    "v_mov_b32 v142, 0\n\t"
    "v_mov_b32 v143, 0\n\t"
    "v_mov_b32 v144, 0\n\t"
    "v_mov_b32 v145, 0\n\t"
    "v_mov_b32 v146, 0\n\t"
    "v_mov_b32 v147, 0\n\t"
    "v_mov_b32 v148, 0\n\t"
    "v_mov_b32 v149, 0\n\t"
    "v_mov_b32 v150, 0\n\t"
    "v_mov_b32 v151, 0\n\t"
    "v_mov_b32 v152, 0\n\t"
    "v_mov_b32 v153, 0\n\t"
    "v_mov_b32 v154, 0\n\t"
    "v_mov_b32 v155, 0\n\t"
    "v_mov_b32 v156, 0\n\t"
    "v_mov_b32 v157, 0\n\t"
    "v_mov_b32 v158, 0\n\t"
    "v_mov_b32 v159, 0\n\t"
    "v_mov_b32 v160, 0\n\t"
    "v_mov_b32 v161, 0\n\t"
    "v_mov_b32 v162, 0\n\t"
    "v_mov_b32 v163, 0\n\t"
    "v_mov_b32 v164, 0\n\t"
    "v_mov_b32 v165, 0\n\t"
    "v_mov_b32 v166, 0\n\t"
    "v_mov_b32 v167, 0\n\t"
    "v_mov_b32 v168, 0\n\t"
    "v_mov_b32 v169, 0\n\t"
    "v_mov_b32 v170, 0\n\t"
    "v_mov_b32 v171, 0\n\t"
    "v_mov_b32 v172, 0\n\t"
    "v_mov_b32 v173, 0\n\t"
    "v_mov_b32 v174, 0\n\t"
    "v_mov_b32 v175, 0\n\t"
    "v_mov_b32 v176, 0\n\t"
    "v_mov_b32 v177, 0\n\t"
    "v_mov_b32 v178, 0\n\t"
    "v_mov_b32 v179, 0\n\t"
    "v_mov_b32 v180, 0\n\t"
    "v_mov_b32 v181, 0\n\t"
    "v_mov_b32 v182, 0\n\t"
    "v_mov_b32 v183, 0\n\t"
    "v_mov_b32 v184, 0\n\t"
    "v_mov_b32 v185, 0\n\t"
    "v_mov_b32 v186, 0\n\t"
    "v_mov_b32 v187, 0\n\t"
    "v_mov_b32 v188, 0\n\t"
    "v_mov_b32 v189, 0\n\t"
    "v_mov_b32 v190, 0\n\t"
    "v_mov_b32 v191, 0\n\t"
    "v_mov_b32 v192, 0\n\t"
    "v_mov_b32 v193, 0\n\t"
    "v_mov_b32 v194, 0\n\t"
    "v_mov_b32 v195, 0\n\t"
    "v_mov_b32 v196, 0\n\t"
    "v_mov_b32 v197, 0\n\t"
    "v_mov_b32 v198, 0\n\t"
    "v_mov_b32 v199, 0\n\t"
    "v_mov_b32 v200, 0\n\t"
    "v_mov_b32 v201, 0\n\t"
    "v_mov_b32 v202, 0\n\t"
    "v_mov_b32 v203, 0\n\t"
    "v_mov_b32 v204, 0\n\t"
    "v_mov_b32 v205, 0\n\t"
    "v_mov_b32 v206, 0\n\t"
    "v_mov_b32 v207, 0\n\t"
    "s_mov_b32 s65, 0x9000\n\t"
    "s_mov_b32 s64, 0\n\t"
    "s_waitcnt vmcnt(14)\n\t"
    "s_barrier\n\t"
    "ds_read_b128 v[208:211], v108\n\t"
    "ds_read_b64_tr_b16 v[212:213], v112 offset:0\n\t"
    "ds_read_b64_tr_b16 v[214:215], v112 offset:1280\n\t"
    "ds_read_b64_tr_b16 v[216:217], v112 offset:64\n\t"
    "ds_read_b64_tr_b16 v[218:219], v112 offset:1344\n\t"
    "ds_read_b64_tr_b16 v[220:221], v112 offset:128\n\t"
    "ds_read_b64_tr_b16 v[222:223], v112 offset:1408\n\t"
    "ds_read_b64_tr_b16 v[224:225], v112 offset:192\n\t"
    "ds_read_b64_tr_b16 v[226:227], v112 offset:1472\n\t"
    "ds_read_b64_tr_b16 v[228:229], v112 offset:256\n\t"
    "ds_read_b64_tr_b16 v[230:231], v112 offset:1536\n\t"
    "v_add_u32 v108, s65, v108\n\t"
    "v_add_u32 v112, s65, v112\n\t"
    ".Lp1b_%=:\n"
    "s_waitcnt lgkmcnt(0)\n\t"
    "v_mfma_f32_32x32x16_bf16 v[128:143], v[212:215], v[208:211], v[128:143]\n\t"
    "ds_read_b128 v[232:235], v109\n\t"
    "ds_read_b64_tr_b16 v[236:237], v113 offset:0\n\t"
    "ds_read_b64_tr_b16 v[238:239], v113 offset:1280\n\t"
    "ds_read_b64_tr_b16 v[240:241], v113 offset:64\n\t"
    "ds_read_b64_tr_b16 v[242:243], v113 offset:1344\n\t"
    "v_mfma_f32_32x32x16_bf16 v[144:159], v[216:219], v[208:211], v[144:159]\n\t"
    "ds_read_b64_tr_b16 v[244:245], v113 offset:128\n\t"
    "ds_read_b64_tr_b16 v[246:247], v113 offset:1408\n\t"
    "ds_read_b64_tr_b16 v[248:249], v113 offset:192\n\t"
    "ds_read_b64_tr_b16 v[250:251], v113 offset:1472\n\t"
    "ds_read_b64_tr_b16 v[252:253], v113 offset:256\n\t"
    "ds_read_b64_tr_b16 v[254:255], v113 offset:1536\n\t"
    "v_mfma_f32_32x32x16_bf16 v[160:175], v[220:223], v[208:211], v[160:175]\n\t"
    "v_add_u32 v109, s65, v109\n\t"
    "v_add_u32 v113, s65, v113\n\t"
    "v_mfma_f32_32x32x16_bf16 v[176:191], v[224:227], v[208:211], v[176:191]\n\t"
    "s_add_u32 m0, s73, 1024\n\t"
    "s_nop 0\n\t"
    "buffer_load_dwordx4 v101, %[ws], s68 offen lds\n\t"
    "v_mfma_f32_32x32x16_bf16 v[192:207], v[228:231], v[208:211], v[192:207]\n\t"
    "s_add_u32 m0, s73, 2048\n\t"
    "s_nop 0\n\t"
    "buffer_load_dwordx4 v102, %[ws], s68 offen lds\n\t"
    "v_dot2c_f32_bf16 v116, s70, v208\n\t"
    "v_dot2c_f32_bf16 v117, v208, v208\n\t"
    "v_dot2c_f32_bf16 v116, s70, v209\n\t"
    "v_dot2c_f32_bf16 v117, v209, v209\n\t"
    "v_dot2c_f32_bf16 v116, s70, v210\n\t"
    "v_dot2c_f32_bf16 v117, v210, v210\n\t"
    "v_dot2c_f32_bf16 v116, s70, v211\n\t"
    "v_dot2c_f32_bf16 v117, v211, v211\n\t"
    "v_dot2c_f32_bf16 v118, s70, v212\n\t"
    "v_dot2c_f32_bf16 v119, s70, v216\n\t"
    "v_dot2c_f32_bf16 v120, s70, v220\n\t"
    "v_dot2c_f32_bf16 v121, s70, v224\n\t"
    "v_dot2c_f32_bf16 v122, s70, v228\n\t"
    "v_dot2c_f32_bf16 v118, s70, v213\n\t"
    "v_dot2c_f32_bf16 v119, s70, v217\n\t"
    "v_dot2c_f32_bf16 v120, s70, v221\n\t"
    "v_dot2c_f32_bf16 v121, s70, v225\n\t"
    "v_dot2c_f32_bf16 v122, s70, v229\n\t"
    "v_dot2c_f32_bf16 v118, s70, v214\n\t"
    "v_dot2c_f32_bf16 v119, s70, v218\n\t"
    "v_dot2c_f32_bf16 v120, s70, v222\n\t"
    "v_dot2c_f32_bf16 v121, s70, v226\n\t"
    "v_dot2c_f32_bf16 v122, s70, v230\n\t"
    "v_dot2c_f32_bf16 v118, s70, v215\n\t"
    "v_dot2c_f32_bf16 v119, s70, v219\n\t"
    "v_dot2c_f32_bf16 v120, s70, v223\n\t"
    "v_dot2c_f32_bf16 v121, s70, v227\n\t"
    "v_dot2c_f32_bf16 v122, s70, v231\n\t"
    "s_waitcnt lgkmcnt(0)\n\t"
    "v_mfma_f32_32x32x16_bf16 v[128:143], v[236:239], v[232:235], v[128:143]\n\t"
    "ds_read_b128 v[208:211], v110\n\t"
    "ds_read_b64_tr_b16 v[212:213], v114 offset:0\n\t"
    "ds_read_b64_tr_b16 v[214:215], v114 offset:1280\n\t"
    "ds_read_b64_tr_b16 v[216:217], v114 offset:64\n\t"
    "ds_read_b64_tr_b16 v[218:219], v114 offset:1344\n\t"
    "v_mfma_f32_32x32x16_bf16 v[144:159], v[240:243], v[232:235], v[144:159]\n\t"
    "ds_read_b64_tr_b16 v[220:221], v114 offset:128\n\t"
    "ds_read_b64_tr_b16 v[222:223], v114 offset:1408\n\t"
    "ds_read_b64_tr_b16 v[224:225], v114 offset:192\n\t"
    "ds_read_b64_tr_b16 v[226:227], v114 offset:1472\n\t"
    "ds_read_b64_tr_b16 v[228:229], v114 offset:256\n\t"
    "ds_read_b64_tr_b16 v[230:231], v114 offset:1536\n\t"
    "v_mfma_f32_32x32x16_bf16 v[160:175], v[244:247], v[232:235], v[160:175]\n\t"
    "v_add_u32 v110, s65, v110\n\t"
    "v_add_u32 v114, s65, v114\n\t"
    "v_mfma_f32_32x32x16_bf16 v[176:191], v[248:251], v[232:235], v[176:191]\n\t"
    "s_add_u32 m0, s73, 3072\n\t"
    "s_nop 0\n\t"
    "buffer_load_dwordx4 v103, %[ws], s68 offen lds\n\t"
    "v_mfma_f32_32x32x16_bf16 v[192:207], v[252:255], v[232:235], v[192:207]\n\t"
    "s_add_u32 m0, s73, 4096\n\t"
    "s_nop 0\n\t"
    "buffer_load_dwordx4 v104, %[ws], s68 offen lds\n\t"
    "s_add_u32 s66, s66, 0x9000\n\t"
    "s_cmp_eq_u32 s66, 0x24000\n\t"
    "s_cselect_b32 s66, 0, s66\n\t"
    "s_add_u32 s72, s66, %[m0x]\n\t"
    "s_add_u32 s73, s66, %[m0w]\n\t"
    "s_add_u32 s67, s67, 0x80\n\t"
    "s_cmp_ge_u32 s64, 6\n\t"
    "s_cselect_b32 s67, 0x7ff00000, s67\n\t"
    "s_add_u32 s68, s68, %[wst4]\n\t"
    "v_dot2c_f32_bf16 v116, s70, v232\n\t"
    "v_dot2c_f32_bf16 v117, v232, v232\n\t"
    "v_dot2c_f32_bf16 v116, s70, v233\n\t"
    "v_dot2c_f32_bf16 v117, v233, v233\n\t"
    "v_dot2c_f32_bf16 v116, s70, v234\n\t"
    "v_dot2c_f32_bf16 v117, v234, v234\n\t"
    "v_dot2c_f32_bf16 v116, s70, v235\n\t"
    "v_dot2c_f32_bf16 v117, v235, v235\n\t"
    "s_waitcnt vmcnt(9)\n\t"
    "s_barrier\n\t"
    "s_waitcnt lgkmcnt(0)\n\t"
    "v_mfma_f32_32x32x16_bf16 v[128:143], v[212:215], v[208:211], v[128:143]\n\t"
    "ds_read_b128 v[232:235], v111\n\t"
    "ds_read_b64_tr_b16 v[236:237], v115 offset:0\n\t"
    "ds_read_b64_tr_b16 v[238:239], v115 offset:1280\n\t"
    "ds_read_b64_tr_b16 v[240:241], v115 offset:64\n\t"
    "ds_read_b64_tr_b16 v[242:243], v115 offset:1344\n\t"
    "v_mfma_f32_32x32x16_bf16 v[144:159], v[216:219], v[208:211], v[144:159]\n\t"
    "ds_read_b64_tr_b16 v[244:245], v115 offset:128\n\t"
    "ds_read_b64_tr_b16 v[246:247], v115 offset:1408\n\t"
    "ds_read_b64_tr_b16 v[248:249], v115 offset:192\n\t"
    "ds_read_b64_tr_b16 v[250:251], v115 offset:1472\n\t"
    "ds_read_b64_tr_b16 v[252:253], v115 offset:256\n\t"
    "ds_read_b64_tr_b16 v[254:255], v115 offset:1536\n\t"
    "v_mfma_f32_32x32x16_bf16 v[160:175], v[220:223], v[208:211], v[160:175]\n\t"
    "v_add_u32 v111, s65, v111\n\t"
    "v_add_u32 v115, s65, v115\n\t"
    "s_add_u32 m0, s72, 0\n\t"
    "s_nop 0\n\t"
    "buffer_load_dwordx4 v96, %[xs], s67 offen lds\n\t"
    "v_mfma_f32_32x32x16_bf16 v[176:191], v[224:227], v[208:211], v[176:191]\n\t"
    "s_add_u32 m0, s72, 1024\n\t"
    "s_nop 0\n\t"
    "buffer_load_dwordx4 v97, %[xs], s67 offen lds\n\t"
    "v_mfma_f32_32x32x16_bf16 v[192:207], v[228:231], v[208:211], v[192:207]\n\t"
    "s_add_u32 m0, s72, 2048\n\t"
    "s_nop 0\n\t"
    "buffer_load_dwordx4 v98, %[xs], s67 offen lds\n\t"
    "v_dot2c_f32_bf16 v116, s70, v208\n\t"
    "v_dot2c_f32_bf16 v117, v208, v208\n\t"
    "v_dot2c_f32_bf16 v116, s70, v209\n\t"
    "v_dot2c_f32_bf16 v117, v209, v209\n\t"
    "v_dot2c_f32_bf16 v116, s70, v210\n\t"
    "v_dot2c_f32_bf16 v117, v210, v210\n\t"
    "v_dot2c_f32_bf16 v116, s70, v211\n\t"
    "v_dot2c_f32_bf16 v117, v211, v211\n\t"
    "s_add_u32 s64, s64, 1\n\t"
    "s_and_b32 s69, s64, 3\n\t"
    "s_cmp_eq_u32 s69, 3\n\t"
    "s_mov_b32 s65, 0x9000\n\t"
    "s_cselect_b32 s65, s71, s65\n\t"
    "s_waitcnt lgkmcnt(0)\n\t"
    "v_mfma_f32_32x32x16_bf16 v[128:143], v[236:239], v[232:235], v[128:143]\n\t"
    "ds_read_b128 v[208:211], v108\n\t"
    "ds_read_b64_tr_b16 v[212:213], v112 offset:0\n\t"
    "ds_read_b64_tr_b16 v[214:215], v112 offset:1280\n\t"
    "ds_read_b64_tr_b16 v[216:217], v112 offset:64\n\t"
    "ds_read_b64_tr_b16 v[218:219], v112 offset:1344\n\t"
    "v_mfma_f32_32x32x16_bf16 v[144:159], v[240:243], v[232:235], v[144:159]\n\t"
    "ds_read_b64_tr_b16 v[220:221], v112 offset:128\n\t"
    "ds_read_b64_tr_b16 v[222:223], v112 offset:1408\n\t"
    "ds_read_b64_tr_b16 v[224:225], v112 offset:192\n\t"
    "ds_read_b64_tr_b16 v[226:227], v112 offset:1472\n\t"
    "ds_read_b64_tr_b16 v[228:229], v112 offset:256\n\t"
    "ds_read_b64_tr_b16 v[230:231], v112 offset:1536\n\t"
    "v_mfma_f32_32x32x16_bf16 v[160:175], v[244:247], v[232:235], v[160:175]\n\t"
    "v_add_u32 v108, s65, v108\n\t"
    "v_add_u32 v112, s65, v112\n\t"
    "v_mfma_f32_32x32x16_bf16 v[176:191], v[248:251], v[232:235], v[176:191]\n\t"
    "s_add_u32 m0, s72, 3072\n\t"
    "s_nop 0\n\t"
    "buffer_load_dwordx4 v99, %[xs], s67 offen lds\n\t"
    "v_mfma_f32_32x32x16_bf16 v[192:207], v[252:255], v[232:235], v[192:207]\n\t"
    "s_add_u32 m0, s73, 0\n\t"
    "s_nop 0\n\t"
    "buffer_load_dwordx4 v100, %[ws], s68 offen lds\n\t"
    "v_dot2c_f32_bf16 v116, s70, v232\n\t"
    "v_dot2c_f32_bf16 v117, v232, v232\n\t"
    "v_dot2c_f32_bf16 v116, s70, v233\n\t"
    "v_dot2c_f32_bf16 v117, v233, v233\n\t"
    "v_dot2c_f32_bf16 v116, s70, v234\n\t"
    "v_dot2c_f32_bf16 v117, v234, v234\n\t"
    "v_dot2c_f32_bf16 v116, s70, v235\n\t"
    "v_dot2c_f32_bf16 v117, v235, v235\n\t"
    "s_cmp_lt_u32 s64, 9\n\t"
    "s_cbranch_scc1 .Lp1b_%=\n\t"
    "s_waitcnt vmcnt(0) lgkmcnt(0)\n\t"
    "s_nop 15\n\t"
    "s_nop 3\n\t"
      P1_OPERANDS);
}

DEV void p1_phase(const Params& P, char* lds, unsigned ldsb, srd_t ws, int xcd, int i, int w, int lane, int& f1idx, float& mu_o, float& rs_o) {
  const int tid = threadIdx.x, c = lane & 31, hh = lane >> 5;
  const int tt = (xcd & 1) * 8 + (i & 7), ct = (i >> 3) < 3 ? 3 * (xcd >> 1) + (i >> 3) : 12 + (xcd >> 1), sec = ct >> 2, sub = ct & 3;
  f1idx = ct * 16 + tt;
  const int wstride = sec < 3 ? 3 * D : D;
  const srd_t wsr = sec < 3 ? mk_srd(P.wqkv, D * 3 * D * 2) : mk_srd(P.w1, D * D * 2);
  const unsigned wso = (sec < 3 ? sec * D + sub * 144 : sub * 144) * 2;
  const srd_t xs = mk_srd(P.x, S * D * 2);
  u32x4 xoff, bo, ao;
  u32x8 woff;
#pragma unroll
  for (int j = 0; j < 4; j++) {
    const int row = 32 * w + 8 * j + (lane >> 3); xoff[j] = row * (D * 2) + (((lane & 7) ^ ((row >> 1) & 7)) << 4);
    const int ks = (j + w) & 3;
    bo[j] = ldsb + (32 * w + c) * 128 + (((2 * ks + hh) ^ ((c >> 1) & 7)) << 4);
    ao[j] = ldsb + 16384 + (8 * hh + ((lane & 15) >> 2)) * 320 + (16 * ((lane >> 4) & 1) + 4 * (lane & 3)) * 2 + ks * 5120;
  }
#pragma unroll
  for (int j = 0; j < 8; j++) { const int q = 64 * (j < 5 ? j : 0) + lane, row = q / 20; int ch = q % 20; if (ch >= 18) ch -= 2; woff[j] = row * wstride * 2 + ch * 16; }
  f32x16 acc[5];
  f32x8 st = f32x8{};
  unsigned long long dbg[4];
  const unsigned wstep = 16 * wstride * 2;
  if (sec == 2) p1_main<true>(xs, tt * 128 * D * 2, wsr, wso + w * wstep, 4 * wstep, ldsb + w * 4096, ldsb + 16384 + w * 5120, xoff, woff, bo, ao, acc, st, dbg);
  else p1_main<false>(xs, tt * 128 * D * 2, wsr, wso + w * wstep, 4 * wstep, ldsb + w * 4096, ldsb + 16384 + w * 5120, xoff, woff, bo, ao, acc, st, dbg);
  float sum = st[0], sq = st[1];
  const float cs[5] = {st[2], st[3], st[4], st[5], st[6]};
  STAMP(1);
  sum = xhalf_sum(sum); sq = xhalf_sum(sq);
  float* csl = (float*)(lds + P1_AUX);
  float* stl = csl + 640 + w * 64;
#pragma unroll
  for (int k = 0; k < 5; k++) csl[w * 160 + 32 * k + c] = xhalf_sum(cs[k]);
  const float mu = sum * (1.f / D);
  const float var = fmaxf(sq * (1.f / D) - mu * mu, 0.f);
  const float mu_b = bfround(mu);
  const float sd_b = bfround(fsqrt(bfround(bfround(var) + 1e-5f)));
  const float rs_b = frcp(sd_b);
  mu_o = mu_b; rs_o = rs_b;
  const int tb = tt * 4 + w, tok = tb * 32 + c;
  BARRIER();
  if (tid < 160) csl[tid] = csl[tid] + csl[160 + tid] + csl[320 + tid] + csl[480 + tid];
  if (sec == 2) { stl[c] = mu_b * rs_b; stl[32 + c] = rs_b; }
  BARRIER();

  if (sec == 2) {
    f32x4 mr[4], rs[4];
#pragma unroll
    for (int g = 0; g < 4; g++) { mr[g] = *(const f32x4*)(stl + 8 * g + 4 * hh); rs[g] = *(const f32x4*)(stl + 32 + 8 * g + 4 * hh); }
    const unsigned so = WS_VF + (sub * 8 * 128 + 2 * tb) * 576;
#pragma unroll
    for (int k = 0; k < 5; k++) {
      const int n = 32 * k + c;
      const float ncs = -csl[n];
      unsigned p[8];
#pragma unroll
      for (int e = 0; e < 8; e++)
        p[e] = cvtpk(__builtin_fmaf(rs[e >> 1][2 * (e & 1)], acc[k][2 * e], mr[e >> 1][2 * (e & 1)] * ncs), __builtin_fmaf(rs[e >> 1][2 * (e & 1) + 1], acc[k][2 * e + 1], mr[e >> 1][2 * (e & 1) + 1] * ncs));
      const unsigned hl = (n * 57u) >> 10, d = n - 18 * hl;
      const unsigned vo = n < 144 ? hl * 73728 + hh * 288 + d * 16 : OOB;
      st128s(ws, vo, so, u32x4{p[0], p[1], p[2], p[3]});
      st128s(ws, vo + 576, so, u32x4{p[4], p[5], p[6], p[7]});
    }
  } else if (sec == 3) {
    const float rstd = __builtin_amdgcn_rsqf(var + 1e-5f * sd_b * sd_b), nmr = -mu * rstd;
    const int dl = sub == 0 ? -1 : sub == 1 ? 1 : sub == 2 ? -2 : 2;
    const int t = tok - dl, tw = t & (S - 1);
    const unsigned keep = t != tw ? 0u : 0xffffffffu;
    const unsigned vo = (tw >> 5) * 36864 + ((tw & 31) + 32 * hh) * 16;
#pragma unroll
    for (int ks = 0; ks < 9; ks++) {
      u32x4 v;
#pragma unroll
      for (int g2 = 0; g2 < 2; g2++) {
        const int k = ks >> 1, g = 2 * (ks & 1) + g2;
        const f32x4 c4 = *(const f32x4*)(csl + 32 * k + 8 * g + 4 * hh);
        float y[4];
#pragma unroll
        for (int e = 0; e < 4; e++) y[e] = gelu_f(__builtin_fmaf(acc[k][4 * g + e], rstd, nmr * c4[e]));
        v[2 * g2] = cvtpk(y[0], y[1]) & keep; v[2 * g2 + 1] = cvtpk(y[2], y[3]) & keep;
      }
      st128s(ws, vo + ks * 1024, WS_HS + 9 * sub * 1024, v);
    }
  } else {
    unsigned Pk[36];
    const float sc = sec == 0 ? rs_b * QSCALE : rs_b, nms = -mu_b * sc;
#pragma unroll
    for (int k = 0; k < 5; k++)
#pragma unroll
      for (int g = 0; g < (k == 4 ? 2 : 4); g++) {
        const f32x4 c4 = *(const f32x4*)(csl + 32 * k + 8 * g + 4 * hh);
        float y[4];
#pragma unroll
        for (int e = 0; e < 4; e++) y[e] = __builtin_fmaf(acc[k][4 * g + e], sc, nms * c4[e]);
        Pk[8 * k + 2 * g] = cvtpk(y[0], y[1]); Pk[8 * k + 2 * g + 1] = cvtpk(y[2], y[3]);
      }
    const unsigned fso = (sec == 0 ? WS_QF : WS_KF) + (sub * 8 * 64 + tb) * 1024, xso2 = (sec == 0 ? WS_QX : WS_KX) + sub * 8 * S * 4;
    const unsigned nso = WS_KN + sub * 8 * S * 4, vn = (sec == 1 && hh == 0) ? tok * 4 : OOB;
    const unsigned vx0 = hh == 0 ? tok * 4 : OOB, vx1 = hh == 1 ? tok * 4 : OOB;
    static_for<8>([&](auto HL) {
      constexpr int hl = HL;
      unsigned v[5];
      static_for<5>([&](auto PP) {
        constexpr int p = PP, d0 = head_dw(hl, 0, p), d1 = head_dw(hl, 1, p);
        const unsigned a0 = d0 >= 0 ? Pk[d0 >= 0 ? d0 : 0] : 0u, a1 = d1 >= 0 ? Pk[d1 >= 0 ? d1 : 0] : 0u;
        v[p] = hh ? a1 : a0;
      });
      st128s(ws, lane * 16, fso + hl * 65536, u32x4{v[0], v[1], v[2], v[3]});
      constexpr int xh = head_dw(hl, 0, 4) >= 0 ? 0 : 1;
      st32s(ws, xh == 0 ? vx0 : vx1, xso2 + hl * S * 4, v[4]);
      float nr = 0.f;
#pragma unroll
      for (int p = 0; p < 5; p++) nr = dot2(v[p], v[p], nr);
      st32s(ws, vn, nso + hl * S * 4, __float_as_uint(xhalf_sum(nr)));
    });
  }
}

constexpr int AT_K = 0, AT_KX = 65536, AT_V = 73728, AT_C = 147456, AT_RED = AT_C + 1280, AT_CSR = AT_RED + 64;

template <bool BIAS>
DEV void attn_slow(unsigned kp, unsigned kxp, unsigned vp, int vstride, const bf16x8 (&qm)[4], const bf16x8 (&qe)[4], const float (&nb)[4], f32x16 (&o)[4]) {
#pragma unroll 1
  for (int kt = 0; kt < 64; kt++) {
    const bf16x8 km = lds_b128(kp + kt * 1024);
    const unsigned kx = *(const LDSP(unsigned))(lds_cptr)(uintptr_t)(kxp + kt * 128);
    const bf16x8 ke = as_bf(u32x4{kx, 0u, 0u, 0u});
    const unsigned v = vp + kt * vstride;
    const bf16x8 v0 = lds_b128(v), v1 = lds_b128(v + 576);
#pragma unroll
    for (int q = 0; q < 4; q++) {
      f32x16 s;
#pragma unroll
      for (int r = 0; r < 16; r++) s[r] = nb[q];
      s = mfma(km, qm[q], s);
      s = mfma(ke, qe[q], s);
      unsigned p[8];
#pragma unroll
      for (int e = 0; e < 8; e++) p[e] = cvtpk(__builtin_amdgcn_exp2f(s[2 * e]), __builtin_amdgcn_exp2f(s[2 * e + 1]));
      o[q] = mfma(v0, as_bf(u32x4{p[0], p[1], p[2], p[3]}), o[q]);
      o[q] = mfma(v1, as_bf(u32x4{p[4], p[5], p[6], p[7]}), o[q]);
    }
  }
}

DEV void attn_fast(unsigned ka, unsigned kxa, unsigned va, unsigned vstride, const bf16x8 (&qm)[4], const bf16x8 (&qe)[4], f32x16 (&o)[4], u32x8 tv, srd_t w3s, srd_t w4s, srd_t wos, unsigned scr) {
  asm volatile(
    "ds_read_b128 v[176:179], %[ka]\n\t"
    "ds_read_b32 v184, %[kx]\n\t"
    "ds_read_b128 v[192:195], %[va]\n\t"
    "ds_read_b128 v[196:199], %[va] offset:576\n\t"
    "v_mov_b32 v185, 0\n\t"
    "v_mov_b32 v186, 0\n\t"
    "v_mov_b32 v187, 0\n\t"
    "v_mov_b32 v188, 0\n\t"
    "v_mov_b32 v189, 0\n\t"
    "v_mov_b32 v190, 0\n\t"
    "v_mov_b32 v191, 0\n\t"
    "v_mov_b32 v168, 0\n\t"
    "v_mov_b32 v169, 0\n\t"
    "v_mov_b32 v170, 0\n\t"
    "v_mov_b32 v171, 0\n\t"
    "v_mov_b32 v172, 0\n\t"
    "v_mov_b32 v173, 0\n\t"
    "v_mov_b32 v174, 0\n\t"
    "v_mov_b32 v175, 0\n\t"
    "v_mov_b32 v200, 0\n\t"
    "v_mov_b32 v201, 0\n\t"
    "v_mov_b32 v202, 0\n\t"
    "v_mov_b32 v203, 0\n\t"
    "v_mov_b32 v204, 0\n\t"
    "v_mov_b32 v205, 0\n\t"
    "v_mov_b32 v206, 0\n\t"
    "v_mov_b32 v207, 0\n\t"
    "v_mov_b32 v210, 0\n\t"
    "v_mov_b32 v211, 0\n\t"
    "s_waitcnt lgkmcnt(0)\n\t"
    "v_mfma_f32_32x32x16_bf16 v[128:143], v[176:179], %[qm0], 0\n\t"
    "v_mfma_f32_32x32x16_bf16 v[128:143], v[184:187], %[qe0], v[128:143]\n\t"
    "s_nop 15\n\t"
    "s_nop 7\n\t"
    "s_mov_b32 s64, 7\n\t"
    "s_mov_b32 s65, 0\n\t"
    ".Lat_%=:\n"
    "v_add_u32 v212, %[va], %[vs]\n\t"
    "v_mfma_f32_32x32x16_bf16 v[144:159], v[176:179], %[qm1], 0\n\t"
    "v_exp_f32 v160, v129\n\t"
    "v_exp_f32 v208, v128\n\t"
    "v_exp_f32 v161, v131\n\t"
    "v_exp_f32 v209, v130\n\t"
    "v_mov_b32_sdwa v174, v210 dst_sel:WORD_0 dst_unused:UNUSED_PRESERVE src0_sel:WORD_1\n\t"
    "v_mov_b32_sdwa v175, v211 dst_sel:WORD_0 dst_unused:UNUSED_PRESERVE src0_sel:WORD_1\n\t"
    "ds_read_b128 v[180:183], %[ka] offset:1024\n\t"
    "ds_read_b32 v188, %[kx] offset:128\n\t"
    "v_mfma_f32_32x32x16_bf16 v[144:159], v[184:187], %[qe1], v[144:159]\n\t"
    "v_exp_f32 v162, v133\n\t"
    "v_exp_f32 v210, v132\n\t"
    "v_exp_f32 v163, v135\n\t"
    "v_exp_f32 v211, v134\n\t"
    "v_mov_b32_sdwa v160, v208 dst_sel:WORD_0 dst_unused:UNUSED_PRESERVE src0_sel:WORD_1\n\t"
    "v_mov_b32_sdwa v161, v209 dst_sel:WORD_0 dst_unused:UNUSED_PRESERVE src0_sel:WORD_1\n\t"
    "v_mfma_f32_32x32x16_bf16 %[o3], v[200:203], v[168:171], %[o3]\n\t"
    "v_exp_f32 v164, v137\n\t"
    "v_exp_f32 v208, v136\n\t"
    "v_exp_f32 v165, v139\n\t"
    "v_exp_f32 v209, v138\n\t"
    "v_mov_b32_sdwa v162, v210 dst_sel:WORD_0 dst_unused:UNUSED_PRESERVE src0_sel:WORD_1\n\t"
    "v_mov_b32_sdwa v163, v211 dst_sel:WORD_0 dst_unused:UNUSED_PRESERVE src0_sel:WORD_1\n\t"
    "v_mfma_f32_32x32x16_bf16 %[o3], v[204:207], v[172:175], %[o3]\n\t"
    "v_exp_f32 v166, v141\n\t"
    "v_exp_f32 v210, v140\n\t"
    "v_exp_f32 v167, v143\n\t"
    "v_exp_f32 v211, v142\n\t"
    "v_mov_b32_sdwa v164, v208 dst_sel:WORD_0 dst_unused:UNUSED_PRESERVE src0_sel:WORD_1\n\t"
    "v_mov_b32_sdwa v165, v209 dst_sel:WORD_0 dst_unused:UNUSED_PRESERVE src0_sel:WORD_1\n\t"
    "v_mfma_f32_32x32x16_bf16 v[128:143], v[176:179], %[qm2], 0\n\t"
    "v_exp_f32 v168, v145\n\t"
    "v_exp_f32 v208, v144\n\t"
    "v_exp_f32 v169, v147\n\t"
    "v_exp_f32 v209, v146\n\t"
    "v_mov_b32_sdwa v166, v210 dst_sel:WORD_0 dst_unused:UNUSED_PRESERVE src0_sel:WORD_1\n\t"
    "v_mov_b32_sdwa v167, v211 dst_sel:WORD_0 dst_unused:UNUSED_PRESERVE src0_sel:WORD_1\n\t"
    "ds_read_b128 v[200:203], v212\n\t"
    "ds_read_b128 v[204:207], v212 offset:576\n\t"
    "v_mfma_f32_32x32x16_bf16 v[128:143], v[184:187], %[qe2], v[128:143]\n\t"
    "v_exp_f32 v170, v149\n\t"
    "v_exp_f32 v210, v148\n\t"
    "v_exp_f32 v171, v151\n\t"
    "v_exp_f32 v211, v150\n\t"
    "v_mov_b32_sdwa v168, v208 dst_sel:WORD_0 dst_unused:UNUSED_PRESERVE src0_sel:WORD_1\n\t"
    "v_mov_b32_sdwa v169, v209 dst_sel:WORD_0 dst_unused:UNUSED_PRESERVE src0_sel:WORD_1\n\t"
    "v_mfma_f32_32x32x16_bf16 %[o0], v[192:195], v[160:163], %[o0]\n\t"
    "v_exp_f32 v172, v153\n\t"
    "v_exp_f32 v208, v152\n\t"
    "v_exp_f32 v173, v155\n\t"
    "v_exp_f32 v209, v154\n\t"
    "v_mov_b32_sdwa v170, v210 dst_sel:WORD_0 dst_unused:UNUSED_PRESERVE src0_sel:WORD_1\n\t"
    "v_mov_b32_sdwa v171, v211 dst_sel:WORD_0 dst_unused:UNUSED_PRESERVE src0_sel:WORD_1\n\t"
    "v_mfma_f32_32x32x16_bf16 %[o0], v[196:199], v[164:167], %[o0]\n\t"
    "v_exp_f32 v174, v157\n\t"
    "v_exp_f32 v210, v156\n\t"
    "v_exp_f32 v175, v159\n\t"
    "v_exp_f32 v211, v158\n\t"
    "v_mov_b32_sdwa v172, v208 dst_sel:WORD_0 dst_unused:UNUSED_PRESERVE src0_sel:WORD_1\n\t"
    "v_mov_b32_sdwa v173, v209 dst_sel:WORD_0 dst_unused:UNUSED_PRESERVE src0_sel:WORD_1\n\t"
    "v_mfma_f32_32x32x16_bf16 v[144:159], v[176:179], %[qm3], 0\n\t"
    "v_exp_f32 v160, v129\n\t"
    "v_exp_f32 v208, v128\n\t"
    "v_exp_f32 v161, v131\n\t"
    "v_exp_f32 v209, v130\n\t"
    "v_mov_b32_sdwa v174, v210 dst_sel:WORD_0 dst_unused:UNUSED_PRESERVE src0_sel:WORD_1\n\t"
    "v_mov_b32_sdwa v175, v211 dst_sel:WORD_0 dst_unused:UNUSED_PRESERVE src0_sel:WORD_1\n\t"
    "v_add_u32 %[va], v212, %[vs]\n\t"
    "v_mfma_f32_32x32x16_bf16 v[144:159], v[184:187], %[qe3], v[144:159]\n\t"
    "v_exp_f32 v162, v133\n\t"
    "v_exp_f32 v210, v132\n\t"
    "v_exp_f32 v163, v135\n\t"
    "v_exp_f32 v211, v134\n\t"
    "v_mov_b32_sdwa v160, v208 dst_sel:WORD_0 dst_unused:UNUSED_PRESERVE src0_sel:WORD_1\n\t"
    "v_mov_b32_sdwa v161, v209 dst_sel:WORD_0 dst_unused:UNUSED_PRESERVE src0_sel:WORD_1\n\t"
    "v_mfma_f32_32x32x16_bf16 %[o1], v[192:195], v[168:171], %[o1]\n\t"
    "v_exp_f32 v164, v137\n\t"
    "v_exp_f32 v208, v136\n\t"
    "v_exp_f32 v165, v139\n\t"
    "v_exp_f32 v209, v138\n\t"
    "v_mov_b32_sdwa v162, v210 dst_sel:WORD_0 dst_unused:UNUSED_PRESERVE src0_sel:WORD_1\n\t"
    "v_mov_b32_sdwa v163, v211 dst_sel:WORD_0 dst_unused:UNUSED_PRESERVE src0_sel:WORD_1\n\t"
    "v_mfma_f32_32x32x16_bf16 %[o1], v[196:199], v[172:175], %[o1]\n\t"
    "v_exp_f32 v166, v141\n\t"
    "v_exp_f32 v210, v140\n\t"
    "v_exp_f32 v167, v143\n\t"
    "v_exp_f32 v211, v142\n\t"
    "v_mov_b32_sdwa v164, v208 dst_sel:WORD_0 dst_unused:UNUSED_PRESERVE src0_sel:WORD_1\n\t"
    "v_mov_b32_sdwa v165, v209 dst_sel:WORD_0 dst_unused:UNUSED_PRESERVE src0_sel:WORD_1\n\t"
    "s_waitcnt lgkmcnt(0)\n\t"
    "v_mfma_f32_32x32x16_bf16 v[128:143], v[180:183], %[qm0], 0\n\t"
    "v_exp_f32 v168, v145\n\t"
    "v_exp_f32 v208, v144\n\t"
    "v_exp_f32 v169, v147\n\t"
    "v_exp_f32 v209, v146\n\t"
    "v_mov_b32_sdwa v166, v210 dst_sel:WORD_0 dst_unused:UNUSED_PRESERVE src0_sel:WORD_1\n\t"
    "v_mov_b32_sdwa v167, v211 dst_sel:WORD_0 dst_unused:UNUSED_PRESERVE src0_sel:WORD_1\n\t"
    "v_mfma_f32_32x32x16_bf16 v[128:143], v[188:191], %[qe0], v[128:143]\n\t"
    "v_exp_f32 v170, v149\n\t"
    "v_exp_f32 v210, v148\n\t"
    "v_exp_f32 v171, v151\n\t"
    "v_exp_f32 v211, v150\n\t"
    "v_mov_b32_sdwa v168, v208 dst_sel:WORD_0 dst_unused:UNUSED_PRESERVE src0_sel:WORD_1\n\t"
    "v_mov_b32_sdwa v169, v209 dst_sel:WORD_0 dst_unused:UNUSED_PRESERVE src0_sel:WORD_1\n\t"
    "v_mfma_f32_32x32x16_bf16 %[o2], v[192:195], v[160:163], %[o2]\n\t"
    "v_exp_f32 v172, v153\n\t"
    "v_exp_f32 v208, v152\n\t"
    "v_exp_f32 v173, v155\n\t"
    "v_exp_f32 v209, v154\n\t"
    "v_mov_b32_sdwa v170, v210 dst_sel:WORD_0 dst_unused:UNUSED_PRESERVE src0_sel:WORD_1\n\t"
    "v_mov_b32_sdwa v171, v211 dst_sel:WORD_0 dst_unused:UNUSED_PRESERVE src0_sel:WORD_1\n\t"
    "v_mfma_f32_32x32x16_bf16 %[o2], v[196:199], v[164:167], %[o2]\n\t"
    "v_exp_f32 v174, v157\n\t"
    "v_exp_f32 v210, v156\n\t"
    "v_exp_f32 v175, v159\n\t"
    "v_exp_f32 v211, v158\n\t"
    "v_mov_b32_sdwa v172, v208 dst_sel:WORD_0 dst_unused:UNUSED_PRESERVE src0_sel:WORD_1\n\t"
    "v_mov_b32_sdwa v173, v209 dst_sel:WORD_0 dst_unused:UNUSED_PRESERVE src0_sel:WORD_1\n\t"
    "v_mfma_f32_32x32x16_bf16 v[144:159], v[180:183], %[qm1], 0\n\t"
    "v_exp_f32 v160, v129\n\t"
    "v_exp_f32 v208, v128\n\t"
    "v_exp_f32 v161, v131\n\t"
    "v_exp_f32 v209, v130\n\t"
    "v_mov_b32_sdwa v174, v210 dst_sel:WORD_0 dst_unused:UNUSED_PRESERVE src0_sel:WORD_1\n\t"
    "v_mov_b32_sdwa v175, v211 dst_sel:WORD_0 dst_unused:UNUSED_PRESERVE src0_sel:WORD_1\n\t"
    "ds_read_b128 v[176:179], %[ka] offset:2048\n\t"
    "ds_read_b32 v184, %[kx] offset:256\n\t"
    "v_mfma_f32_32x32x16_bf16 v[144:159], v[188:191], %[qe1], v[144:159]\n\t"
    "v_exp_f32 v162, v133\n\t"
    "v_exp_f32 v210, v132\n\t"
    "v_exp_f32 v163, v135\n\t"
    "v_exp_f32 v211, v134\n\t"
    "v_mov_b32_sdwa v160, v208 dst_sel:WORD_0 dst_unused:UNUSED_PRESERVE src0_sel:WORD_1\n\t"
    "v_mov_b32_sdwa v161, v209 dst_sel:WORD_0 dst_unused:UNUSED_PRESERVE src0_sel:WORD_1\n\t"
    "v_mfma_f32_32x32x16_bf16 %[o3], v[192:195], v[168:171], %[o3]\n\t"
    "v_exp_f32 v164, v137\n\t"
    "v_exp_f32 v208, v136\n\t"
    "v_exp_f32 v165, v139\n\t"
    "v_exp_f32 v209, v138\n\t"
    "v_mov_b32_sdwa v162, v210 dst_sel:WORD_0 dst_unused:UNUSED_PRESERVE src0_sel:WORD_1\n\t"
    "v_mov_b32_sdwa v163, v211 dst_sel:WORD_0 dst_unused:UNUSED_PRESERVE src0_sel:WORD_1\n\t"
    "v_mfma_f32_32x32x16_bf16 %[o3], v[196:199], v[172:175], %[o3]\n\t"
    "v_exp_f32 v166, v141\n\t"
    "v_exp_f32 v210, v140\n\t"
    "v_exp_f32 v167, v143\n\t"
    "v_exp_f32 v211, v142\n\t"
    "v_mov_b32_sdwa v164, v208 dst_sel:WORD_0 dst_unused:UNUSED_PRESERVE src0_sel:WORD_1\n\t"
    "v_mov_b32_sdwa v165, v209 dst_sel:WORD_0 dst_unused:UNUSED_PRESERVE src0_sel:WORD_1\n\t"
    "v_mfma_f32_32x32x16_bf16 v[128:143], v[180:183], %[qm2], 0\n\t"
    "v_exp_f32 v168, v145\n\t"
    "v_exp_f32 v208, v144\n\t"
    "v_exp_f32 v169, v147\n\t"
    "v_exp_f32 v209, v146\n\t"
    "v_mov_b32_sdwa v166, v210 dst_sel:WORD_0 dst_unused:UNUSED_PRESERVE src0_sel:WORD_1\n\t"
    "v_mov_b32_sdwa v167, v211 dst_sel:WORD_0 dst_unused:UNUSED_PRESERVE src0_sel:WORD_1\n\t"
    "ds_read_b128 v[192:195], %[va]\n\t"
    "ds_read_b128 v[196:199], %[va] offset:576\n\t"
    "v_mfma_f32_32x32x16_bf16 v[128:143], v[188:191], %[qe2], v[128:143]\n\t"
    "v_exp_f32 v170, v149\n\t"
    "v_exp_f32 v210, v148\n\t"
    "v_exp_f32 v171, v151\n\t"
    "v_exp_f32 v211, v150\n\t"
    "v_mov_b32_sdwa v168, v208 dst_sel:WORD_0 dst_unused:UNUSED_PRESERVE src0_sel:WORD_1\n\t"
    "v_mov_b32_sdwa v169, v209 dst_sel:WORD_0 dst_unused:UNUSED_PRESERVE src0_sel:WORD_1\n\t"
    "v_mfma_f32_32x32x16_bf16 %[o0], v[200:203], v[160:163], %[o0]\n\t"
    "v_exp_f32 v172, v153\n\t"
    "v_exp_f32 v208, v152\n\t"
    "v_exp_f32 v173, v155\n\t"
    "v_exp_f32 v209, v154\n\t"
    "v_mov_b32_sdwa v170, v210 dst_sel:WORD_0 dst_unused:UNUSED_PRESERVE src0_sel:WORD_1\n\t"
    "v_mov_b32_sdwa v171, v211 dst_sel:WORD_0 dst_unused:UNUSED_PRESERVE src0_sel:WORD_1\n\t"
    "v_mfma_f32_32x32x16_bf16 %[o0], v[204:207], v[164:167], %[o0]\n\t"
    "v_exp_f32 v174, v157\n\t"
    "v_exp_f32 v210, v156\n\t"
    "v_exp_f32 v175, v159\n\t"
    "v_exp_f32 v211, v158\n\t"
    "v_mov_b32_sdwa v172, v208 dst_sel:WORD_0 dst_unused:UNUSED_PRESERVE src0_sel:WORD_1\n\t"
    "v_mov_b32_sdwa v173, v209 dst_sel:WORD_0 dst_unused:UNUSED_PRESERVE src0_sel:WORD_1\n\t"
    "v_mfma_f32_32x32x16_bf16 v[144:159], v[180:183], %[qm3], 0\n\t"
    "v_exp_f32 v160, v129\n\t"
    "v_exp_f32 v208, v128\n\t"
    "v_exp_f32 v161, v131\n\t"
    "v_exp_f32 v209, v130\n\t"
    "v_mov_b32_sdwa v174, v210 dst_sel:WORD_0 dst_unused:UNUSED_PRESERVE src0_sel:WORD_1\n\t"
    "v_mov_b32_sdwa v175, v211 dst_sel:WORD_0 dst_unused:UNUSED_PRESERVE src0_sel:WORD_1\n\t"
    "v_add_u32 %[ka], 0x800, %[ka]\n\t"
    "v_mfma_f32_32x32x16_bf16 v[144:159], v[188:191], %[qe3], v[144:159]\n\t"
    "v_exp_f32 v162, v133\n\t"
    "v_exp_f32 v210, v132\n\t"
    "v_exp_f32 v163, v135\n\t"
    "v_exp_f32 v211, v134\n\t"
    "v_mov_b32_sdwa v160, v208 dst_sel:WORD_0 dst_unused:UNUSED_PRESERVE src0_sel:WORD_1\n\t"
    "v_mov_b32_sdwa v161, v209 dst_sel:WORD_0 dst_unused:UNUSED_PRESERVE src0_sel:WORD_1\n\t"
    "v_mfma_f32_32x32x16_bf16 %[o1], v[200:203], v[168:171], %[o1]\n\t"
    "v_exp_f32 v164, v137\n\t"
    "v_exp_f32 v208, v136\n\t"
    "v_exp_f32 v165, v139\n\t"
    "v_exp_f32 v209, v138\n\t"
    "v_mov_b32_sdwa v162, v210 dst_sel:WORD_0 dst_unused:UNUSED_PRESERVE src0_sel:WORD_1\n\t"
    "v_mov_b32_sdwa v163, v211 dst_sel:WORD_0 dst_unused:UNUSED_PRESERVE src0_sel:WORD_1\n\t"
    "v_add_u32 %[kx], 0x100, %[kx]\n\t"
    "v_mfma_f32_32x32x16_bf16 %[o1], v[204:207], v[172:175], %[o1]\n\t"
    "v_exp_f32 v166, v141\n\t"
    "v_exp_f32 v210, v140\n\t"
    "v_exp_f32 v167, v143\n\t"
    "v_exp_f32 v211, v142\n\t"
    "v_mov_b32_sdwa v164, v208 dst_sel:WORD_0 dst_unused:UNUSED_PRESERVE src0_sel:WORD_1\n\t"
    "v_mov_b32_sdwa v165, v209 dst_sel:WORD_0 dst_unused:UNUSED_PRESERVE src0_sel:WORD_1\n\t"
    "s_waitcnt lgkmcnt(0)\n\t"
    "v_mfma_f32_32x32x16_bf16 v[128:143], v[176:179], %[qm0], 0\n\t"
    "v_exp_f32 v168, v145\n\t"
    "v_exp_f32 v208, v144\n\t"
    "v_exp_f32 v169, v147\n\t"
    "v_exp_f32 v209, v146\n\t"
    "v_mov_b32_sdwa v166, v210 dst_sel:WORD_0 dst_unused:UNUSED_PRESERVE src0_sel:WORD_1\n\t"
    "v_mov_b32_sdwa v167, v211 dst_sel:WORD_0 dst_unused:UNUSED_PRESERVE src0_sel:WORD_1\n\t"
    "s_sub_u32 s64, s64, 1\n\t"
    "v_mfma_f32_32x32x16_bf16 v[128:143], v[184:187], %[qe0], v[128:143]\n\t"
    "v_exp_f32 v170, v149\n\t"
    "v_exp_f32 v210, v148\n\t"
    "v_exp_f32 v171, v151\n\t"
    "v_exp_f32 v211, v150\n\t"
    "v_mov_b32_sdwa v168, v208 dst_sel:WORD_0 dst_unused:UNUSED_PRESERVE src0_sel:WORD_1\n\t"
    "v_mov_b32_sdwa v169, v209 dst_sel:WORD_0 dst_unused:UNUSED_PRESERVE src0_sel:WORD_1\n\t"
    "v_mfma_f32_32x32x16_bf16 %[o2], v[200:203], v[160:163], %[o2]\n\t"
    "v_exp_f32 v172, v153\n\t"
    "v_exp_f32 v208, v152\n\t"
    "v_exp_f32 v173, v155\n\t"
    "v_exp_f32 v209, v154\n\t"
    "v_mov_b32_sdwa v170, v210 dst_sel:WORD_0 dst_unused:UNUSED_PRESERVE src0_sel:WORD_1\n\t"
    "v_mov_b32_sdwa v171, v211 dst_sel:WORD_0 dst_unused:UNUSED_PRESERVE src0_sel:WORD_1\n\t"
    "s_cmp_lg_u32 s64, 0\n\t"
    "v_mfma_f32_32x32x16_bf16 %[o2], v[204:207], v[164:167], %[o2]\n\t"
    "v_exp_f32 v174, v157\n\t"
    "v_exp_f32 v210, v156\n\t"
    "v_exp_f32 v175, v159\n\t"
    "v_exp_f32 v211, v158\n\t"
    "v_mov_b32_sdwa v172, v208 dst_sel:WORD_0 dst_unused:UNUSED_PRESERVE src0_sel:WORD_1\n\t"
    "v_mov_b32_sdwa v173, v209 dst_sel:WORD_0 dst_unused:UNUSED_PRESERVE src0_sel:WORD_1\n\t"
    "s_cbranch_scc1 .Lat_%=\n\t"
    "s_add_u32 s65, s65, 1\n\t"
    "s_mov_b32 s64, 8\n\t"
    "s_cmp_eq_u32 s65, 1\n\t"
    "s_cbranch_scc0 .Lat2_%=\n\t"
    "s_waitcnt vmcnt(18)\n\t"
    "s_barrier\n\t"
    "s_mov_b32 m0, %[scr]\n\t"
    "s_nop 0\n\t"
    "buffer_load_dword v216, %[w3s], 0 offen lds\n\t"
    "buffer_load_dword v217, %[w3s], 0 offen lds\n\t"
    "buffer_load_dword v218, %[w3s], 0 offen lds\n\t"
    "s_branch .Lat_%=\n\t"
    ".Lat2_%=:\n"
    "s_cmp_eq_u32 s65, 2\n\t"
    "s_cbranch_scc0 .Lat3_%=\n\t"
    "s_waitcnt vmcnt(12)\n\t"
    "s_barrier\n\t"
    "s_mov_b32 m0, %[scr]\n\t"
    "s_nop 0\n\t"
    "buffer_load_dword v219, %[w4s], 0 offen lds\n\t"
    "buffer_load_dword v220, %[w4s], 0 offen lds\n\t"
    "buffer_load_dword v221, %[w4s], 0 offen lds\n\t"
    "s_branch .Lat_%=\n\t"
    ".Lat3_%=:\n"
    "s_cmp_eq_u32 s65, 3\n\t"
    "s_cbranch_scc0 .Lat4_%=\n\t"
    "s_waitcnt vmcnt(6)\n\t"
    "s_barrier\n\t"
    "s_mov_b32 m0, %[scr]\n\t"
    "s_nop 0\n\t"
    "buffer_load_dword v222, %[wos], 0 offen lds\n\t"
    "buffer_load_dword v223, %[wos], 0 offen lds\n\t"
    "s_mov_b32 s64, 9\n\t"
    "s_branch .Lat_%=\n\t"
    ".Lat4_%=:\n"
    "v_mov_b32_sdwa v174, v210 dst_sel:WORD_0 dst_unused:UNUSED_PRESERVE src0_sel:WORD_1\n\t"
    "v_mov_b32_sdwa v175, v211 dst_sel:WORD_0 dst_unused:UNUSED_PRESERVE src0_sel:WORD_1\n\t"
    "s_nop 1\n\t"
    "v_mfma_f32_32x32x16_bf16 %[o3], v[200:203], v[168:171], %[o3]\n\t"
    "v_mfma_f32_32x32x16_bf16 %[o3], v[204:207], v[172:175], %[o3]\n\t"
    "s_nop 15\n\t"
    "s_nop 7\n\t"
      : [o0] "+a"(o[0]), [o1] "+a"(o[1]), [o2] "+a"(o[2]), [o3] "+a"(o[3]), [ka] "+v"(ka), [kx] "+v"(kxa), [va] "+v"(va)
      : [qm0] "a"(qm[0]), [qm1] "a"(qm[1]), [qm2] "a"(qm[2]), [qm3] "a"(qm[3]), [qe0] "a"(qe[0]), [qe1] "a"(qe[1]), [qe2] "a"(qe[2]), [qe3] "a"(qe[3]), [vs] "v"(vstride),
        "{v[216:223]}"(tv), [w3s] "s"(w3s), [w4s] "s"(w4s), [wos] "s"(wos), [scr] "s"(scr)
      : "memory", "scc", "m0", "s64", "s65", "v128", "v129", "v130", "v131", "v132", "v133", "v134", "v135", "v136", "v137", "v138", "v139", "v140", "v141", "v142", "v143", "v144", "v145", "v146", "v147", "v148", "v149", "v150", "v151", "v152", "v153", "v154", "v155", "v156", "v157", "v158", "v159", "v160", "v161", "v162", "v163", "v164", "v165", "v166", "v167", "v168", "v169", "v170", "v171", "v172", "v173", "v174", "v175", "v176", "v177", "v178", "v179", "v180", "v181", "v182", "v183", "v184", "v185", "v186", "v187", "v188", "v189", "v190", "v191", "v192", "v193", "v194", "v195", "v196", "v197", "v198", "v199", "v200", "v201", "v202", "v203", "v204", "v205", "v206", "v207", "v208", "v209", "v210", "v211", "v212");
}

DEV void attn_phase(const Params& P, char* lds, unsigned ldsb, srd_t ws, unsigned tag, int head, int qr, int w) {
  const int lane = lane_id(), c = lane & 31, hh = lane >> 5;
  unsigned* flags = (unsigned*)(P.ws + WS_FLAG);
  const int tid = w * 64 + lane, cs8 = head >> 2, csp = (head & 3) * 4 + qr;
  unsigned cw[42];
  {
    const srd_t w3s = mk_srd(P.w3, D * FF * 2);
    const unsigned rg = (tid * 57u) >> 10, d = tid - 18 * rg;
    const unsigned vo = tid < 252 ? (rg * FF + cs8 * 576 + csp * 36 + 2 * d) * 2u : OOB;
    static_for<42>([&](auto K) { ld32_raw(cw[K], w3s, vo, K * 14 * FF * 2); });
  }
  await_wg(ws, ldsb, FL_P1 + ((lane >> 4) * 4 + (head >> 3)) * 16 + (lane & 15), lane < 48, tag, w);
  STAMP(3);
  u32x4 qraw[4], kn0, kn1; unsigned qx[4];
  const unsigned voff16 = lane * 16;
  {
    const unsigned qs = WS_QF + (head * 64 + qr * 16 + w * 4) * 1024, xs = WS_QX + (head * S + (qr * 16 + w * 4) * 32) * 4, ns = WS_KN + head * 8192 + w * 1024;
    asm volatile("buffer_load_dwordx4 %0, %10, %12, %13 offen sc1\n\tbuffer_load_dwordx4 %1, %10, %12, %13 offen offset:1024 sc1\n\t"
                 "buffer_load_dwordx4 %2, %10, %12, %13 offen offset:2048 sc1\n\tbuffer_load_dwordx4 %3, %10, %12, %13 offen offset:3072 sc1\n\t"
                 "buffer_load_dword %4, %11, %12, %14 offen sc1\n\tbuffer_load_dword %5, %11, %12, %14 offen offset:128 sc1\n\t"
                 "buffer_load_dword %6, %11, %12, %14 offen offset:256 sc1\n\tbuffer_load_dword %7, %11, %12, %14 offen offset:384 sc1\n\t"
                 "buffer_load_dwordx4 %8, %10, %12, %15 offen sc1\n\tbuffer_load_dwordx4 %9, %10, %12, %16 offen sc1"
                 : "=&v"(qraw[0]), "=&v"(qraw[1]), "=&v"(qraw[2]), "=&v"(qraw[3]), "=&v"(qx[0]), "=&v"(qx[1]), "=&v"(qx[2]), "=&v"(qx[3]), "=&v"(kn0), "=&v"(kn1)
                 : "v"(voff16), "v"(c * 4), "s"(ws), "s"(qs), "s"(xs), "s"(ns), "s"(ns + 4096));
  }
  auto kv_chunk = [&](int ch) {
    const unsigned kf = WS_KF + head * 65536 + w * 4096, vf = WS_VF + head * 73728 + w * 5120, kx = WS_KX + head * 8192;
#pragma unroll
    for (int j = 0; j < 4; j++) dma_dev(voff16, ws, kf + ch * 16384 + j * 1024, ldsb + AT_K + ch * 16384 + w * 4096 + j * 1024);
#pragma unroll
    for (int j = 0; j < 5; j++) {
      const bool isv = 5 * w + j < 18;
      dma_dev(voff16, ws, isv ? vf + ch * 18432 + j * 1024 : kx + ch * 2048 + (j - 3) * 1024, isv ? ldsb + AT_V + ch * 18432 + w * 5120 + j * 1024 : ldsb + AT_KX + ch * 2048 + (j - 3) * 1024);
    }
  };
  kv_chunk(0);
  u32x8 tv;
  {
    const unsigned g4 = head >> 3, a64 = ((head & 3) * 4 + qr) * 4 + w;
#pragma unroll
    for (int k = 0; k < 3; k++) {
      const unsigned l = 64 * k + lane, L = 162 * a64 + l, row = (L * 3641u) >> 16, col = L - 18 * row;
      tv[k] = l < 162 ? row * (FF * 2) + g4 * 2304 + col * 128 : OOB;
      tv[3 + k] = l < 162 ? g4 * 1327104 + L * 128 : OOB;
    }
    tv[6] = (81 * a64 + lane) * 128; tv[7] = lane < 17 ? (81 * a64 + 64 + lane) * 128 : OOB;
  }
  if (w == 0 && lane < 8) { const unsigned v = (lane & 4) ? 0u : 0x3f803f80u; *(unsigned*)(lds + AT_C + lane * 4) = v; *(unsigned*)(lds + AT_C + 576 + lane * 4) = v; }
  {
    WAIT_VM(19);
    static_for<42>([&](auto I) { pin_v(cw[I]); });
    unsigned sel_lo = 0x00003f80u, sel_hi = 0x3f800000u;
    pin_v(sel_lo); pin_v(sel_hi);
    float lo = 0.f, hi = 0.f;
#pragma unroll
    for (int k = 0; k < 42; k++) { lo = dot2(cw[k], sel_lo, lo); hi = dot2(cw[k], sel_hi, hi); }
    float* red = (float*)(lds + AT_CSR + 256);
    if (tid < 252) *(f32x2*)(red + 2 * tid) = f32x2{lo, hi};
  }
  asm volatile("s_waitcnt vmcnt(9)" : "+v"(qraw[0]), "+v"(qraw[1]), "+v"(qraw[2]), "+v"(qraw[3]), "+v"(qx[0]), "+v"(qx[1]), "+v"(qx[2]), "+v"(qx[3]), "+v"(kn0), "+v"(kn1));
  const int xh = (head >> 1) & 1;
  bf16x8 qm[4], qe[4];
  unsigned qn = 0u, kn = 0u;
#pragma unroll
  for (int q = 0; q < 4; q++) {
    const unsigned e = hh != xh ? 0u : qx[q];
    qm[q] = as_bf(qraw[q]); qe[q] = as_bf(u32x4{e, 0u, 0u, 0u});
    float n2 = dot2(e, e, 0.f);
#pragma unroll
    for (int i = 0; i < 4; i++) n2 = dot2(qraw[q][i], qraw[q][i], n2);
    qn = max(qn, __float_as_uint(xhalf_sum(n2)) & 0x7fffffffu);
    kn = max(kn, max(kn0[q] & 0x7fffffffu, kn1[q] & 0x7fffffffu));
  }
  kn = wave_umax(kn); qn = wave_umax(qn);
  unsigned* red = (unsigned*)(lds + AT_RED);
  if (lane == 0) { red[w] = kn; red[4 + w] = qn; }
  const int vstride = c < 18 ? 1152 : 0;
  const unsigned ka = ldsb + AT_K + lane * 16, kxa = ldsb + AT_KX + c * 4;
  const unsigned va = ldsb + (c < 18 ? AT_V + hh * 288 + c * 16 : c == 18 ? AT_C : AT_C + 16);
  f32x16 o[4];
#pragma unroll
  for (int q = 0; q < 4; q++) o[q] = f32x16{};
  WAIT_VM(0); BARRIER();
#pragma unroll 1
  for (int ch = 1; ch < 4; ch++) kv_chunk(ch);
  if (tid < 36) {
    const float* red = (const float*)(lds + AT_CSR + 256);
    float t = 0.f;
#pragma unroll
    for (int g = 0; g < 14; g++) t += red[g * 36 + tid];
    __hip_atomic_store((unsigned long long*)(P.ws + WS_CS3) + cs8 * 576 + csp * 36 + tid, ((unsigned long long)tag << 32) | __float_as_uint(t), __ATOMIC_RELAXED, __HIP_MEMORY_SCOPE_AGENT);
  }
  const u32x4 r0 = *(const u32x4*)red, r1 = *(const u32x4*)(red + 4);
  const float kmax2 = __uint_as_float(max(max(r0[0], r0[1]), max(r0[2], r0[3]))), qmax2 = __uint_as_float(max(max(r1[0], r1[1]), max(r1[2], r1[3])));
  const bool safe = __builtin_amdgcn_readfirstlane(qmax2 * kmax2 * 1.02f <= 3600.f);
  STAMP(4);
  if (safe) attn_fast(ka, kxa, va, vstride, qm, qe, o, tv, mk_srd(P.w3, D * FF * 2), mk_srd(P.w4, FF * D * 2), mk_srd(P.wo, D * D * 2), ldsb + AT_CSR);
  else { WAIT_VM(0); BARRIER(); }
  bool redo = !safe;
  if (safe) {
    float t = 0.f;
#pragma unroll
    for (int q = 0; q < 4; q++)
#pragma unroll
      for (int r = 0; r < 11; r++) t += fabsf(o[q][r]);
    redo = __any(!(t < 3.0e38f));
  }
  if (redo) {
    float mx[4] = {-3.0e38f, -3.0e38f, -3.0e38f, -3.0e38f};
#pragma unroll 1
    for (int kt = 0; kt < 64; kt++) {
      const bf16x8 km = lds_b128(ka + kt * 1024);
      const unsigned kxv = *(const LDSP(unsigned))(lds_cptr)(uintptr_t)(kxa + kt * 128);
      const bf16x8 ke = as_bf(u32x4{kxv, 0u, 0u, 0u});
#pragma unroll
      for (int q = 0; q < 4; q++) {
        f32x16 s = mfma(km, qm[q], f32x16{});
        s = mfma(ke, qe[q], s);
#pragma unroll
        for (int r = 0; r < 16; r++) mx[q] = fmaxf(mx[q], s[r]);
      }
    }
    float nb[4];
#pragma unroll
    for (int q = 0; q < 4; q++) { nb[q] = -xhalf_max(mx[q]); o[q] = f32x16{}; }
    attn_slow<true>(ka, kxa, va, vstride, qm, qe, nb, o);
  }
  STAMP(5);
  {
    const unsigned dst = WS_OF + (qr * 16 + w * 4) * 36864, vx = hh == 0 ? (c + 32 * ((head >> 2) & 1)) * 16 + (head & 3) * 4 : OOB;
#pragma unroll
    for (int q = 0; q < 4; q++) {
      const float inv = frcp(lo_bcast(o[q][10]));
      unsigned p[5];
#pragma unroll
      for (int e = 0; e < 5; e++) p[e] = cvtpk(o[q][2 * e] * inv, o[q][2 * e + 1] * inv);
      st128s(ws, voff16, dst + q * 36864 + head * 1024, u32x4{p[0], p[1], p[2], p[3]});
      st32s(ws, vx, dst + q * 36864 + (32 + (head >> 3)) * 1024, p[4]);
    }
  }
}

constexpr int ST_B = 36864;
DEV unsigned stage_voff(int lane, int rowbytes) { return (lane >> 3) * rowbytes + (((lane & 7) ^ ((lane >> 2) & 4)) << 4); }
DEV void stage_bases(unsigned ldsb, int lane, unsigned (&ab)[2]) {
  const int hh = lane >> 5, blk = (lane >> 4) & 1, q = (lane & 15) >> 2, p = lane & 3;
#pragma unroll
  for (int b = 0; b < 2; b++) ab[b] = ldsb + (4 * hh + q) * 128 + ((b ^ (q >> 1)) << 6) + 32 * blk + 8 * p;
}
struct XRegs { f32x16 t[9]; };
#define X_IN(x) "{a[0:15]}"(x.t[0]), "{a[16:31]}"(x.t[1]), "{a[32:47]}"(x.t[2]), "{a[48:63]}"(x.t[3]), "{a[64:79]}"(x.t[4]), "{a[80:95]}"(x.t[5]), "{a[96:111]}"(x.t[6]), "{a[112:127]}"(x.t[7]), "{a[128:143]}"(x.t[8])
#define X_OUT(x) "={a[0:15]}"(x.t[0]), "={a[16:31]}"(x.t[1]), "={a[32:47]}"(x.t[2]), "={a[48:63]}"(x.t[3]), "={a[64:79]}"(x.t[4]), "={a[80:95]}"(x.t[5]), "={a[96:111]}"(x.t[6]), "={a[112:127]}"(x.t[7]), "={a[128:143]}"(x.t[8])
#define X_IO(x) "+{a[0:15]}"(x.t[0]), "+{a[16:31]}"(x.t[1]), "+{a[32:47]}"(x.t[2]), "+{a[48:63]}"(x.t[3]), "+{a[64:79]}"(x.t[4]), "+{a[80:95]}"(x.t[5]), "+{a[96:111]}"(x.t[6]), "+{a[112:127]}"(x.t[7]), "+{a[128:143]}"(x.t[8])
DEV void mm01_load(XRegs& x, srd_t ws, unsigned voff16, unsigned off, unsigned ab0, unsigned ab1, f32x16& acc0, f32x16& acc1) {
  asm volatile(
    "s_mov_b32 s64, %[off]\n\t"
    "buffer_load_dwordx4 a[0:3], %[vo], %[ws], s64 offen offset:0 sc1\n\t"
    "buffer_load_dwordx4 a[4:7], %[vo], %[ws], s64 offen offset:1024 sc1\n\t"
    "buffer_load_dwordx4 a[8:11], %[vo], %[ws], s64 offen offset:2048 sc1\n\t"
    "buffer_load_dwordx4 a[12:15], %[vo], %[ws], s64 offen offset:3072 sc1\n\t"
    "s_add_u32 s64, s64, 0x1000\n\t"
    "buffer_load_dwordx4 a[16:19], %[vo], %[ws], s64 offen offset:0 sc1\n\t"
    "buffer_load_dwordx4 a[20:23], %[vo], %[ws], s64 offen offset:1024 sc1\n\t"
    "buffer_load_dwordx4 a[24:27], %[vo], %[ws], s64 offen offset:2048 sc1\n\t"
    "buffer_load_dwordx4 a[28:31], %[vo], %[ws], s64 offen offset:3072 sc1\n\t"
    "s_add_u32 s64, s64, 0x1000\n\t"
    "buffer_load_dwordx4 a[32:35], %[vo], %[ws], s64 offen offset:0 sc1\n\t"
    "buffer_load_dwordx4 a[36:39], %[vo], %[ws], s64 offen offset:1024 sc1\n\t"
    "buffer_load_dwordx4 a[40:43], %[vo], %[ws], s64 offen offset:2048 sc1\n\t"
    "buffer_load_dwordx4 a[44:47], %[vo], %[ws], s64 offen offset:3072 sc1\n\t"
    "s_add_u32 s64, s64, 0x1000\n\t"
    "buffer_load_dwordx4 a[48:51], %[vo], %[ws], s64 offen offset:0 sc1\n\t"
    "buffer_load_dwordx4 a[52:55], %[vo], %[ws], s64 offen offset:1024 sc1\n\t"
    "buffer_load_dwordx4 a[56:59], %[vo], %[ws], s64 offen offset:2048 sc1\n\t"
    "buffer_load_dwordx4 a[60:63], %[vo], %[ws], s64 offen offset:3072 sc1\n\t"
    "s_add_u32 s64, s64, 0x1000\n\t"
    "buffer_load_dwordx4 a[64:67], %[vo], %[ws], s64 offen offset:0 sc1\n\t"
    "buffer_load_dwordx4 a[68:71], %[vo], %[ws], s64 offen offset:1024 sc1\n\t"
    "buffer_load_dwordx4 a[72:75], %[vo], %[ws], s64 offen offset:2048 sc1\n\t"
    "buffer_load_dwordx4 a[76:79], %[vo], %[ws], s64 offen offset:3072 sc1\n\t"
    "s_add_u32 s64, s64, 0x1000\n\t"
    "buffer_load_dwordx4 a[80:83], %[vo], %[ws], s64 offen offset:0 sc1\n\t"
    "buffer_load_dwordx4 a[84:87], %[vo], %[ws], s64 offen offset:1024 sc1\n\t"
    "buffer_load_dwordx4 a[88:91], %[vo], %[ws], s64 offen offset:2048 sc1\n\t"
    "buffer_load_dwordx4 a[92:95], %[vo], %[ws], s64 offen offset:3072 sc1\n\t"
    "s_add_u32 s64, s64, 0x1000\n\t"
    "buffer_load_dwordx4 a[96:99], %[vo], %[ws], s64 offen offset:0 sc1\n\t"
    "buffer_load_dwordx4 a[100:103], %[vo], %[ws], s64 offen offset:1024 sc1\n\t"
    "buffer_load_dwordx4 a[104:107], %[vo], %[ws], s64 offen offset:2048 sc1\n\t"
    "buffer_load_dwordx4 a[108:111], %[vo], %[ws], s64 offen offset:3072 sc1\n\t"
    "s_add_u32 s64, s64, 0x1000\n\t"
    "buffer_load_dwordx4 a[112:115], %[vo], %[ws], s64 offen offset:0 sc1\n\t"
    "buffer_load_dwordx4 a[116:119], %[vo], %[ws], s64 offen offset:1024 sc1\n\t"
    "buffer_load_dwordx4 a[120:123], %[vo], %[ws], s64 offen offset:2048 sc1\n\t"
    "buffer_load_dwordx4 a[124:127], %[vo], %[ws], s64 offen offset:3072 sc1\n\t"
    "s_add_u32 s64, s64, 0x1000\n\t"
    "buffer_load_dwordx4 a[128:131], %[vo], %[ws], s64 offen offset:0 sc1\n\t"
    "buffer_load_dwordx4 a[132:135], %[vo], %[ws], s64 offen offset:1024 sc1\n\t"
    "buffer_load_dwordx4 a[136:139], %[vo], %[ws], s64 offen offset:2048 sc1\n\t"
    "buffer_load_dwordx4 a[140:143], %[vo], %[ws], s64 offen offset:3072 sc1\n\t"
    "s_waitcnt vmcnt(36)\n\t"
    "s_barrier\n\t"
    "v_add_u32 v22, 0x9000, %[ab0]\n\t"
    "v_add_u32 v23, 0x9000, %[ab1]\n\t"
    "ds_read_b64_tr_b16 v[24:25], %[ab0] offset:0\n\t"
    "ds_read_b64_tr_b16 v[26:27], %[ab0] offset:1024\n\t"
    "ds_read_b64_tr_b16 v[28:29], %[ab1] offset:0\n\t"
    "ds_read_b64_tr_b16 v[30:31], %[ab1] offset:1024\n\t"
    "ds_read_b64_tr_b16 v[32:33], %[ab0] offset:2048\n\t"
    "ds_read_b64_tr_b16 v[34:35], %[ab0] offset:3072\n\t"
    "ds_read_b64_tr_b16 v[36:37], %[ab1] offset:2048\n\t"
    "ds_read_b64_tr_b16 v[38:39], %[ab1] offset:3072\n\t"
    "ds_read_b64_tr_b16 v[40:41], %[ab0] offset:4096\n\t"
    "ds_read_b64_tr_b16 v[42:43], %[ab0] offset:5120\n\t"
    "ds_read_b64_tr_b16 v[44:45], %[ab1] offset:4096\n\t"
    "ds_read_b64_tr_b16 v[46:47], %[ab1] offset:5120\n\t"
    "ds_read_b64_tr_b16 v[48:49], %[ab0] offset:6144\n\t"
    "ds_read_b64_tr_b16 v[50:51], %[ab0] offset:7168\n\t"
    "ds_read_b64_tr_b16 v[52:53], %[ab1] offset:6144\n\t"
    "ds_read_b64_tr_b16 v[54:55], %[ab1] offset:7168\n\t"
    "s_waitcnt vmcnt(35) lgkmcnt(12)\n\t"
    "v_mfma_f32_32x32x16_bf16 v[56:71], v[24:27], a[0:3], 0\n\t"
    "v_mfma_f32_32x32x16_bf16 v[72:87], v[28:31], a[0:3], 0\n\t"
    "ds_read_b64_tr_b16 v[24:25], %[ab0] offset:8192\n\t"
    "ds_read_b64_tr_b16 v[26:27], %[ab0] offset:9216\n\t"
    "ds_read_b64_tr_b16 v[28:29], %[ab1] offset:8192\n\t"
    "ds_read_b64_tr_b16 v[30:31], %[ab1] offset:9216\n\t"
    "s_waitcnt vmcnt(34) lgkmcnt(12)\n\t"
    "v_mfma_f32_32x32x16_bf16 v[56:71], v[32:35], a[4:7], v[56:71]\n\t"
    "v_mfma_f32_32x32x16_bf16 v[72:87], v[36:39], a[4:7], v[72:87]\n\t"
    "ds_read_b64_tr_b16 v[32:33], %[ab0] offset:10240\n\t"
    "ds_read_b64_tr_b16 v[34:35], %[ab0] offset:11264\n\t"
    "ds_read_b64_tr_b16 v[36:37], %[ab1] offset:10240\n\t"
    "ds_read_b64_tr_b16 v[38:39], %[ab1] offset:11264\n\t"
    "s_waitcnt vmcnt(33) lgkmcnt(12)\n\t"
    "v_mfma_f32_32x32x16_bf16 v[56:71], v[40:43], a[8:11], v[56:71]\n\t"
    "v_mfma_f32_32x32x16_bf16 v[72:87], v[44:47], a[8:11], v[72:87]\n\t"
    "ds_read_b64_tr_b16 v[40:41], %[ab0] offset:12288\n\t"
    "ds_read_b64_tr_b16 v[42:43], %[ab0] offset:13312\n\t"
    "ds_read_b64_tr_b16 v[44:45], %[ab1] offset:12288\n\t"
    "ds_read_b64_tr_b16 v[46:47], %[ab1] offset:13312\n\t"
    "s_waitcnt vmcnt(32) lgkmcnt(12)\n\t"
    "v_mfma_f32_32x32x16_bf16 v[56:71], v[48:51], a[12:15], v[56:71]\n\t"
    "v_mfma_f32_32x32x16_bf16 v[72:87], v[52:55], a[12:15], v[72:87]\n\t"
    "ds_read_b64_tr_b16 v[48:49], %[ab0] offset:14336\n\t"
    "ds_read_b64_tr_b16 v[50:51], %[ab0] offset:15360\n\t"
    "ds_read_b64_tr_b16 v[52:53], %[ab1] offset:14336\n\t"
    "ds_read_b64_tr_b16 v[54:55], %[ab1] offset:15360\n\t"
    "s_waitcnt vmcnt(31) lgkmcnt(12)\n\t"
    "v_mfma_f32_32x32x16_bf16 v[56:71], v[24:27], a[16:19], v[56:71]\n\t"
    "v_mfma_f32_32x32x16_bf16 v[72:87], v[28:31], a[16:19], v[72:87]\n\t"
    "ds_read_b64_tr_b16 v[24:25], %[ab0] offset:16384\n\t"
    "ds_read_b64_tr_b16 v[26:27], %[ab0] offset:17408\n\t"
    "ds_read_b64_tr_b16 v[28:29], %[ab1] offset:16384\n\t"
    "ds_read_b64_tr_b16 v[30:31], %[ab1] offset:17408\n\t"
    "s_waitcnt vmcnt(30) lgkmcnt(12)\n\t"
    "v_mfma_f32_32x32x16_bf16 v[56:71], v[32:35], a[20:23], v[56:71]\n\t"
    "v_mfma_f32_32x32x16_bf16 v[72:87], v[36:39], a[20:23], v[72:87]\n\t"
    "ds_read_b64_tr_b16 v[32:33], %[ab0] offset:18432\n\t"
    "ds_read_b64_tr_b16 v[34:35], %[ab0] offset:19456\n\t"
    "ds_read_b64_tr_b16 v[36:37], %[ab1] offset:18432\n\t"
    "ds_read_b64_tr_b16 v[38:39], %[ab1] offset:19456\n\t"
    "s_waitcnt vmcnt(29) lgkmcnt(12)\n\t"
    "v_mfma_f32_32x32x16_bf16 v[56:71], v[40:43], a[24:27], v[56:71]\n\t"
    "v_mfma_f32_32x32x16_bf16 v[72:87], v[44:47], a[24:27], v[72:87]\n\t"
    "ds_read_b64_tr_b16 v[40:41], %[ab0] offset:20480\n\t"
    "ds_read_b64_tr_b16 v[42:43], %[ab0] offset:21504\n\t"
    "ds_read_b64_tr_b16 v[44:45], %[ab1] offset:20480\n\t"
    "ds_read_b64_tr_b16 v[46:47], %[ab1] offset:21504\n\t"
    "s_waitcnt vmcnt(28) lgkmcnt(12)\n\t"
    "v_mfma_f32_32x32x16_bf16 v[56:71], v[48:51], a[28:31], v[56:71]\n\t"
    "v_mfma_f32_32x32x16_bf16 v[72:87], v[52:55], a[28:31], v[72:87]\n\t"
    "ds_read_b64_tr_b16 v[48:49], %[ab0] offset:22528\n\t"
    "ds_read_b64_tr_b16 v[50:51], %[ab0] offset:23552\n\t"
    "ds_read_b64_tr_b16 v[52:53], %[ab1] offset:22528\n\t"
    "ds_read_b64_tr_b16 v[54:55], %[ab1] offset:23552\n\t"
    "s_waitcnt vmcnt(27) lgkmcnt(12)\n\t"
    "v_mfma_f32_32x32x16_bf16 v[56:71], v[24:27], a[32:35], v[56:71]\n\t"
    "v_mfma_f32_32x32x16_bf16 v[72:87], v[28:31], a[32:35], v[72:87]\n\t"
    "ds_read_b64_tr_b16 v[24:25], %[ab0] offset:24576\n\t"
    "ds_read_b64_tr_b16 v[26:27], %[ab0] offset:25600\n\t"
    "ds_read_b64_tr_b16 v[28:29], %[ab1] offset:24576\n\t"
    "ds_read_b64_tr_b16 v[30:31], %[ab1] offset:25600\n\t"
    "s_waitcnt vmcnt(26) lgkmcnt(12)\n\t"
    "v_mfma_f32_32x32x16_bf16 v[56:71], v[32:35], a[36:39], v[56:71]\n\t"
    "v_mfma_f32_32x32x16_bf16 v[72:87], v[36:39], a[36:39], v[72:87]\n\t"
    "ds_read_b64_tr_b16 v[32:33], %[ab0] offset:26624\n\t"
    "ds_read_b64_tr_b16 v[34:35], %[ab0] offset:27648\n\t"
    "ds_read_b64_tr_b16 v[36:37], %[ab1] offset:26624\n\t"
    "ds_read_b64_tr_b16 v[38:39], %[ab1] offset:27648\n\t"
    "s_waitcnt vmcnt(25) lgkmcnt(12)\n\t"
    "v_mfma_f32_32x32x16_bf16 v[56:71], v[40:43], a[40:43], v[56:71]\n\t"
    "v_mfma_f32_32x32x16_bf16 v[72:87], v[44:47], a[40:43], v[72:87]\n\t"
    "ds_read_b64_tr_b16 v[40:41], %[ab0] offset:28672\n\t"
    "ds_read_b64_tr_b16 v[42:43], %[ab0] offset:29696\n\t"
    "ds_read_b64_tr_b16 v[44:45], %[ab1] offset:28672\n\t"
    "ds_read_b64_tr_b16 v[46:47], %[ab1] offset:29696\n\t"
    "s_waitcnt vmcnt(24) lgkmcnt(12)\n\t"
    "v_mfma_f32_32x32x16_bf16 v[56:71], v[48:51], a[44:47], v[56:71]\n\t"
    "v_mfma_f32_32x32x16_bf16 v[72:87], v[52:55], a[44:47], v[72:87]\n\t"
    "ds_read_b64_tr_b16 v[48:49], %[ab0] offset:30720\n\t"
    "ds_read_b64_tr_b16 v[50:51], %[ab0] offset:31744\n\t"
    "ds_read_b64_tr_b16 v[52:53], %[ab1] offset:30720\n\t"
    "ds_read_b64_tr_b16 v[54:55], %[ab1] offset:31744\n\t"
    "s_waitcnt vmcnt(23) lgkmcnt(12)\n\t"
    "v_mfma_f32_32x32x16_bf16 v[56:71], v[24:27], a[48:51], v[56:71]\n\t"
    "v_mfma_f32_32x32x16_bf16 v[72:87], v[28:31], a[48:51], v[72:87]\n\t"
    "ds_read_b64_tr_b16 v[24:25], %[ab0] offset:32768\n\t"
    "ds_read_b64_tr_b16 v[26:27], %[ab0] offset:33792\n\t"
    "ds_read_b64_tr_b16 v[28:29], %[ab1] offset:32768\n\t"
    "ds_read_b64_tr_b16 v[30:31], %[ab1] offset:33792\n\t"
    "s_waitcnt vmcnt(22) lgkmcnt(12)\n\t"
    "v_mfma_f32_32x32x16_bf16 v[56:71], v[32:35], a[52:55], v[56:71]\n\t"
    "v_mfma_f32_32x32x16_bf16 v[72:87], v[36:39], a[52:55], v[72:87]\n\t"
    "ds_read_b64_tr_b16 v[32:33], %[ab0] offset:34816\n\t"
    "ds_read_b64_tr_b16 v[34:35], %[ab0] offset:35840\n\t"
    "ds_read_b64_tr_b16 v[36:37], %[ab1] offset:34816\n\t"
    "ds_read_b64_tr_b16 v[38:39], %[ab1] offset:35840\n\t"
    "s_waitcnt vmcnt(21) lgkmcnt(12)\n\t"
    "v_mfma_f32_32x32x16_bf16 v[56:71], v[40:43], a[56:59], v[56:71]\n\t"
    "v_mfma_f32_32x32x16_bf16 v[72:87], v[44:47], a[56:59], v[72:87]\n\t"
    "ds_read_b64_tr_b16 v[40:41], v22 offset:0\n\t"
    "ds_read_b64_tr_b16 v[42:43], v22 offset:1024\n\t"
    "ds_read_b64_tr_b16 v[44:45], v23 offset:0\n\t"
    "ds_read_b64_tr_b16 v[46:47], v23 offset:1024\n\t"
    "s_waitcnt vmcnt(20) lgkmcnt(12)\n\t"
    "v_mfma_f32_32x32x16_bf16 v[56:71], v[48:51], a[60:63], v[56:71]\n\t"
    "v_mfma_f32_32x32x16_bf16 v[72:87], v[52:55], a[60:63], v[72:87]\n\t"
    "ds_read_b64_tr_b16 v[48:49], v22 offset:2048\n\t"
    "ds_read_b64_tr_b16 v[50:51], v22 offset:3072\n\t"
    "ds_read_b64_tr_b16 v[52:53], v23 offset:2048\n\t"
    "ds_read_b64_tr_b16 v[54:55], v23 offset:3072\n\t"
    "s_waitcnt vmcnt(19) lgkmcnt(12)\n\t"
    "v_mfma_f32_32x32x16_bf16 v[56:71], v[24:27], a[64:67], v[56:71]\n\t"
    "v_mfma_f32_32x32x16_bf16 v[72:87], v[28:31], a[64:67], v[72:87]\n\t"
    "ds_read_b64_tr_b16 v[24:25], v22 offset:4096\n\t"
    "ds_read_b64_tr_b16 v[26:27], v22 offset:5120\n\t"
    "ds_read_b64_tr_b16 v[28:29], v23 offset:4096\n\t"
    "ds_read_b64_tr_b16 v[30:31], v23 offset:5120\n\t"
    "s_waitcnt vmcnt(18) lgkmcnt(12)\n\t"
    "v_mfma_f32_32x32x16_bf16 v[56:71], v[32:35], a[68:71], v[56:71]\n\t"
    "v_mfma_f32_32x32x16_bf16 v[72:87], v[36:39], a[68:71], v[72:87]\n\t"
    "ds_read_b64_tr_b16 v[32:33], v22 offset:6144\n\t"
    "ds_read_b64_tr_b16 v[34:35], v22 offset:7168\n\t"
    "ds_read_b64_tr_b16 v[36:37], v23 offset:6144\n\t"
    "ds_read_b64_tr_b16 v[38:39], v23 offset:7168\n\t"
    "s_waitcnt vmcnt(17) lgkmcnt(12)\n\t"
    "v_mfma_f32_32x32x16_bf16 v[56:71], v[40:43], a[72:75], v[56:71]\n\t"
    "v_mfma_f32_32x32x16_bf16 v[72:87], v[44:47], a[72:75], v[72:87]\n\t"
    "ds_read_b64_tr_b16 v[40:41], v22 offset:8192\n\t"
    "ds_read_b64_tr_b16 v[42:43], v22 offset:9216\n\t"
    "ds_read_b64_tr_b16 v[44:45], v23 offset:8192\n\t"
    "ds_read_b64_tr_b16 v[46:47], v23 offset:9216\n\t"
    "s_waitcnt vmcnt(16) lgkmcnt(12)\n\t"
    "v_mfma_f32_32x32x16_bf16 v[56:71], v[48:51], a[76:79], v[56:71]\n\t"
    "v_mfma_f32_32x32x16_bf16 v[72:87], v[52:55], a[76:79], v[72:87]\n\t"
    "ds_read_b64_tr_b16 v[48:49], v22 offset:10240\n\t"
    "ds_read_b64_tr_b16 v[50:51], v22 offset:11264\n\t"
    "ds_read_b64_tr_b16 v[52:53], v23 offset:10240\n\t"
    "ds_read_b64_tr_b16 v[54:55], v23 offset:11264\n\t"
    "s_waitcnt vmcnt(15) lgkmcnt(12)\n\t"
    "v_mfma_f32_32x32x16_bf16 v[56:71], v[24:27], a[80:83], v[56:71]\n\t"
    "v_mfma_f32_32x32x16_bf16 v[72:87], v[28:31], a[80:83], v[72:87]\n\t"
    "ds_read_b64_tr_b16 v[24:25], v22 offset:12288\n\t"
    "ds_read_b64_tr_b16 v[26:27], v22 offset:13312\n\t"
    "ds_read_b64_tr_b16 v[28:29], v23 offset:12288\n\t"
    "ds_read_b64_tr_b16 v[30:31], v23 offset:13312\n\t"
    "s_waitcnt vmcnt(14) lgkmcnt(12)\n\t"
    "v_mfma_f32_32x32x16_bf16 v[56:71], v[32:35], a[84:87], v[56:71]\n\t"
    "v_mfma_f32_32x32x16_bf16 v[72:87], v[36:39], a[84:87], v[72:87]\n\t"
    "ds_read_b64_tr_b16 v[32:33], v22 offset:14336\n\t"
    "ds_read_b64_tr_b16 v[34:35], v22 offset:15360\n\t"
    "ds_read_b64_tr_b16 v[36:37], v23 offset:14336\n\t"
    "ds_read_b64_tr_b16 v[38:39], v23 offset:15360\n\t"
    "s_waitcnt vmcnt(13) lgkmcnt(12)\n\t"
    "v_mfma_f32_32x32x16_bf16 v[56:71], v[40:43], a[88:91], v[56:71]\n\t"
    "v_mfma_f32_32x32x16_bf16 v[72:87], v[44:47], a[88:91], v[72:87]\n\t"
    "ds_read_b64_tr_b16 v[40:41], v22 offset:16384\n\t"
    "ds_read_b64_tr_b16 v[42:43], v22 offset:17408\n\t"
    "ds_read_b64_tr_b16 v[44:45], v23 offset:16384\n\t"
    "ds_read_b64_tr_b16 v[46:47], v23 offset:17408\n\t"
    "s_waitcnt vmcnt(12) lgkmcnt(12)\n\t"
    "v_mfma_f32_32x32x16_bf16 v[56:71], v[48:51], a[92:95], v[56:71]\n\t"
    "v_mfma_f32_32x32x16_bf16 v[72:87], v[52:55], a[92:95], v[72:87]\n\t"
    "ds_read_b64_tr_b16 v[48:49], v22 offset:18432\n\t"
    "ds_read_b64_tr_b16 v[50:51], v22 offset:19456\n\t"
    "ds_read_b64_tr_b16 v[52:53], v23 offset:18432\n\t"
    "ds_read_b64_tr_b16 v[54:55], v23 offset:19456\n\t"
    "s_waitcnt vmcnt(11) lgkmcnt(12)\n\t"
    "v_mfma_f32_32x32x16_bf16 v[56:71], v[24:27], a[96:99], v[56:71]\n\t"
    "v_mfma_f32_32x32x16_bf16 v[72:87], v[28:31], a[96:99], v[72:87]\n\t"
    "ds_read_b64_tr_b16 v[24:25], v22 offset:20480\n\t"
    "ds_read_b64_tr_b16 v[26:27], v22 offset:21504\n\t"
    "ds_read_b64_tr_b16 v[28:29], v23 offset:20480\n\t"
    "ds_read_b64_tr_b16 v[30:31], v23 offset:21504\n\t"
    "s_waitcnt vmcnt(10) lgkmcnt(12)\n\t"
    "v_mfma_f32_32x32x16_bf16 v[56:71], v[32:35], a[100:103], v[56:71]\n\t"
    "v_mfma_f32_32x32x16_bf16 v[72:87], v[36:39], a[100:103], v[72:87]\n\t"
    "ds_read_b64_tr_b16 v[32:33], v22 offset:22528\n\t"
    "ds_read_b64_tr_b16 v[34:35], v22 offset:23552\n\t"
    "ds_read_b64_tr_b16 v[36:37], v23 offset:22528\n\t"
    "ds_read_b64_tr_b16 v[38:39], v23 offset:23552\n\t"
    "s_waitcnt vmcnt(9) lgkmcnt(12)\n\t"
    "v_mfma_f32_32x32x16_bf16 v[56:71], v[40:43], a[104:107], v[56:71]\n\t"
    "v_mfma_f32_32x32x16_bf16 v[72:87], v[44:47], a[104:107], v[72:87]\n\t"
    "ds_read_b64_tr_b16 v[40:41], v22 offset:24576\n\t"
    "ds_read_b64_tr_b16 v[42:43], v22 offset:25600\n\t"
    "ds_read_b64_tr_b16 v[44:45], v23 offset:24576\n\t"
    "ds_read_b64_tr_b16 v[46:47], v23 offset:25600\n\t"
    "s_waitcnt vmcnt(8) lgkmcnt(12)\n\t"
    "v_mfma_f32_32x32x16_bf16 v[56:71], v[48:51], a[108:111], v[56:71]\n\t"
    "v_mfma_f32_32x32x16_bf16 v[72:87], v[52:55], a[108:111], v[72:87]\n\t"
    "ds_read_b64_tr_b16 v[48:49], v22 offset:26624\n\t"
    "ds_read_b64_tr_b16 v[50:51], v22 offset:27648\n\t"
    "ds_read_b64_tr_b16 v[52:53], v23 offset:26624\n\t"
    "ds_read_b64_tr_b16 v[54:55], v23 offset:27648\n\t"
    "s_waitcnt vmcnt(7) lgkmcnt(12)\n\t"
    "v_mfma_f32_32x32x16_bf16 v[56:71], v[24:27], a[112:115], v[56:71]\n\t"
    "v_mfma_f32_32x32x16_bf16 v[72:87], v[28:31], a[112:115], v[72:87]\n\t"
    "ds_read_b64_tr_b16 v[24:25], v22 offset:28672\n\t"
    "ds_read_b64_tr_b16 v[26:27], v22 offset:29696\n\t"
    "ds_read_b64_tr_b16 v[28:29], v23 offset:28672\n\t"
    "ds_read_b64_tr_b16 v[30:31], v23 offset:29696\n\t"
    "s_waitcnt vmcnt(6) lgkmcnt(12)\n\t"
    "v_mfma_f32_32x32x16_bf16 v[56:71], v[32:35], a[116:119], v[56:71]\n\t"
    "v_mfma_f32_32x32x16_bf16 v[72:87], v[36:39], a[116:119], v[72:87]\n\t"
    "ds_read_b64_tr_b16 v[32:33], v22 offset:30720\n\t"
    "ds_read_b64_tr_b16 v[34:35], v22 offset:31744\n\t"
    "ds_read_b64_tr_b16 v[36:37], v23 offset:30720\n\t"
    "ds_read_b64_tr_b16 v[38:39], v23 offset:31744\n\t"
    "s_waitcnt vmcnt(5) lgkmcnt(12)\n\t"
    "v_mfma_f32_32x32x16_bf16 v[56:71], v[40:43], a[120:123], v[56:71]\n\t"
    "v_mfma_f32_32x32x16_bf16 v[72:87], v[44:47], a[120:123], v[72:87]\n\t"
    "ds_read_b64_tr_b16 v[40:41], v22 offset:32768\n\t"
    "ds_read_b64_tr_b16 v[42:43], v22 offset:33792\n\t"
    "ds_read_b64_tr_b16 v[44:45], v23 offset:32768\n\t"
    "ds_read_b64_tr_b16 v[46:47], v23 offset:33792\n\t"
    "s_waitcnt vmcnt(4) lgkmcnt(12)\n\t"
    "v_mfma_f32_32x32x16_bf16 v[56:71], v[48:51], a[124:127], v[56:71]\n\t"
    "v_mfma_f32_32x32x16_bf16 v[72:87], v[52:55], a[124:127], v[72:87]\n\t"
    "ds_read_b64_tr_b16 v[48:49], v22 offset:34816\n\t"
    "ds_read_b64_tr_b16 v[50:51], v22 offset:35840\n\t"
    "ds_read_b64_tr_b16 v[52:53], v23 offset:34816\n\t"
    "ds_read_b64_tr_b16 v[54:55], v23 offset:35840\n\t"
    "s_waitcnt vmcnt(3) lgkmcnt(12)\n\t"
    "v_mfma_f32_32x32x16_bf16 v[56:71], v[24:27], a[128:131], v[56:71]\n\t"
    "v_mfma_f32_32x32x16_bf16 v[72:87], v[28:31], a[128:131], v[72:87]\n\t"
    "s_waitcnt vmcnt(2) lgkmcnt(8)\n\t"
    "v_mfma_f32_32x32x16_bf16 v[56:71], v[32:35], a[132:135], v[56:71]\n\t"
    "v_mfma_f32_32x32x16_bf16 v[72:87], v[36:39], a[132:135], v[72:87]\n\t"
    "s_waitcnt vmcnt(1) lgkmcnt(4)\n\t"
    "v_mfma_f32_32x32x16_bf16 v[56:71], v[40:43], a[136:139], v[56:71]\n\t"
    "v_mfma_f32_32x32x16_bf16 v[72:87], v[44:47], a[136:139], v[72:87]\n\t"
    "s_waitcnt vmcnt(0) lgkmcnt(0)\n\t"
    "v_mfma_f32_32x32x16_bf16 v[56:71], v[48:51], a[140:143], v[56:71]\n\t"
    "v_mfma_f32_32x32x16_bf16 v[72:87], v[52:55], a[140:143], v[72:87]\n\t"
    "s_nop 15\n\t"
    "s_nop 3\n\t"
      : "={v[56:71]}"(acc0), "={v[72:87]}"(acc1), X_OUT(x) : [ab0] "v"(ab0), [ab1] "v"(ab1), [vo] "v"(voff16), [ws] "s"(ws), [off] "s"(off) : "memory", "scc", "s64", "v22", "v23", "v24", "v25", "v26", "v27", "v28", "v29", "v30", "v31", "v32", "v33", "v34", "v35", "v36", "v37", "v38", "v39", "v40", "v41", "v42", "v43", "v44", "v45", "v46", "v47", "v48", "v49", "v50", "v51", "v52", "v53", "v54", "v55");
}
DEV void mm01c_load(XRegs& x, srd_t ws, unsigned voff16, unsigned off, unsigned ab0, unsigned ab1, f32x16& acc0, f32x16& acc1) {
  asm volatile(
    "s_mov_b32 s64, %[off]\n\t"
    "buffer_load_dwordx4 a[0:3], %[vo], %[ws], s64 offen offset:0 sc1\n\t"
    "buffer_load_dwordx4 a[4:7], %[vo], %[ws], s64 offen offset:1024 sc1\n\t"
    "buffer_load_dwordx4 a[8:11], %[vo], %[ws], s64 offen offset:2048 sc1\n\t"
    "buffer_load_dwordx4 a[12:15], %[vo], %[ws], s64 offen offset:3072 sc1\n\t"
    "s_add_u32 s64, s64, 0x1000\n\t"
    "buffer_load_dwordx4 a[16:19], %[vo], %[ws], s64 offen offset:0 sc1\n\t"
    "buffer_load_dwordx4 a[20:23], %[vo], %[ws], s64 offen offset:1024 sc1\n\t"
    "buffer_load_dwordx4 a[24:27], %[vo], %[ws], s64 offen offset:2048 sc1\n\t"
    "buffer_load_dwordx4 a[28:31], %[vo], %[ws], s64 offen offset:3072 sc1\n\t"
    "s_add_u32 s64, s64, 0x1000\n\t"
    "buffer_load_dwordx4 a[32:35], %[vo], %[ws], s64 offen offset:0 sc1\n\t"
    "buffer_load_dwordx4 a[36:39], %[vo], %[ws], s64 offen offset:1024 sc1\n\t"
    "buffer_load_dwordx4 a[40:43], %[vo], %[ws], s64 offen offset:2048 sc1\n\t"
    "buffer_load_dwordx4 a[44:47], %[vo], %[ws], s64 offen offset:3072 sc1\n\t"
    "s_add_u32 s64, s64, 0x1000\n\t"
    "buffer_load_dwordx4 a[48:51], %[vo], %[ws], s64 offen offset:0 sc1\n\t"
    "buffer_load_dwordx4 a[52:55], %[vo], %[ws], s64 offen offset:1024 sc1\n\t"
    "buffer_load_dwordx4 a[56:59], %[vo], %[ws], s64 offen offset:2048 sc1\n\t"
    "buffer_load_dwordx4 a[60:63], %[vo], %[ws], s64 offen offset:3072 sc1\n\t"
    "s_add_u32 s64, s64, 0x1000\n\t"
    "buffer_load_dwordx4 a[64:67], %[vo], %[ws], s64 offen offset:0 sc1\n\t"
    "buffer_load_dwordx4 a[68:71], %[vo], %[ws], s64 offen offset:1024 sc1\n\t"
    "buffer_load_dwordx4 a[72:75], %[vo], %[ws], s64 offen offset:2048 sc1\n\t"
    "buffer_load_dwordx4 a[76:79], %[vo], %[ws], s64 offen offset:3072 sc1\n\t"
    "s_add_u32 s64, s64, 0x1000\n\t"
    "buffer_load_dwordx4 a[80:83], %[vo], %[ws], s64 offen offset:0 sc1\n\t"
    "buffer_load_dwordx4 a[84:87], %[vo], %[ws], s64 offen offset:1024 sc1\n\t"
    "buffer_load_dwordx4 a[88:91], %[vo], %[ws], s64 offen offset:2048 sc1\n\t"
    "buffer_load_dwordx4 a[92:95], %[vo], %[ws], s64 offen offset:3072 sc1\n\t"
    "s_add_u32 s64, s64, 0x1000\n\t"
    "buffer_load_dwordx4 a[96:99], %[vo], %[ws], s64 offen offset:0 sc1\n\t"
    "buffer_load_dwordx4 a[100:103], %[vo], %[ws], s64 offen offset:1024 sc1\n\t"
    "buffer_load_dwordx4 a[104:107], %[vo], %[ws], s64 offen offset:2048 sc1\n\t"
    "buffer_load_dwordx4 a[108:111], %[vo], %[ws], s64 offen offset:3072 sc1\n\t"
    "s_add_u32 s64, s64, 0x1000\n\t"
    "buffer_load_dwordx4 a[112:115], %[vo], %[ws], s64 offen offset:0 sc1\n\t"
    "buffer_load_dwordx4 a[116:119], %[vo], %[ws], s64 offen offset:1024 sc1\n\t"
    "buffer_load_dwordx4 a[120:123], %[vo], %[ws], s64 offen offset:2048 sc1\n\t"
    "buffer_load_dwordx4 a[124:127], %[vo], %[ws], s64 offen offset:3072 sc1\n\t"
    "s_add_u32 s64, s64, 0x1000\n\t"
    "buffer_load_dwordx4 a[128:131], %[vo], %[ws], s64 offen offset:0 sc1\n\t"
    "buffer_load_dwordx4 a[132:135], %[vo], %[ws], s64 offen offset:1024 sc1\n\t"
    "buffer_load_dwordx4 a[136:139], %[vo], %[ws], s64 offen offset:2048 sc1\n\t"
    "buffer_load_dwordx4 a[140:143], %[vo], %[ws], s64 offen offset:3072 sc1\n\t"
    "s_waitcnt vmcnt(36)\n\t"
    "s_barrier\n\t"
    "v_add_u32 v22, 0x9000, %[ab0]\n\t"
    "v_add_u32 v23, 0x9000, %[ab1]\n\t"
    "ds_read_b64_tr_b16 v[24:25], %[ab0] offset:0\n\t"
    "ds_read_b64_tr_b16 v[26:27], %[ab0] offset:1024\n\t"
    "ds_read_b64_tr_b16 v[28:29], %[ab1] offset:0\n\t"
    "ds_read_b64_tr_b16 v[30:31], %[ab1] offset:1024\n\t"
    "ds_read_b64_tr_b16 v[32:33], %[ab0] offset:2048\n\t"
    "ds_read_b64_tr_b16 v[34:35], %[ab0] offset:3072\n\t"
    "ds_read_b64_tr_b16 v[36:37], %[ab1] offset:2048\n\t"
    "ds_read_b64_tr_b16 v[38:39], %[ab1] offset:3072\n\t"
    "ds_read_b64_tr_b16 v[40:41], %[ab0] offset:4096\n\t"
    "ds_read_b64_tr_b16 v[42:43], %[ab0] offset:5120\n\t"
    "ds_read_b64_tr_b16 v[44:45], %[ab1] offset:4096\n\t"
    "ds_read_b64_tr_b16 v[46:47], %[ab1] offset:5120\n\t"
    "ds_read_b64_tr_b16 v[48:49], %[ab0] offset:6144\n\t"
    "ds_read_b64_tr_b16 v[50:51], %[ab0] offset:7168\n\t"
    "ds_read_b64_tr_b16 v[52:53], %[ab1] offset:6144\n\t"
    "ds_read_b64_tr_b16 v[54:55], %[ab1] offset:7168\n\t"
    "s_waitcnt vmcnt(35) lgkmcnt(12)\n\t"
    "v_mfma_f32_32x32x16_bf16 v[56:71], v[24:27], a[0:3], v[56:71]\n\t"
    "v_mfma_f32_32x32x16_bf16 v[72:87], v[28:31], a[0:3], v[72:87]\n\t"
    "ds_read_b64_tr_b16 v[24:25], %[ab0] offset:8192\n\t"
    "ds_read_b64_tr_b16 v[26:27], %[ab0] offset:9216\n\t"
    "ds_read_b64_tr_b16 v[28:29], %[ab1] offset:8192\n\t"
    "ds_read_b64_tr_b16 v[30:31], %[ab1] offset:9216\n\t"
    "s_waitcnt vmcnt(34) lgkmcnt(12)\n\t"
    "v_mfma_f32_32x32x16_bf16 v[56:71], v[32:35], a[4:7], v[56:71]\n\t"
    "v_mfma_f32_32x32x16_bf16 v[72:87], v[36:39], a[4:7], v[72:87]\n\t"
    "ds_read_b64_tr_b16 v[32:33], %[ab0] offset:10240\n\t"
    "ds_read_b64_tr_b16 v[34:35], %[ab0] offset:11264\n\t"
    "ds_read_b64_tr_b16 v[36:37], %[ab1] offset:10240\n\t"
    "ds_read_b64_tr_b16 v[38:39], %[ab1] offset:11264\n\t"
    "s_waitcnt vmcnt(33) lgkmcnt(12)\n\t"
    "v_mfma_f32_32x32x16_bf16 v[56:71], v[40:43], a[8:11], v[56:71]\n\t"
    "v_mfma_f32_32x32x16_bf16 v[72:87], v[44:47], a[8:11], v[72:87]\n\t"
    "ds_read_b64_tr_b16 v[40:41], %[ab0] offset:12288\n\t"
    "ds_read_b64_tr_b16 v[42:43], %[ab0] offset:13312\n\t"
    "ds_read_b64_tr_b16 v[44:45], %[ab1] offset:12288\n\t"
    "ds_read_b64_tr_b16 v[46:47], %[ab1] offset:13312\n\t"
    "s_waitcnt vmcnt(32) lgkmcnt(12)\n\t"
    "v_mfma_f32_32x32x16_bf16 v[56:71], v[48:51], a[12:15], v[56:71]\n\t"
    "v_mfma_f32_32x32x16_bf16 v[72:87], v[52:55], a[12:15], v[72:87]\n\t"
    "ds_read_b64_tr_b16 v[48:49], %[ab0] offset:14336\n\t"
    "ds_read_b64_tr_b16 v[50:51], %[ab0] offset:15360\n\t"
    "ds_read_b64_tr_b16 v[52:53], %[ab1] offset:14336\n\t"
    "ds_read_b64_tr_b16 v[54:55], %[ab1] offset:15360\n\t"
    "s_waitcnt vmcnt(31) lgkmcnt(12)\n\t"
    "v_mfma_f32_32x32x16_bf16 v[56:71], v[24:27], a[16:19], v[56:71]\n\t"
    "v_mfma_f32_32x32x16_bf16 v[72:87], v[28:31], a[16:19], v[72:87]\n\t"
    "ds_read_b64_tr_b16 v[24:25], %[ab0] offset:16384\n\t"
    "ds_read_b64_tr_b16 v[26:27], %[ab0] offset:17408\n\t"
    "ds_read_b64_tr_b16 v[28:29], %[ab1] offset:16384\n\t"
    "ds_read_b64_tr_b16 v[30:31], %[ab1] offset:17408\n\t"
    "s_waitcnt vmcnt(30) lgkmcnt(12)\n\t"
    "v_mfma_f32_32x32x16_bf16 v[56:71], v[32:35], a[20:23], v[56:71]\n\t"
    "v_mfma_f32_32x32x16_bf16 v[72:87], v[36:39], a[20:23], v[72:87]\n\t"
    "ds_read_b64_tr_b16 v[32:33], %[ab0] offset:18432\n\t"
    "ds_read_b64_tr_b16 v[34:35], %[ab0] offset:19456\n\t"
    "ds_read_b64_tr_b16 v[36:37], %[ab1] offset:18432\n\t"
    "ds_read_b64_tr_b16 v[38:39], %[ab1] offset:19456\n\t"
    "s_waitcnt vmcnt(29) lgkmcnt(12)\n\t"
    "v_mfma_f32_32x32x16_bf16 v[56:71], v[40:43], a[24:27], v[56:71]\n\t"
    "v_mfma_f32_32x32x16_bf16 v[72:87], v[44:47], a[24:27], v[72:87]\n\t"
    "ds_read_b64_tr_b16 v[40:41], %[ab0] offset:20480\n\t"
    "ds_read_b64_tr_b16 v[42:43], %[ab0] offset:21504\n\t"
    "ds_read_b64_tr_b16 v[44:45], %[ab1] offset:20480\n\t"
    "ds_read_b64_tr_b16 v[46:47], %[ab1] offset:21504\n\t"
    "s_waitcnt vmcnt(28) lgkmcnt(12)\n\t"
    "v_mfma_f32_32x32x16_bf16 v[56:71], v[48:51], a[28:31], v[56:71]\n\t"
    "v_mfma_f32_32x32x16_bf16 v[72:87], v[52:55], a[28:31], v[72:87]\n\t"
    "ds_read_b64_tr_b16 v[48:49], %[ab0] offset:22528\n\t"
    "ds_read_b64_tr_b16 v[50:51], %[ab0] offset:23552\n\t"
    "ds_read_b64_tr_b16 v[52:53], %[ab1] offset:22528\n\t"
    "ds_read_b64_tr_b16 v[54:55], %[ab1] offset:23552\n\t"
    "s_waitcnt vmcnt(27) lgkmcnt(12)\n\t"
    "v_mfma_f32_32x32x16_bf16 v[56:71], v[24:27], a[32:35], v[56:71]\n\t"
    "v_mfma_f32_32x32x16_bf16 v[72:87], v[28:31], a[32:35], v[72:87]\n\t"
    "ds_read_b64_tr_b16 v[24:25], %[ab0] offset:24576\n\t"
    "ds_read_b64_tr_b16 v[26:27], %[ab0] offset:25600\n\t"
    "ds_read_b64_tr_b16 v[28:29], %[ab1] offset:24576\n\t"
    "ds_read_b64_tr_b16 v[30:31], %[ab1] offset:25600\n\t"
    "s_waitcnt vmcnt(26) lgkmcnt(12)\n\t"
    "v_mfma_f32_32x32x16_bf16 v[56:71], v[32:35], a[36:39], v[56:71]\n\t"
    "v_mfma_f32_32x32x16_bf16 v[72:87], v[36:39], a[36:39], v[72:87]\n\t"
    "ds_read_b64_tr_b16 v[32:33], %[ab0] offset:26624\n\t"
    "ds_read_b64_tr_b16 v[34:35], %[ab0] offset:27648\n\t"
    "ds_read_b64_tr_b16 v[36:37], %[ab1] offset:26624\n\t"
    "ds_read_b64_tr_b16 v[38:39], %[ab1] offset:27648\n\t"
    "s_waitcnt vmcnt(25) lgkmcnt(12)\n\t"
    "v_mfma_f32_32x32x16_bf16 v[56:71], v[40:43], a[40:43], v[56:71]\n\t"
    "v_mfma_f32_32x32x16_bf16 v[72:87], v[44:47], a[40:43], v[72:87]\n\t"
    "ds_read_b64_tr_b16 v[40:41], %[ab0] offset:28672\n\t"
    "ds_read_b64_tr_b16 v[42:43], %[ab0] offset:29696\n\t"
    "ds_read_b64_tr_b16 v[44:45], %[ab1] offset:28672\n\t"
    "ds_read_b64_tr_b16 v[46:47], %[ab1] offset:29696\n\t"
    "s_waitcnt vmcnt(24) lgkmcnt(12)\n\t"
    "v_mfma_f32_32x32x16_bf16 v[56:71], v[48:51], a[44:47], v[56:71]\n\t"
    "v_mfma_f32_32x32x16_bf16 v[72:87], v[52:55], a[44:47], v[72:87]\n\t"
    "ds_read_b64_tr_b16 v[48:49], %[ab0] offset:30720\n\t"
    "ds_read_b64_tr_b16 v[50:51], %[ab0] offset:31744\n\t"
    "ds_read_b64_tr_b16 v[52:53], %[ab1] offset:30720\n\t"
    "ds_read_b64_tr_b16 v[54:55], %[ab1] offset:31744\n\t"
    "s_waitcnt vmcnt(23) lgkmcnt(12)\n\t"
    "v_mfma_f32_32x32x16_bf16 v[56:71], v[24:27], a[48:51], v[56:71]\n\t"
    "v_mfma_f32_32x32x16_bf16 v[72:87], v[28:31], a[48:51], v[72:87]\n\t"
    "ds_read_b64_tr_b16 v[24:25], %[ab0] offset:32768\n\t"
    "ds_read_b64_tr_b16 v[26:27], %[ab0] offset:33792\n\t"
    "ds_read_b64_tr_b16 v[28:29], %[ab1] offset:32768\n\t"
    "ds_read_b64_tr_b16 v[30:31], %[ab1] offset:33792\n\t"
    "s_waitcnt vmcnt(22) lgkmcnt(12)\n\t"
    "v_mfma_f32_32x32x16_bf16 v[56:71], v[32:35], a[52:55], v[56:71]\n\t"
    "v_mfma_f32_32x32x16_bf16 v[72:87], v[36:39], a[52:55], v[72:87]\n\t"
    "ds_read_b64_tr_b16 v[32:33], %[ab0] offset:34816\n\t"
    "ds_read_b64_tr_b16 v[34:35], %[ab0] offset:35840\n\t"
    "ds_read_b64_tr_b16 v[36:37], %[ab1] offset:34816\n\t"
    "ds_read_b64_tr_b16 v[38:39], %[ab1] offset:35840\n\t"
    "s_waitcnt vmcnt(21) lgkmcnt(12)\n\t"
    "v_mfma_f32_32x32x16_bf16 v[56:71], v[40:43], a[56:59], v[56:71]\n\t"
    "v_mfma_f32_32x32x16_bf16 v[72:87], v[44:47], a[56:59], v[72:87]\n\t"
    "ds_read_b64_tr_b16 v[40:41], v22 offset:0\n\t"
    "ds_read_b64_tr_b16 v[42:43], v22 offset:1024\n\t"
    "ds_read_b64_tr_b16 v[44:45], v23 offset:0\n\t"
    "ds_read_b64_tr_b16 v[46:47], v23 offset:1024\n\t"
    "s_waitcnt vmcnt(20) lgkmcnt(12)\n\t"
    "v_mfma_f32_32x32x16_bf16 v[56:71], v[48:51], a[60:63], v[56:71]\n\t"
    "v_mfma_f32_32x32x16_bf16 v[72:87], v[52:55], a[60:63], v[72:87]\n\t"
    "ds_read_b64_tr_b16 v[48:49], v22 offset:2048\n\t"
    "ds_read_b64_tr_b16 v[50:51], v22 offset:3072\n\t"
    "ds_read_b64_tr_b16 v[52:53], v23 offset:2048\n\t"
    "ds_read_b64_tr_b16 v[54:55], v23 offset:3072\n\t"
    "s_waitcnt vmcnt(19) lgkmcnt(12)\n\t"
    "v_mfma_f32_32x32x16_bf16 v[56:71], v[24:27], a[64:67], v[56:71]\n\t"
    "v_mfma_f32_32x32x16_bf16 v[72:87], v[28:31], a[64:67], v[72:87]\n\t"
    "ds_read_b64_tr_b16 v[24:25], v22 offset:4096\n\t"
    "ds_read_b64_tr_b16 v[26:27], v22 offset:5120\n\t"
    "ds_read_b64_tr_b16 v[28:29], v23 offset:4096\n\t"
    "ds_read_b64_tr_b16 v[30:31], v23 offset:5120\n\t"
    "s_waitcnt vmcnt(18) lgkmcnt(12)\n\t"
    "v_mfma_f32_32x32x16_bf16 v[56:71], v[32:35], a[68:71], v[56:71]\n\t"
    "v_mfma_f32_32x32x16_bf16 v[72:87], v[36:39], a[68:71], v[72:87]\n\t"
    "ds_read_b64_tr_b16 v[32:33], v22 offset:6144\n\t"
    "ds_read_b64_tr_b16 v[34:35], v22 offset:7168\n\t"
    "ds_read_b64_tr_b16 v[36:37], v23 offset:6144\n\t"
    "ds_read_b64_tr_b16 v[38:39], v23 offset:7168\n\t"
    "s_waitcnt vmcnt(17) lgkmcnt(12)\n\t"
    "v_mfma_f32_32x32x16_bf16 v[56:71], v[40:43], a[72:75], v[56:71]\n\t"
    "v_mfma_f32_32x32x16_bf16 v[72:87], v[44:47], a[72:75], v[72:87]\n\t"
    "ds_read_b64_tr_b16 v[40:41], v22 offset:8192\n\t"
    "ds_read_b64_tr_b16 v[42:43], v22 offset:9216\n\t"
    "ds_read_b64_tr_b16 v[44:45], v23 offset:8192\n\t"
    "ds_read_b64_tr_b16 v[46:47], v23 offset:9216\n\t"
    "s_waitcnt vmcnt(16) lgkmcnt(12)\n\t"
    "v_mfma_f32_32x32x16_bf16 v[56:71], v[48:51], a[76:79], v[56:71]\n\t"
    "v_mfma_f32_32x32x16_bf16 v[72:87], v[52:55], a[76:79], v[72:87]\n\t"
    "ds_read_b64_tr_b16 v[48:49], v22 offset:10240\n\t"
    "ds_read_b64_tr_b16 v[50:51], v22 offset:11264\n\t"
    "ds_read_b64_tr_b16 v[52:53], v23 offset:10240\n\t"
    "ds_read_b64_tr_b16 v[54:55], v23 offset:11264\n\t"
    "s_waitcnt vmcnt(15) lgkmcnt(12)\n\t"
    "v_mfma_f32_32x32x16_bf16 v[56:71], v[24:27], a[80:83], v[56:71]\n\t"
    "v_mfma_f32_32x32x16_bf16 v[72:87], v[28:31], a[80:83], v[72:87]\n\t"
    "ds_read_b64_tr_b16 v[24:25], v22 offset:12288\n\t"
    "ds_read_b64_tr_b16 v[26:27], v22 offset:13312\n\t"
    "ds_read_b64_tr_b16 v[28:29], v23 offset:12288\n\t"
    "ds_read_b64_tr_b16 v[30:31], v23 offset:13312\n\t"
    "s_waitcnt vmcnt(14) lgkmcnt(12)\n\t"
    "v_mfma_f32_32x32x16_bf16 v[56:71], v[32:35], a[84:87], v[56:71]\n\t"
    "v_mfma_f32_32x32x16_bf16 v[72:87], v[36:39], a[84:87], v[72:87]\n\t"
    "ds_read_b64_tr_b16 v[32:33], v22 offset:14336\n\t"
    "ds_read_b64_tr_b16 v[34:35], v22 offset:15360\n\t"
    "ds_read_b64_tr_b16 v[36:37], v23 offset:14336\n\t"
    "ds_read_b64_tr_b16 v[38:39], v23 offset:15360\n\t"
    "s_waitcnt vmcnt(13) lgkmcnt(12)\n\t"
    "v_mfma_f32_32x32x16_bf16 v[56:71], v[40:43], a[88:91], v[56:71]\n\t"
    "v_mfma_f32_32x32x16_bf16 v[72:87], v[44:47], a[88:91], v[72:87]\n\t"
    "ds_read_b64_tr_b16 v[40:41], v22 offset:16384\n\t"
    "ds_read_b64_tr_b16 v[42:43], v22 offset:17408\n\t"
    "ds_read_b64_tr_b16 v[44:45], v23 offset:16384\n\t"
    "ds_read_b64_tr_b16 v[46:47], v23 offset:17408\n\t"
    "s_waitcnt vmcnt(12) lgkmcnt(12)\n\t"
    "v_mfma_f32_32x32x16_bf16 v[56:71], v[48:51], a[92:95], v[56:71]\n\t"
    "v_mfma_f32_32x32x16_bf16 v[72:87], v[52:55], a[92:95], v[72:87]\n\t"
    "ds_read_b64_tr_b16 v[48:49], v22 offset:18432\n\t"
    "ds_read_b64_tr_b16 v[50:51], v22 offset:19456\n\t"
    "ds_read_b64_tr_b16 v[52:53], v23 offset:18432\n\t"
    "ds_read_b64_tr_b16 v[54:55], v23 offset:19456\n\t"
    "s_waitcnt vmcnt(11) lgkmcnt(12)\n\t"
    "v_mfma_f32_32x32x16_bf16 v[56:71], v[24:27], a[96:99], v[56:71]\n\t"
    "v_mfma_f32_32x32x16_bf16 v[72:87], v[28:31], a[96:99], v[72:87]\n\t"
    "ds_read_b64_tr_b16 v[24:25], v22 offset:20480\n\t"
    "ds_read_b64_tr_b16 v[26:27], v22 offset:21504\n\t"
    "ds_read_b64_tr_b16 v[28:29], v23 offset:20480\n\t"
    "ds_read_b64_tr_b16 v[30:31], v23 offset:21504\n\t"
    "s_waitcnt vmcnt(10) lgkmcnt(12)\n\t"
    "v_mfma_f32_32x32x16_bf16 v[56:71], v[32:35], a[100:103], v[56:71]\n\t"
    "v_mfma_f32_32x32x16_bf16 v[72:87], v[36:39], a[100:103], v[72:87]\n\t"
    "ds_read_b64_tr_b16 v[32:33], v22 offset:22528\n\t"
    "ds_read_b64_tr_b16 v[34:35], v22 offset:23552\n\t"
    "ds_read_b64_tr_b16 v[36:37], v23 offset:22528\n\t"
    "ds_read_b64_tr_b16 v[38:39], v23 offset:23552\n\t"
    "s_waitcnt vmcnt(9) lgkmcnt(12)\n\t"
    "v_mfma_f32_32x32x16_bf16 v[56:71], v[40:43], a[104:107], v[56:71]\n\t"
    "v_mfma_f32_32x32x16_bf16 v[72:87], v[44:47], a[104:107], v[72:87]\n\t"
    "ds_read_b64_tr_b16 v[40:41], v22 offset:24576\n\t"
    "ds_read_b64_tr_b16 v[42:43], v22 offset:25600\n\t"
    "ds_read_b64_tr_b16 v[44:45], v23 offset:24576\n\t"
    "ds_read_b64_tr_b16 v[46:47], v23 offset:25600\n\t"
    "s_waitcnt vmcnt(8) lgkmcnt(12)\n\t"
    "v_mfma_f32_32x32x16_bf16 v[56:71], v[48:51], a[108:111], v[56:71]\n\t"
    "v_mfma_f32_32x32x16_bf16 v[72:87], v[52:55], a[108:111], v[72:87]\n\t"
    "ds_read_b64_tr_b16 v[48:49], v22 offset:26624\n\t"
    "ds_read_b64_tr_b16 v[50:51], v22 offset:27648\n\t"
    "ds_read_b64_tr_b16 v[52:53], v23 offset:26624\n\t"
    "ds_read_b64_tr_b16 v[54:55], v23 offset:27648\n\t"
    "s_waitcnt vmcnt(7) lgkmcnt(12)\n\t"
    "v_mfma_f32_32x32x16_bf16 v[56:71], v[24:27], a[112:115], v[56:71]\n\t"
    "v_mfma_f32_32x32x16_bf16 v[72:87], v[28:31], a[112:115], v[72:87]\n\t"
    "ds_read_b64_tr_b16 v[24:25], v22 offset:28672\n\t"
    "ds_read_b64_tr_b16 v[26:27], v22 offset:29696\n\t"
    "ds_read_b64_tr_b16 v[28:29], v23 offset:28672\n\t"
    "ds_read_b64_tr_b16 v[30:31], v23 offset:29696\n\t"
    "s_waitcnt vmcnt(6) lgkmcnt(12)\n\t"
    "v_mfma_f32_32x32x16_bf16 v[56:71], v[32:35], a[116:119], v[56:71]\n\t"
    "v_mfma_f32_32x32x16_bf16 v[72:87], v[36:39], a[116:119], v[72:87]\n\t"
    "ds_read_b64_tr_b16 v[32:33], v22 offset:30720\n\t"
    "ds_read_b64_tr_b16 v[34:35], v22 offset:31744\n\t"
    "ds_read_b64_tr_b16 v[36:37], v23 offset:30720\n\t"
    "ds_read_b64_tr_b16 v[38:39], v23 offset:31744\n\t"
    "s_waitcnt vmcnt(5) lgkmcnt(12)\n\t"
    "v_mfma_f32_32x32x16_bf16 v[56:71], v[40:43], a[120:123], v[56:71]\n\t"
    "v_mfma_f32_32x32x16_bf16 v[72:87], v[44:47], a[120:123], v[72:87]\n\t"
    "ds_read_b64_tr_b16 v[40:41], v22 offset:32768\n\t"
    "ds_read_b64_tr_b16 v[42:43], v22 offset:33792\n\t"
    "ds_read_b64_tr_b16 v[44:45], v23 offset:32768\n\t"
    "ds_read_b64_tr_b16 v[46:47], v23 offset:33792\n\t"
    "s_waitcnt vmcnt(4) lgkmcnt(12)\n\t"
    "v_mfma_f32_32x32x16_bf16 v[56:71], v[48:51], a[124:127], v[56:71]\n\t"
    "v_mfma_f32_32x32x16_bf16 v[72:87], v[52:55], a[124:127], v[72:87]\n\t"
    "ds_read_b64_tr_b16 v[48:49], v22 offset:34816\n\t"
    "ds_read_b64_tr_b16 v[50:51], v22 offset:35840\n\t"
    "ds_read_b64_tr_b16 v[52:53], v23 offset:34816\n\t"
    "ds_read_b64_tr_b16 v[54:55], v23 offset:35840\n\t"
    "s_waitcnt vmcnt(3) lgkmcnt(12)\n\t"
    "v_mfma_f32_32x32x16_bf16 v[56:71], v[24:27], a[128:131], v[56:71]\n\t"
    "v_mfma_f32_32x32x16_bf16 v[72:87], v[28:31], a[128:131], v[72:87]\n\t"
    "s_waitcnt vmcnt(2) lgkmcnt(8)\n\t"
    "v_mfma_f32_32x32x16_bf16 v[56:71], v[32:35], a[132:135], v[56:71]\n\t"
    "v_mfma_f32_32x32x16_bf16 v[72:87], v[36:39], a[132:135], v[72:87]\n\t"
    "s_waitcnt vmcnt(1) lgkmcnt(4)\n\t"
    "v_mfma_f32_32x32x16_bf16 v[56:71], v[40:43], a[136:139], v[56:71]\n\t"
    "v_mfma_f32_32x32x16_bf16 v[72:87], v[44:47], a[136:139], v[72:87]\n\t"
    "s_waitcnt vmcnt(0) lgkmcnt(0)\n\t"
    "v_mfma_f32_32x32x16_bf16 v[56:71], v[48:51], a[140:143], v[56:71]\n\t"
    "v_mfma_f32_32x32x16_bf16 v[72:87], v[52:55], a[140:143], v[72:87]\n\t"
    "s_nop 15\n\t"
    "s_nop 3\n\t"
      : "+{v[56:71]}"(acc0), "+{v[72:87]}"(acc1), X_OUT(x) : [ab0] "v"(ab0), [ab1] "v"(ab1), [vo] "v"(voff16), [ws] "s"(ws), [off] "s"(off) : "memory", "scc", "s64", "v22", "v23", "v24", "v25", "v26", "v27", "v28", "v29", "v30", "v31", "v32", "v33", "v34", "v35", "v36", "v37", "v38", "v39", "v40", "v41", "v42", "v43", "v44", "v45", "v46", "v47", "v48", "v49", "v50", "v51", "v52", "v53", "v54", "v55");
}
DEV void mm0(const XRegs& x, unsigned ab0, f32x16& acc0) {
  asm volatile(
    "v_add_u32 v22, 0x9000, %[ab0]\n\t"
    "ds_read_b64_tr_b16 v[24:25], %[ab0] offset:0\n\t"
    "ds_read_b64_tr_b16 v[26:27], %[ab0] offset:1024\n\t"
    "ds_read_b64_tr_b16 v[32:33], %[ab0] offset:2048\n\t"
    "ds_read_b64_tr_b16 v[34:35], %[ab0] offset:3072\n\t"
    "ds_read_b64_tr_b16 v[40:41], %[ab0] offset:4096\n\t"
    "ds_read_b64_tr_b16 v[42:43], %[ab0] offset:5120\n\t"
    "ds_read_b64_tr_b16 v[48:49], %[ab0] offset:6144\n\t"
    "ds_read_b64_tr_b16 v[50:51], %[ab0] offset:7168\n\t"
    "s_waitcnt lgkmcnt(6)\n\t"
    "v_mfma_f32_32x32x16_bf16 v[56:71], v[24:27], a[0:3], 0\n\t"
    "ds_read_b64_tr_b16 v[24:25], %[ab0] offset:8192\n\t"
    "ds_read_b64_tr_b16 v[26:27], %[ab0] offset:9216\n\t"
    "s_waitcnt lgkmcnt(6)\n\t"
    "v_mfma_f32_32x32x16_bf16 v[56:71], v[32:35], a[4:7], v[56:71]\n\t"
    "ds_read_b64_tr_b16 v[32:33], %[ab0] offset:10240\n\t"
    "ds_read_b64_tr_b16 v[34:35], %[ab0] offset:11264\n\t"
    "s_waitcnt lgkmcnt(6)\n\t"
    "v_mfma_f32_32x32x16_bf16 v[56:71], v[40:43], a[8:11], v[56:71]\n\t"
    "ds_read_b64_tr_b16 v[40:41], %[ab0] offset:12288\n\t"
    "ds_read_b64_tr_b16 v[42:43], %[ab0] offset:13312\n\t"
    "s_waitcnt lgkmcnt(6)\n\t"
    "v_mfma_f32_32x32x16_bf16 v[56:71], v[48:51], a[12:15], v[56:71]\n\t"
    "ds_read_b64_tr_b16 v[48:49], %[ab0] offset:14336\n\t"
    "ds_read_b64_tr_b16 v[50:51], %[ab0] offset:15360\n\t"
    "s_waitcnt lgkmcnt(6)\n\t"
    "v_mfma_f32_32x32x16_bf16 v[56:71], v[24:27], a[16:19], v[56:71]\n\t"
    "ds_read_b64_tr_b16 v[24:25], %[ab0] offset:16384\n\t"
    "ds_read_b64_tr_b16 v[26:27], %[ab0] offset:17408\n\t"
    "s_waitcnt lgkmcnt(6)\n\t"
    "v_mfma_f32_32x32x16_bf16 v[56:71], v[32:35], a[20:23], v[56:71]\n\t"
    "ds_read_b64_tr_b16 v[32:33], %[ab0] offset:18432\n\t"
    "ds_read_b64_tr_b16 v[34:35], %[ab0] offset:19456\n\t"
    "s_waitcnt lgkmcnt(6)\n\t"
    "v_mfma_f32_32x32x16_bf16 v[56:71], v[40:43], a[24:27], v[56:71]\n\t"
    "ds_read_b64_tr_b16 v[40:41], %[ab0] offset:20480\n\t"
    "ds_read_b64_tr_b16 v[42:43], %[ab0] offset:21504\n\t"
    "s_waitcnt lgkmcnt(6)\n\t"
    "v_mfma_f32_32x32x16_bf16 v[56:71], v[48:51], a[28:31], v[56:71]\n\t"
    "ds_read_b64_tr_b16 v[48:49], %[ab0] offset:22528\n\t"
    "ds_read_b64_tr_b16 v[50:51], %[ab0] offset:23552\n\t"
    "s_waitcnt lgkmcnt(6)\n\t"
    "v_mfma_f32_32x32x16_bf16 v[56:71], v[24:27], a[32:35], v[56:71]\n\t"
    "ds_read_b64_tr_b16 v[24:25], %[ab0] offset:24576\n\t"
    "ds_read_b64_tr_b16 v[26:27], %[ab0] offset:25600\n\t"
    "s_waitcnt lgkmcnt(6)\n\t"
    "v_mfma_f32_32x32x16_bf16 v[56:71], v[32:35], a[36:39], v[56:71]\n\t"
    "ds_read_b64_tr_b16 v[32:33], %[ab0] offset:26624\n\t"
    "ds_read_b64_tr_b16 v[34:35], %[ab0] offset:27648\n\t"
    "s_waitcnt lgkmcnt(6)\n\t"
    "v_mfma_f32_32x32x16_bf16 v[56:71], v[40:43], a[40:43], v[56:71]\n\t"
    "ds_read_b64_tr_b16 v[40:41], %[ab0] offset:28672\n\t"
    "ds_read_b64_tr_b16 v[42:43], %[ab0] offset:29696\n\t"
    "s_waitcnt lgkmcnt(6)\n\t"
    "v_mfma_f32_32x32x16_bf16 v[56:71], v[48:51], a[44:47], v[56:71]\n\t"
    "ds_read_b64_tr_b16 v[48:49], %[ab0] offset:30720\n\t"
    "ds_read_b64_tr_b16 v[50:51], %[ab0] offset:31744\n\t"
    "s_waitcnt lgkmcnt(6)\n\t"
    "v_mfma_f32_32x32x16_bf16 v[56:71], v[24:27], a[48:51], v[56:71]\n\t"
    "ds_read_b64_tr_b16 v[24:25], %[ab0] offset:32768\n\t"
    "ds_read_b64_tr_b16 v[26:27], %[ab0] offset:33792\n\t"
    "s_waitcnt lgkmcnt(6)\n\t"
    "v_mfma_f32_32x32x16_bf16 v[56:71], v[32:35], a[52:55], v[56:71]\n\t"
    "ds_read_b64_tr_b16 v[32:33], %[ab0] offset:34816\n\t"
    "ds_read_b64_tr_b16 v[34:35], %[ab0] offset:35840\n\t"
    "s_waitcnt lgkmcnt(6)\n\t"
    "v_mfma_f32_32x32x16_bf16 v[56:71], v[40:43], a[56:59], v[56:71]\n\t"
    "ds_read_b64_tr_b16 v[40:41], v22 offset:0\n\t"
    "ds_read_b64_tr_b16 v[42:43], v22 offset:1024\n\t"
    "s_waitcnt lgkmcnt(6)\n\t"
    "v_mfma_f32_32x32x16_bf16 v[56:71], v[48:51], a[60:63], v[56:71]\n\t"
    "ds_read_b64_tr_b16 v[48:49], v22 offset:2048\n\t"
    "ds_read_b64_tr_b16 v[50:51], v22 offset:3072\n\t"
    "s_waitcnt lgkmcnt(6)\n\t"
    "v_mfma_f32_32x32x16_bf16 v[56:71], v[24:27], a[64:67], v[56:71]\n\t"
    "ds_read_b64_tr_b16 v[24:25], v22 offset:4096\n\t"
    "ds_read_b64_tr_b16 v[26:27], v22 offset:5120\n\t"
    "s_waitcnt lgkmcnt(6)\n\t"
    "v_mfma_f32_32x32x16_bf16 v[56:71], v[32:35], a[68:71], v[56:71]\n\t"
    "ds_read_b64_tr_b16 v[32:33], v22 offset:6144\n\t"
    "ds_read_b64_tr_b16 v[34:35], v22 offset:7168\n\t"
    "s_waitcnt lgkmcnt(6)\n\t"
    "v_mfma_f32_32x32x16_bf16 v[56:71], v[40:43], a[72:75], v[56:71]\n\t"
    "ds_read_b64_tr_b16 v[40:41], v22 offset:8192\n\t"
    "ds_read_b64_tr_b16 v[42:43], v22 offset:9216\n\t"
    "s_waitcnt lgkmcnt(6)\n\t"
    "v_mfma_f32_32x32x16_bf16 v[56:71], v[48:51], a[76:79], v[56:71]\n\t"
    "ds_read_b64_tr_b16 v[48:49], v22 offset:10240\n\t"
    "ds_read_b64_tr_b16 v[50:51], v22 offset:11264\n\t"
    "s_waitcnt lgkmcnt(6)\n\t"
    "v_mfma_f32_32x32x16_bf16 v[56:71], v[24:27], a[80:83], v[56:71]\n\t"
    "ds_read_b64_tr_b16 v[24:25], v22 offset:12288\n\t"
    "ds_read_b64_tr_b16 v[26:27], v22 offset:13312\n\t"
    "s_waitcnt lgkmcnt(6)\n\t"
    "v_mfma_f32_32x32x16_bf16 v[56:71], v[32:35], a[84:87], v[56:71]\n\t"
    "ds_read_b64_tr_b16 v[32:33], v22 offset:14336\n\t"
    "ds_read_b64_tr_b16 v[34:35], v22 offset:15360\n\t"
    "s_waitcnt lgkmcnt(6)\n\t"
    "v_mfma_f32_32x32x16_bf16 v[56:71], v[40:43], a[88:91], v[56:71]\n\t"
    "ds_read_b64_tr_b16 v[40:41], v22 offset:16384\n\t"
    "ds_read_b64_tr_b16 v[42:43], v22 offset:17408\n\t"
    "s_waitcnt lgkmcnt(6)\n\t"
    "v_mfma_f32_32x32x16_bf16 v[56:71], v[48:51], a[92:95], v[56:71]\n\t"
    "ds_read_b64_tr_b16 v[48:49], v22 offset:18432\n\t"
    "ds_read_b64_tr_b16 v[50:51], v22 offset:19456\n\t"
    "s_waitcnt lgkmcnt(6)\n\t"
    "v_mfma_f32_32x32x16_bf16 v[56:71], v[24:27], a[96:99], v[56:71]\n\t"
    "ds_read_b64_tr_b16 v[24:25], v22 offset:20480\n\t"
    "ds_read_b64_tr_b16 v[26:27], v22 offset:21504\n\t"
    "s_waitcnt lgkmcnt(6)\n\t"
    "v_mfma_f32_32x32x16_bf16 v[56:71], v[32:35], a[100:103], v[56:71]\n\t"
    "ds_read_b64_tr_b16 v[32:33], v22 offset:22528\n\t"
    "ds_read_b64_tr_b16 v[34:35], v22 offset:23552\n\t"
    "s_waitcnt lgkmcnt(6)\n\t"
    "v_mfma_f32_32x32x16_bf16 v[56:71], v[40:43], a[104:107], v[56:71]\n\t"
    "ds_read_b64_tr_b16 v[40:41], v22 offset:24576\n\t"
    "ds_read_b64_tr_b16 v[42:43], v22 offset:25600\n\t"
    "s_waitcnt lgkmcnt(6)\n\t"
    "v_mfma_f32_32x32x16_bf16 v[56:71], v[48:51], a[108:111], v[56:71]\n\t"
    "ds_read_b64_tr_b16 v[48:49], v22 offset:26624\n\t"
    "ds_read_b64_tr_b16 v[50:51], v22 offset:27648\n\t"
    "s_waitcnt lgkmcnt(6)\n\t"
    "v_mfma_f32_32x32x16_bf16 v[56:71], v[24:27], a[112:115], v[56:71]\n\t"
    "ds_read_b64_tr_b16 v[24:25], v22 offset:28672\n\t"
    "ds_read_b64_tr_b16 v[26:27], v22 offset:29696\n\t"
    "s_waitcnt lgkmcnt(6)\n\t"
    "v_mfma_f32_32x32x16_bf16 v[56:71], v[32:35], a[116:119], v[56:71]\n\t"
    "ds_read_b64_tr_b16 v[32:33], v22 offset:30720\n\t"
    "ds_read_b64_tr_b16 v[34:35], v22 offset:31744\n\t"
    "s_waitcnt lgkmcnt(6)\n\t"
    "v_mfma_f32_32x32x16_bf16 v[56:71], v[40:43], a[120:123], v[56:71]\n\t"
    "ds_read_b64_tr_b16 v[40:41], v22 offset:32768\n\t"
    "ds_read_b64_tr_b16 v[42:43], v22 offset:33792\n\t"
    "s_waitcnt lgkmcnt(6)\n\t"
    "v_mfma_f32_32x32x16_bf16 v[56:71], v[48:51], a[124:127], v[56:71]\n\t"
    "ds_read_b64_tr_b16 v[48:49], v22 offset:34816\n\t"
    "ds_read_b64_tr_b16 v[50:51], v22 offset:35840\n\t"
    "s_waitcnt lgkmcnt(6)\n\t"
    "v_mfma_f32_32x32x16_bf16 v[56:71], v[24:27], a[128:131], v[56:71]\n\t"
    "s_waitcnt lgkmcnt(4)\n\t"
    "v_mfma_f32_32x32x16_bf16 v[56:71], v[32:35], a[132:135], v[56:71]\n\t"
    "s_waitcnt lgkmcnt(2)\n\t"
    "v_mfma_f32_32x32x16_bf16 v[56:71], v[40:43], a[136:139], v[56:71]\n\t"
    "s_waitcnt lgkmcnt(0)\n\t"
    "v_mfma_f32_32x32x16_bf16 v[56:71], v[48:51], a[140:143], v[56:71]\n\t"
    "s_nop 15\n\t"
    "s_nop 3\n\t"
      : "={v[56:71]}"(acc0) : [ab0] "v"(ab0), X_IN(x) : "memory", "v22", "v23", "v24", "v25", "v26", "v27", "v28", "v29", "v30", "v31", "v32", "v33", "v34", "v35", "v36", "v37", "v38", "v39", "v40", "v41", "v42", "v43", "v44", "v45", "v46", "v47", "v48", "v49", "v50", "v51", "v52", "v53", "v54", "v55");
}
DEV void mm0c(const XRegs& x, unsigned ab0, f32x16& acc0) {
  asm volatile("s_nop 1\n\t"
    "v_add_u32 v22, 0x9000, %[ab0]\n\t"
    "ds_read_b64_tr_b16 v[24:25], %[ab0] offset:0\n\t"
    "ds_read_b64_tr_b16 v[26:27], %[ab0] offset:1024\n\t"
    "ds_read_b64_tr_b16 v[32:33], %[ab0] offset:2048\n\t"
    "ds_read_b64_tr_b16 v[34:35], %[ab0] offset:3072\n\t"
    "ds_read_b64_tr_b16 v[40:41], %[ab0] offset:4096\n\t"
    "ds_read_b64_tr_b16 v[42:43], %[ab0] offset:5120\n\t"
    "ds_read_b64_tr_b16 v[48:49], %[ab0] offset:6144\n\t"
    "ds_read_b64_tr_b16 v[50:51], %[ab0] offset:7168\n\t"
    "s_waitcnt lgkmcnt(6)\n\t"
    "v_mfma_f32_32x32x16_bf16 v[56:71], v[24:27], a[0:3], v[56:71]\n\t"
    "ds_read_b64_tr_b16 v[24:25], %[ab0] offset:8192\n\t"
    "ds_read_b64_tr_b16 v[26:27], %[ab0] offset:9216\n\t"
    "s_waitcnt lgkmcnt(6)\n\t"
    "v_mfma_f32_32x32x16_bf16 v[56:71], v[32:35], a[4:7], v[56:71]\n\t"
    "ds_read_b64_tr_b16 v[32:33], %[ab0] offset:10240\n\t"
    "ds_read_b64_tr_b16 v[34:35], %[ab0] offset:11264\n\t"
    "s_waitcnt lgkmcnt(6)\n\t"
    "v_mfma_f32_32x32x16_bf16 v[56:71], v[40:43], a[8:11], v[56:71]\n\t"
    "ds_read_b64_tr_b16 v[40:41], %[ab0] offset:12288\n\t"
    "ds_read_b64_tr_b16 v[42:43], %[ab0] offset:13312\n\t"
    "s_waitcnt lgkmcnt(6)\n\t"
    "v_mfma_f32_32x32x16_bf16 v[56:71], v[48:51], a[12:15], v[56:71]\n\t"
    "ds_read_b64_tr_b16 v[48:49], %[ab0] offset:14336\n\t"
    "ds_read_b64_tr_b16 v[50:51], %[ab0] offset:15360\n\t"
    "s_waitcnt lgkmcnt(6)\n\t"
    "v_mfma_f32_32x32x16_bf16 v[56:71], v[24:27], a[16:19], v[56:71]\n\t"
    "ds_read_b64_tr_b16 v[24:25], %[ab0] offset:16384\n\t"
    "ds_read_b64_tr_b16 v[26:27], %[ab0] offset:17408\n\t"
    "s_waitcnt lgkmcnt(6)\n\t"
    "v_mfma_f32_32x32x16_bf16 v[56:71], v[32:35], a[20:23], v[56:71]\n\t"
    "ds_read_b64_tr_b16 v[32:33], %[ab0] offset:18432\n\t"
    "ds_read_b64_tr_b16 v[34:35], %[ab0] offset:19456\n\t"
    "s_waitcnt lgkmcnt(6)\n\t"
    "v_mfma_f32_32x32x16_bf16 v[56:71], v[40:43], a[24:27], v[56:71]\n\t"
    "ds_read_b64_tr_b16 v[40:41], %[ab0] offset:20480\n\t"
    "ds_read_b64_tr_b16 v[42:43], %[ab0] offset:21504\n\t"
    "s_waitcnt lgkmcnt(6)\n\t"
    "v_mfma_f32_32x32x16_bf16 v[56:71], v[48:51], a[28:31], v[56:71]\n\t"
    "ds_read_b64_tr_b16 v[48:49], %[ab0] offset:22528\n\t"
    "ds_read_b64_tr_b16 v[50:51], %[ab0] offset:23552\n\t"
    "s_waitcnt lgkmcnt(6)\n\t"
    "v_mfma_f32_32x32x16_bf16 v[56:71], v[24:27], a[32:35], v[56:71]\n\t"
    "ds_read_b64_tr_b16 v[24:25], %[ab0] offset:24576\n\t"
    "ds_read_b64_tr_b16 v[26:27], %[ab0] offset:25600\n\t"
    "s_waitcnt lgkmcnt(6)\n\t"
    "v_mfma_f32_32x32x16_bf16 v[56:71], v[32:35], a[36:39], v[56:71]\n\t"
    "ds_read_b64_tr_b16 v[32:33], %[ab0] offset:26624\n\t"
    "ds_read_b64_tr_b16 v[34:35], %[ab0] offset:27648\n\t"
    "s_waitcnt lgkmcnt(6)\n\t"
    "v_mfma_f32_32x32x16_bf16 v[56:71], v[40:43], a[40:43], v[56:71]\n\t"
    "ds_read_b64_tr_b16 v[40:41], %[ab0] offset:28672\n\t"
    "ds_read_b64_tr_b16 v[42:43], %[ab0] offset:29696\n\t"
    "s_waitcnt lgkmcnt(6)\n\t"
    "v_mfma_f32_32x32x16_bf16 v[56:71], v[48:51], a[44:47], v[56:71]\n\t"
    "ds_read_b64_tr_b16 v[48:49], %[ab0] offset:30720\n\t"
    "ds_read_b64_tr_b16 v[50:51], %[ab0] offset:31744\n\t"
    "s_waitcnt lgkmcnt(6)\n\t"
    "v_mfma_f32_32x32x16_bf16 v[56:71], v[24:27], a[48:51], v[56:71]\n\t"
    "ds_read_b64_tr_b16 v[24:25], %[ab0] offset:32768\n\t"
    "ds_read_b64_tr_b16 v[26:27], %[ab0] offset:33792\n\t"
    "s_waitcnt lgkmcnt(6)\n\t"
    "v_mfma_f32_32x32x16_bf16 v[56:71], v[32:35], a[52:55], v[56:71]\n\t"
    "ds_read_b64_tr_b16 v[32:33], %[ab0] offset:34816\n\t"
    "ds_read_b64_tr_b16 v[34:35], %[ab0] offset:35840\n\t"
    "s_waitcnt lgkmcnt(6)\n\t"
    "v_mfma_f32_32x32x16_bf16 v[56:71], v[40:43], a[56:59], v[56:71]\n\t"
    "ds_read_b64_tr_b16 v[40:41], v22 offset:0\n\t"
    "ds_read_b64_tr_b16 v[42:43], v22 offset:1024\n\t"
    "s_waitcnt lgkmcnt(6)\n\t"
    "v_mfma_f32_32x32x16_bf16 v[56:71], v[48:51], a[60:63], v[56:71]\n\t"
    "ds_read_b64_tr_b16 v[48:49], v22 offset:2048\n\t"
    "ds_read_b64_tr_b16 v[50:51], v22 offset:3072\n\t"
    "s_waitcnt lgkmcnt(6)\n\t"
    "v_mfma_f32_32x32x16_bf16 v[56:71], v[24:27], a[64:67], v[56:71]\n\t"
    "ds_read_b64_tr_b16 v[24:25], v22 offset:4096\n\t"
    "ds_read_b64_tr_b16 v[26:27], v22 offset:5120\n\t"
    "s_waitcnt lgkmcnt(6)\n\t"
    "v_mfma_f32_32x32x16_bf16 v[56:71], v[32:35], a[68:71], v[56:71]\n\t"
    "ds_read_b64_tr_b16 v[32:33], v22 offset:6144\n\t"
    "ds_read_b64_tr_b16 v[34:35], v22 offset:7168\n\t"
    "s_waitcnt lgkmcnt(6)\n\t"
    "v_mfma_f32_32x32x16_bf16 v[56:71], v[40:43], a[72:75], v[56:71]\n\t"
    "ds_read_b64_tr_b16 v[40:41], v22 offset:8192\n\t"
    "ds_read_b64_tr_b16 v[42:43], v22 offset:9216\n\t"
    "s_waitcnt lgkmcnt(6)\n\t"
    "v_mfma_f32_32x32x16_bf16 v[56:71], v[48:51], a[76:79], v[56:71]\n\t"
    "ds_read_b64_tr_b16 v[48:49], v22 offset:10240\n\t"
    "ds_read_b64_tr_b16 v[50:51], v22 offset:11264\n\t"
    "s_waitcnt lgkmcnt(6)\n\t"
    "v_mfma_f32_32x32x16_bf16 v[56:71], v[24:27], a[80:83], v[56:71]\n\t"
    "ds_read_b64_tr_b16 v[24:25], v22 offset:12288\n\t"
    "ds_read_b64_tr_b16 v[26:27], v22 offset:13312\n\t"
    "s_waitcnt lgkmcnt(6)\n\t"
    "v_mfma_f32_32x32x16_bf16 v[56:71], v[32:35], a[84:87], v[56:71]\n\t"
    "ds_read_b64_tr_b16 v[32:33], v22 offset:14336\n\t"
    "ds_read_b64_tr_b16 v[34:35], v22 offset:15360\n\t"
    "s_waitcnt lgkmcnt(6)\n\t"
    "v_mfma_f32_32x32x16_bf16 v[56:71], v[40:43], a[88:91], v[56:71]\n\t"
    "ds_read_b64_tr_b16 v[40:41], v22 offset:16384\n\t"
    "ds_read_b64_tr_b16 v[42:43], v22 offset:17408\n\t"
    "s_waitcnt lgkmcnt(6)\n\t"
    "v_mfma_f32_32x32x16_bf16 v[56:71], v[48:51], a[92:95], v[56:71]\n\t"
    "ds_read_b64_tr_b16 v[48:49], v22 offset:18432\n\t"
    "ds_read_b64_tr_b16 v[50:51], v22 offset:19456\n\t"
    "s_waitcnt lgkmcnt(6)\n\t"
    "v_mfma_f32_32x32x16_bf16 v[56:71], v[24:27], a[96:99], v[56:71]\n\t"
    "ds_read_b64_tr_b16 v[24:25], v22 offset:20480\n\t"
    "ds_read_b64_tr_b16 v[26:27], v22 offset:21504\n\t"
    "s_waitcnt lgkmcnt(6)\n\t"
    "v_mfma_f32_32x32x16_bf16 v[56:71], v[32:35], a[100:103], v[56:71]\n\t"
    "ds_read_b64_tr_b16 v[32:33], v22 offset:22528\n\t"
    "ds_read_b64_tr_b16 v[34:35], v22 offset:23552\n\t"
    "s_waitcnt lgkmcnt(6)\n\t"
    "v_mfma_f32_32x32x16_bf16 v[56:71], v[40:43], a[104:107], v[56:71]\n\t"
    "ds_read_b64_tr_b16 v[40:41], v22 offset:24576\n\t"
    "ds_read_b64_tr_b16 v[42:43], v22 offset:25600\n\t"
    "s_waitcnt lgkmcnt(6)\n\t"
    "v_mfma_f32_32x32x16_bf16 v[56:71], v[48:51], a[108:111], v[56:71]\n\t"
    "ds_read_b64_tr_b16 v[48:49], v22 offset:26624\n\t"
    "ds_read_b64_tr_b16 v[50:51], v22 offset:27648\n\t"
    "s_waitcnt lgkmcnt(6)\n\t"
    "v_mfma_f32_32x32x16_bf16 v[56:71], v[24:27], a[112:115], v[56:71]\n\t"
    "ds_read_b64_tr_b16 v[24:25], v22 offset:28672\n\t"
    "ds_read_b64_tr_b16 v[26:27], v22 offset:29696\n\t"
    "s_waitcnt lgkmcnt(6)\n\t"
    "v_mfma_f32_32x32x16_bf16 v[56:71], v[32:35], a[116:119], v[56:71]\n\t"
    "ds_read_b64_tr_b16 v[32:33], v22 offset:30720\n\t"
    "ds_read_b64_tr_b16 v[34:35], v22 offset:31744\n\t"
    "s_waitcnt lgkmcnt(6)\n\t"
    "v_mfma_f32_32x32x16_bf16 v[56:71], v[40:43], a[120:123], v[56:71]\n\t"
    "ds_read_b64_tr_b16 v[40:41], v22 offset:32768\n\t"
    "ds_read_b64_tr_b16 v[42:43], v22 offset:33792\n\t"
    "s_waitcnt lgkmcnt(6)\n\t"
    "v_mfma_f32_32x32x16_bf16 v[56:71], v[48:51], a[124:127], v[56:71]\n\t"
    "ds_read_b64_tr_b16 v[48:49], v22 offset:34816\n\t"
    "ds_read_b64_tr_b16 v[50:51], v22 offset:35840\n\t"
    "s_waitcnt lgkmcnt(6)\n\t"
    "v_mfma_f32_32x32x16_bf16 v[56:71], v[24:27], a[128:131], v[56:71]\n\t"
    "s_waitcnt lgkmcnt(4)\n\t"
    "v_mfma_f32_32x32x16_bf16 v[56:71], v[32:35], a[132:135], v[56:71]\n\t"
    "s_waitcnt lgkmcnt(2)\n\t"
    "v_mfma_f32_32x32x16_bf16 v[56:71], v[40:43], a[136:139], v[56:71]\n\t"
    "s_waitcnt lgkmcnt(0)\n\t"
    "v_mfma_f32_32x32x16_bf16 v[56:71], v[48:51], a[140:143], v[56:71]\n\t"
    "s_nop 15\n\t"
    "s_nop 3\n\t"
      : "+{v[56:71]}"(acc0) : [ab0] "v"(ab0), X_IN(x) : "memory", "v22", "v23", "v24", "v25", "v26", "v27", "v28", "v29", "v30", "v31", "v32", "v33", "v34", "v35", "v36", "v37", "v38", "v39", "v40", "v41", "v42", "v43", "v44", "v45", "v46", "v47", "v48", "v49", "v50", "v51", "v52", "v53", "v54", "v55");
}
template <bool WO> DEV void stage4(srd_t wsrd, unsigned ldsb, int jp, int w, int lane) {
  const unsigned sw = ((lane & 7) ^ ((lane >> 2) & 4)) << 4, r8 = lane >> 3;
  const unsigned v0 = r8 * (D * 2) + sw, v1 = v0 + 8 * (D * 2);
  const unsigned a0 = (18 * (4 * (r8 >> 2) + ((r8 & 3) >> 1)) + 16 + (r8 & 1)) * (D * 2) + sw, a1 = a0 + 36 * (D * 2);
#pragma unroll 1
  for (int s = 9 * w; s < 9 * w + 9; s++) {
    const bool tail = WO && s >= 32;
    const unsigned so = (WO ? (tail ? 144 * (s - 32) : 18 * s) : 16 * s) * (D * 2), dst = ldsb + s * 2048;
    const unsigned o0 = tail ? a0 : v0, o1 = tail ? a1 : v1;
    dma(o0, wsrd, so + jp * 128, dst); dma(o1, wsrd, so + jp * 128, dst + 1024);
    dma(o0, wsrd, so + 1024, dst + 2 * ST_B); dma(o1, wsrd, so + 1024, dst + 2 * ST_B + 1024);
  }
}

constexpr int ML_CS = 4 * ST_B, ML_RED = ML_CS + 576 * 4, ML_PRG = ML_RED + 2048;
DEV void mlp_phase(const Params& P, char* lds, unsigned ldsb, srd_t ws, unsigned tag, int tt, int c8, int w, float mu_x, float rs_x) {
  int lane = lane_id(), c = lane & 31, hh = lane >> 5;
  const int tid = w * 64 + lane;
  unsigned* flags = (unsigned*)(P.ws + WS_FLAG);
  const int tb = tt * 4 + w;
  unsigned voff16 = lane * 16;
  const int tok = tb * 32 + c;
  const int xdb = 16 + (c8 >> 2);
  u32x2 xr[3][4];
  {
    const srd_t xs = mk_srd(P.x, S * D * 2);
#pragma unroll
    for (int u = 0; u < 3; u++)
#pragma unroll
      for (int g = 0; g < 4; g++) xr[u][g] = __builtin_amdgcn_raw_buffer_load_b64(xs, tok * (D * 2) + 8 * hh + 16 * g, (u < 2 ? 2 * c8 + u : xdb) * 64, 0);
  }
  f32x16 acc[3];
  {
    const float nmr = -mu_x * rs_x;
#pragma unroll
    for (int u = 0; u < 3; u++)
#pragma unroll
      for (int g = 0; g < 4; g++) {
        acc[u][4 * g] = __builtin_fmaf(bflo(xr[u][g][0]), rs_x, nmr); acc[u][4 * g + 1] = __builtin_fmaf(bfhi(xr[u][g][0]), rs_x, nmr);
        acc[u][4 * g + 2] = __builtin_fmaf(bflo(xr[u][g][1]), rs_x, nmr); acc[u][4 * g + 3] = __builtin_fmaf(bfhi(xr[u][g][1]), rs_x, nmr);
      }
    pin_v(acc[0]); pin_v(acc[1]); pin_v(acc[2]);
  }
  await_wg(ws, ldsb, FL_P1 + (12 + (lane & 3)) * 16 + ((tt - 1 + (lane >> 2)) & 15), lane < 12, tag, w);
  STAMP(3);
  XRegs X;
  {
    auto x1_out = [&](const f32x16& a, int db) {
      unsigned p[8];
#pragma unroll
      for (int e = 0; e < 8; e++) p[e] = cvtpk(a[2 * e], a[2 * e + 1]);
      const unsigned so = WS_X1F + (tb * 36 + 2 * db) * 1024;
      st128s(ws, voff16, so, u32x4{p[0], p[1], p[2], p[3]});
      st128s(ws, voff16 + 1024, so, u32x4{p[4], p[5], p[6], p[7]});
      float sm = 0.f, sq = 0.f;
#pragma unroll
      for (int e = 0; e < 8; e++) { sm = dot2(p[e], 0x3f803f80u, sm); sq = dot2(p[e], p[e], sq); }
      sm = xhalf_sum(sm); sq = xhalf_sum(sq);
      __builtin_amdgcn_raw_buffer_store_b64(u32x2{__float_as_uint(sm), __float_as_uint(sq)}, ws, hh == 0 ? c * 8 : OOB, WS_ST1 + (tb * 18 + db) * 256, 16);
    };
    unsigned ab[2];
    stage_bases(ldsb, lane, ab);
    mm01c_load(X, ws, voff16, WS_HS + tb * 36864, ab[0], ab[1], acc[0], acc[1]);
    x1_out(acc[0], 2 * c8); x1_out(acc[1], 2 * c8 + 1);
    if (w == (c8 & 3)) { mm0c(X, ab[c8 >> 2] + 2 * ST_B, acc[2]); x1_out(acc[2], xdb); }
  }
  STAMP(14);
  const unsigned vo576 = stage_voff(lane, D * 2), vo4608 = stage_voff(lane, FF * 2);
  const srd_t w3s = mk_srd(P.w3, D * FF * 2), w4s = mk_srd(P.w4, FF * D * 2);
  auto piece = [&](int t, int j) {
    const int p = 9 * w + j;
    const unsigned dst = ldsb + (t & 3) * ST_B + p * 1024;
    if (t < 18) dma(vo4608, w3s, (unsigned)((288 * (t & 1) + 8 * p) * FF + c8 * 576 + 64 * (t >> 1)) * 2u, dst);
    else dma(vo576, w4s, (unsigned)((c8 * 576 + 288 * (t & 1) + 8 * p) * D + 64 * ((t - 18) >> 1)) * 2u, dst);
  };
  BARRIER();
#pragma unroll 1
  for (int j = 0; j < 9; j++) { piece(0, j); piece(1, j); piece(2, j); piece(3, j); }
  WAIT_VM(36);
  BARRIER();
  if (tid == 0) __hip_atomic_store(flags + FL_X1 + tt * 8 + c8, tag, __ATOMIC_RELAXED, __HIP_MEMORY_SCOPE_AGENT);
  STAMP(4);
  {
    const unsigned long long* g = (const unsigned long long*)(P.ws + WS_CS3) + c8 * 576;
    float* csl = (float*)(lds + ML_CS);
    const unsigned long long t0 = __builtin_amdgcn_s_memrealtime();
    for (;;) {
      bool ok = true;
#pragma unroll
      for (int k = 0; k < 3; k++) {
        const int f = tid + 256 * k;
        if (f < 576) { const unsigned long long v = __hip_atomic_load(g + f, __ATOMIC_RELAXED, __HIP_MEMORY_SCOPE_AGENT); ok &= (unsigned)(v >> 32) == tag; csl[f] = __uint_as_float((unsigned)v); }
      }
      if (__all(ok) || __builtin_amdgcn_s_memrealtime() - t0 > 2000000ull) break;
      __builtin_amdgcn_s_sleep(2);
    }
  }
  await_wg(ws, ldsb, FL_X1 + tt * 8 + lane, lane < 8, tag, w);
  STAMP(5);
  {
    unsigned ab[2];
    stage_bases(ldsb, lane, ab);
    unsigned long long dbg[4];
    const unsigned long long w3p = (unsigned long long)P.w3, w4p = (unsigned long long)P.w4;
    const unsigned so3 = (unsigned)((72 * w) * FF + c8 * 576 + 128) * 2u, so4 = (unsigned)((c8 * 576 + 72 * w) * D) * 2u;
    unsigned csa = ldsb + ML_CS + 16 * hh, vo3 = vo4608;
    asm volatile(
    "s_mov_b32 s64, %[sst]\n\t"
    "buffer_load_dwordx2 v[128:129], %[vst], %[wsr], s64 offen offset:0 sc1\n\t"
    "buffer_load_dwordx2 v[130:131], %[vst], %[wsr], s64 offen offset:256 sc1\n\t"
    "buffer_load_dwordx2 v[132:133], %[vst], %[wsr], s64 offen offset:512 sc1\n\t"
    "buffer_load_dwordx2 v[134:135], %[vst], %[wsr], s64 offen offset:768 sc1\n\t"
    "buffer_load_dwordx2 v[136:137], %[vst], %[wsr], s64 offen offset:1024 sc1\n\t"
    "buffer_load_dwordx2 v[138:139], %[vst], %[wsr], s64 offen offset:1280 sc1\n\t"
    "buffer_load_dwordx2 v[140:141], %[vst], %[wsr], s64 offen offset:1536 sc1\n\t"
    "buffer_load_dwordx2 v[142:143], %[vst], %[wsr], s64 offen offset:1792 sc1\n\t"
    "buffer_load_dwordx2 v[144:145], %[vst], %[wsr], s64 offen offset:2048 sc1\n\t"
    "buffer_load_dwordx2 v[146:147], %[vst], %[wsr], s64 offen offset:2304 sc1\n\t"
    "buffer_load_dwordx2 v[148:149], %[vst], %[wsr], s64 offen offset:2560 sc1\n\t"
    "buffer_load_dwordx2 v[150:151], %[vst], %[wsr], s64 offen offset:2816 sc1\n\t"
    "buffer_load_dwordx2 v[152:153], %[vst], %[wsr], s64 offen offset:3072 sc1\n\t"
    "buffer_load_dwordx2 v[154:155], %[vst], %[wsr], s64 offen offset:3328 sc1\n\t"
    "buffer_load_dwordx2 v[156:157], %[vst], %[wsr], s64 offen offset:3584 sc1\n\t"
    "buffer_load_dwordx2 v[158:159], %[vst], %[wsr], s64 offen offset:3840 sc1\n\t"
    "s_add_u32 s64, s64, 0x1000\n\t"
    "buffer_load_dwordx2 v[160:161], %[vst], %[wsr], s64 offen offset:0 sc1\n\t"
    "buffer_load_dwordx2 v[162:163], %[vst], %[wsr], s64 offen offset:256 sc1\n\t"
    "s_mov_b32 s64, %[sx1]\n\t"
    "buffer_load_dwordx4 a[0:3], %[vo16], %[wsr], s64 offen offset:0 sc1\n\t"
    "buffer_load_dwordx4 a[4:7], %[vo16], %[wsr], s64 offen offset:1024 sc1\n\t"
    "buffer_load_dwordx4 a[8:11], %[vo16], %[wsr], s64 offen offset:2048 sc1\n\t"
    "buffer_load_dwordx4 a[12:15], %[vo16], %[wsr], s64 offen offset:3072 sc1\n\t"
    "s_add_u32 s64, s64, 0x1000\n\t"
    "buffer_load_dwordx4 a[16:19], %[vo16], %[wsr], s64 offen offset:0 sc1\n\t"
    "buffer_load_dwordx4 a[20:23], %[vo16], %[wsr], s64 offen offset:1024 sc1\n\t"
    "buffer_load_dwordx4 a[24:27], %[vo16], %[wsr], s64 offen offset:2048 sc1\n\t"
    "buffer_load_dwordx4 a[28:31], %[vo16], %[wsr], s64 offen offset:3072 sc1\n\t"
    "s_add_u32 s64, s64, 0x1000\n\t"
    "buffer_load_dwordx4 a[32:35], %[vo16], %[wsr], s64 offen offset:0 sc1\n\t"
    "buffer_load_dwordx4 a[36:39], %[vo16], %[wsr], s64 offen offset:1024 sc1\n\t"
    "buffer_load_dwordx4 a[40:43], %[vo16], %[wsr], s64 offen offset:2048 sc1\n\t"
    "buffer_load_dwordx4 a[44:47], %[vo16], %[wsr], s64 offen offset:3072 sc1\n\t"
    "s_add_u32 s64, s64, 0x1000\n\t"
    "buffer_load_dwordx4 a[48:51], %[vo16], %[wsr], s64 offen offset:0 sc1\n\t"
    "buffer_load_dwordx4 a[52:55], %[vo16], %[wsr], s64 offen offset:1024 sc1\n\t"
    "buffer_load_dwordx4 a[56:59], %[vo16], %[wsr], s64 offen offset:2048 sc1\n\t"
    "buffer_load_dwordx4 a[60:63], %[vo16], %[wsr], s64 offen offset:3072 sc1\n\t"
    "s_add_u32 s64, s64, 0x1000\n\t"
    "buffer_load_dwordx4 a[64:67], %[vo16], %[wsr], s64 offen offset:0 sc1\n\t"
    "buffer_load_dwordx4 a[68:71], %[vo16], %[wsr], s64 offen offset:1024 sc1\n\t"
    "buffer_load_dwordx4 a[72:75], %[vo16], %[wsr], s64 offen offset:2048 sc1\n\t"
    "buffer_load_dwordx4 a[76:79], %[vo16], %[wsr], s64 offen offset:3072 sc1\n\t"
    "s_add_u32 s64, s64, 0x1000\n\t"
    "buffer_load_dwordx4 a[80:83], %[vo16], %[wsr], s64 offen offset:0 sc1\n\t"
    "buffer_load_dwordx4 a[84:87], %[vo16], %[wsr], s64 offen offset:1024 sc1\n\t"
    "buffer_load_dwordx4 a[88:91], %[vo16], %[wsr], s64 offen offset:2048 sc1\n\t"
    "buffer_load_dwordx4 a[92:95], %[vo16], %[wsr], s64 offen offset:3072 sc1\n\t"
    "s_add_u32 s64, s64, 0x1000\n\t"
    "buffer_load_dwordx4 a[96:99], %[vo16], %[wsr], s64 offen offset:0 sc1\n\t"
    "buffer_load_dwordx4 a[100:103], %[vo16], %[wsr], s64 offen offset:1024 sc1\n\t"
    "buffer_load_dwordx4 a[104:107], %[vo16], %[wsr], s64 offen offset:2048 sc1\n\t"
    "buffer_load_dwordx4 a[108:111], %[vo16], %[wsr], s64 offen offset:3072 sc1\n\t"
    "s_add_u32 s64, s64, 0x1000\n\t"
    "buffer_load_dwordx4 a[112:115], %[vo16], %[wsr], s64 offen offset:0 sc1\n\t"
    "buffer_load_dwordx4 a[116:119], %[vo16], %[wsr], s64 offen offset:1024 sc1\n\t"
    "buffer_load_dwordx4 a[120:123], %[vo16], %[wsr], s64 offen offset:2048 sc1\n\t"
    "buffer_load_dwordx4 a[124:127], %[vo16], %[wsr], s64 offen offset:3072 sc1\n\t"
    "s_add_u32 s64, s64, 0x1000\n\t"
    "buffer_load_dwordx4 a[128:131], %[vo16], %[wsr], s64 offen offset:0 sc1\n\t"
    "buffer_load_dwordx4 a[132:135], %[vo16], %[wsr], s64 offen offset:1024 sc1\n\t"
    "buffer_load_dwordx4 a[136:139], %[vo16], %[wsr], s64 offen offset:2048 sc1\n\t"
    "buffer_load_dwordx4 a[140:143], %[vo16], %[wsr], s64 offen offset:3072 sc1\n\t"
    "v_add_u32 v18, 36864, v16\n\t"
    "v_add_u32 v19, 36864, v17\n\t"
    "v_add_u32 v20, 73728, v16\n\t"
    "v_add_u32 v21, 73728, v17\n\t"
    "v_add_u32 v22, 110592, v16\n\t"
    "v_add_u32 v23, 110592, v17\n\t"
    "s_mov_b32 s60, %[w3a]\n\t"
    "s_mov_b32 s61, %[w3b]\n\t"
    "s_mov_b32 s62, 0x510000\n\t"
    "s_mov_b32 s63, 0x20000\n\t"
    "s_sub_u32 s64, %[so3], 73728\n\t"
    "s_mov_b32 s65, 73728\n\t"
    "s_mov_b32 s66, 1990656\n\t"
    "s_mov_b32 s67, 0xffcd6080\n\t"
    "s_mov_b32 s70, 0x8000\n\t"
    "s_mov_b32 s71, 0\n\t"
    "s_mov_b32 s69, 0\n\t"
    "v_mov_b32 v126, 1.0\n\t"
    "s_waitcnt vmcnt(54) lgkmcnt(0)\n\t"
    "s_barrier\n\t"
    "ds_read_b64_tr_b16 a[144:145], v16 offset:0\n\t"
    "ds_read_b64_tr_b16 a[146:147], v16 offset:1024\n\t"
    "ds_read_b64_tr_b16 a[148:149], v17 offset:0\n\t"
    "ds_read_b64_tr_b16 a[150:151], v17 offset:1024\n\t"
    "ds_read_b64_tr_b16 a[152:153], v16 offset:2048\n\t"
    "ds_read_b64_tr_b16 a[154:155], v16 offset:3072\n\t"
    "ds_read_b64_tr_b16 a[156:157], v17 offset:2048\n\t"
    "ds_read_b64_tr_b16 a[158:159], v17 offset:3072\n\t"
    "ds_read_b64_tr_b16 a[160:161], v16 offset:4096\n\t"
    "ds_read_b64_tr_b16 a[162:163], v16 offset:5120\n\t"
    "ds_read_b64_tr_b16 a[164:165], v17 offset:4096\n\t"
    "ds_read_b64_tr_b16 a[166:167], v17 offset:5120\n\t"
    "ds_read_b64_tr_b16 a[168:169], v16 offset:6144\n\t"
    "ds_read_b64_tr_b16 a[170:171], v16 offset:7168\n\t"
    "ds_read_b64_tr_b16 a[172:173], v17 offset:6144\n\t"
    "ds_read_b64_tr_b16 a[174:175], v17 offset:7168\n\t"
    "s_waitcnt vmcnt(35) lgkmcnt(8)\n\t"
    "v_mfma_f32_32x32x16_bf16 v[56:71], a[144:147], a[0:3], 0\n\t"
    "v_mfma_f32_32x32x16_bf16 v[72:87], a[148:151], a[0:3], 0\n\t"
    "ds_read_b64_tr_b16 a[144:145], v16 offset:8192\n\t"
    "ds_read_b64_tr_b16 a[146:147], v16 offset:9216\n\t"
    "ds_read_b64_tr_b16 a[148:149], v17 offset:8192\n\t"
    "ds_read_b64_tr_b16 a[150:151], v17 offset:9216\n\t"
    "s_waitcnt vmcnt(34)\n\t"
    "v_mfma_f32_32x32x16_bf16 v[56:71], a[152:155], a[4:7], v[56:71]\n\t"
    "v_mfma_f32_32x32x16_bf16 v[72:87], a[156:159], a[4:7], v[72:87]\n\t"
    "ds_read_b64_tr_b16 a[152:153], v16 offset:10240\n\t"
    "ds_read_b64_tr_b16 a[154:155], v16 offset:11264\n\t"
    "ds_read_b64_tr_b16 a[156:157], v17 offset:10240\n\t"
    "ds_read_b64_tr_b16 a[158:159], v17 offset:11264\n\t"
    "s_waitcnt vmcnt(33) lgkmcnt(8)\n\t"
    "v_mfma_f32_32x32x16_bf16 v[56:71], a[160:163], a[8:11], v[56:71]\n\t"
    "v_add_f32 v128, v128, v130\n\t"
    "v_add_f32 v129, v129, v131\n\t"
    "v_add_f32 v128, v128, v132\n\t"
    "v_add_f32 v129, v129, v133\n\t"
    "v_add_f32 v128, v128, v134\n\t"
    "v_mfma_f32_32x32x16_bf16 v[72:87], a[164:167], a[8:11], v[72:87]\n\t"
    "ds_read_b64_tr_b16 a[160:161], v16 offset:12288\n\t"
    "ds_read_b64_tr_b16 a[162:163], v16 offset:13312\n\t"
    "ds_read_b64_tr_b16 a[164:165], v17 offset:12288\n\t"
    "ds_read_b64_tr_b16 a[166:167], v17 offset:13312\n\t"
    "s_waitcnt vmcnt(32)\n\t"
    "v_mfma_f32_32x32x16_bf16 v[56:71], a[168:171], a[12:15], v[56:71]\n\t"
    "v_add_f32 v129, v129, v135\n\t"
    "v_add_f32 v128, v128, v136\n\t"
    "v_add_f32 v129, v129, v137\n\t"
    "v_add_f32 v128, v128, v138\n\t"
    "v_add_f32 v129, v129, v139\n\t"
    "v_mfma_f32_32x32x16_bf16 v[72:87], a[172:175], a[12:15], v[72:87]\n\t"
    "ds_read_b64_tr_b16 a[168:169], v16 offset:14336\n\t"
    "ds_read_b64_tr_b16 a[170:171], v16 offset:15360\n\t"
    "ds_read_b64_tr_b16 a[172:173], v17 offset:14336\n\t"
    "ds_read_b64_tr_b16 a[174:175], v17 offset:15360\n\t"
    "s_waitcnt vmcnt(31) lgkmcnt(8)\n\t"
    "v_mfma_f32_32x32x16_bf16 v[56:71], a[144:147], a[16:19], v[56:71]\n\t"
    "v_add_f32 v128, v128, v140\n\t"
    "v_add_f32 v129, v129, v141\n\t"
    "v_add_f32 v128, v128, v142\n\t"
    "v_add_f32 v129, v129, v143\n\t"
    "v_add_f32 v128, v128, v144\n\t"
    "v_mfma_f32_32x32x16_bf16 v[72:87], a[148:151], a[16:19], v[72:87]\n\t"
    "ds_read_b64_tr_b16 a[144:145], v16 offset:16384\n\t"
    "ds_read_b64_tr_b16 a[146:147], v16 offset:17408\n\t"
    "ds_read_b64_tr_b16 a[148:149], v17 offset:16384\n\t"
    "ds_read_b64_tr_b16 a[150:151], v17 offset:17408\n\t"
    "s_waitcnt vmcnt(30)\n\t"
    "v_mfma_f32_32x32x16_bf16 v[56:71], a[152:155], a[20:23], v[56:71]\n\t"
    "v_add_f32 v129, v129, v145\n\t"
    "v_add_f32 v128, v128, v146\n\t"
    "v_add_f32 v129, v129, v147\n\t"
    "v_add_f32 v128, v128, v148\n\t"
    "v_add_f32 v129, v129, v149\n\t"
    "v_mfma_f32_32x32x16_bf16 v[72:87], a[156:159], a[20:23], v[72:87]\n\t"
    "ds_read_b64_tr_b16 a[152:153], v16 offset:18432\n\t"
    "ds_read_b64_tr_b16 a[154:155], v16 offset:19456\n\t"
    "ds_read_b64_tr_b16 a[156:157], v17 offset:18432\n\t"
    "ds_read_b64_tr_b16 a[158:159], v17 offset:19456\n\t"
    "s_waitcnt vmcnt(29) lgkmcnt(8)\n\t"
    "v_mfma_f32_32x32x16_bf16 v[56:71], a[160:163], a[24:27], v[56:71]\n\t"
    "v_add_f32 v128, v128, v150\n\t"
    "v_add_f32 v129, v129, v151\n\t"
    "v_add_f32 v128, v128, v152\n\t"
    "v_add_f32 v129, v129, v153\n\t"
    "v_add_f32 v128, v128, v154\n\t"
    "v_mfma_f32_32x32x16_bf16 v[72:87], a[164:167], a[24:27], v[72:87]\n\t"
    "ds_read_b64_tr_b16 a[160:161], v16 offset:20480\n\t"
    "ds_read_b64_tr_b16 a[162:163], v16 offset:21504\n\t"
    "ds_read_b64_tr_b16 a[164:165], v17 offset:20480\n\t"
    "ds_read_b64_tr_b16 a[166:167], v17 offset:21504\n\t"
    "s_waitcnt vmcnt(28)\n\t"
    "v_mfma_f32_32x32x16_bf16 v[56:71], a[168:171], a[28:31], v[56:71]\n\t"
    "v_add_f32 v129, v129, v155\n\t"
    "v_add_f32 v128, v128, v156\n\t"
    "v_add_f32 v129, v129, v157\n\t"
    "v_add_f32 v128, v128, v158\n\t"
    "v_add_f32 v129, v129, v159\n\t"
    "v_mfma_f32_32x32x16_bf16 v[72:87], a[172:175], a[28:31], v[72:87]\n\t"
    "ds_read_b64_tr_b16 a[168:169], v16 offset:22528\n\t"
    "ds_read_b64_tr_b16 a[170:171], v16 offset:23552\n\t"
    "ds_read_b64_tr_b16 a[172:173], v17 offset:22528\n\t"
    "ds_read_b64_tr_b16 a[174:175], v17 offset:23552\n\t"
    "s_waitcnt vmcnt(27) lgkmcnt(8)\n\t"
    "v_mfma_f32_32x32x16_bf16 v[56:71], a[144:147], a[32:35], v[56:71]\n\t"
    "v_add_f32 v128, v128, v160\n\t"
    "v_add_f32 v129, v129, v161\n\t"
    "v_add_f32 v128, v128, v162\n\t"
    "v_add_f32 v129, v129, v163\n\t"
    "v_mul_f32 v128, 0x3ae38e39, v128\n\t"
    "v_mfma_f32_32x32x16_bf16 v[72:87], a[148:151], a[32:35], v[72:87]\n\t"
    "ds_read_b64_tr_b16 a[144:145], v16 offset:24576\n\t"
    "ds_read_b64_tr_b16 a[146:147], v16 offset:25600\n\t"
    "ds_read_b64_tr_b16 a[148:149], v17 offset:24576\n\t"
    "ds_read_b64_tr_b16 a[150:151], v17 offset:25600\n\t"
    "s_waitcnt vmcnt(26)\n\t"
    "v_mfma_f32_32x32x16_bf16 v[56:71], a[152:155], a[36:39], v[56:71]\n\t"
    "v_mul_f32 v129, 0x3ae38e39, v129\n\t"
    "v_fma_f32 v129, -v128, v128, v129\n\t"
    "v_max_f32 v129, 0, v129\n\t"
    "v_add_f32 v129, 0x3727c5ac, v129\n\t"
    "v_rsq_f32 v129, v129\n\t"
    "v_mfma_f32_32x32x16_bf16 v[72:87], a[156:159], a[36:39], v[72:87]\n\t"
    "ds_read_b64_tr_b16 a[152:153], v16 offset:26624\n\t"
    "ds_read_b64_tr_b16 a[154:155], v16 offset:27648\n\t"
    "ds_read_b64_tr_b16 a[156:157], v17 offset:26624\n\t"
    "ds_read_b64_tr_b16 a[158:159], v17 offset:27648\n\t"
    "s_waitcnt vmcnt(25) lgkmcnt(8)\n\t"
    "v_mfma_f32_32x32x16_bf16 v[56:71], a[160:163], a[40:43], v[56:71]\n\t"
    "v_sub_f32 v124, 0, v128\n\t"
    "s_nop 0\n\t"
    "v_mul_f32 v120, 0xc0135761, v129\n\t"
    "v_mul_f32 v128, v129, v129\n\t"
    "v_mul_f32 v128, v128, v129\n\t"
    "v_mfma_f32_32x32x16_bf16 v[72:87], a[164:167], a[40:43], v[72:87]\n\t"
    "ds_read_b64_tr_b16 a[160:161], v16 offset:28672\n\t"
    "ds_read_b64_tr_b16 a[162:163], v16 offset:29696\n\t"
    "ds_read_b64_tr_b16 a[164:165], v17 offset:28672\n\t"
    "ds_read_b64_tr_b16 a[166:167], v17 offset:29696\n\t"
    "s_waitcnt vmcnt(24)\n\t"
    "v_mfma_f32_32x32x16_bf16 v[56:71], a[168:171], a[44:47], v[56:71]\n\t"
    "v_mul_f32 v122, 0xbdd2d3e7, v128\n\t"
    "v_mfma_f32_32x32x16_bf16 v[72:87], a[172:175], a[44:47], v[72:87]\n\t"
    "ds_read_b64_tr_b16 a[168:169], v16 offset:30720\n\t"
    "ds_read_b64_tr_b16 a[170:171], v16 offset:31744\n\t"
    "ds_read_b64_tr_b16 a[172:173], v17 offset:30720\n\t"
    "ds_read_b64_tr_b16 a[174:175], v17 offset:31744\n\t"
    "s_waitcnt vmcnt(23) lgkmcnt(8)\n\t"
    "v_mfma_f32_32x32x16_bf16 v[56:71], a[144:147], a[48:51], v[56:71]\n\t"
    "v_mfma_f32_32x32x16_bf16 v[72:87], a[148:151], a[48:51], v[72:87]\n\t"
    "ds_read_b64_tr_b16 a[144:145], v16 offset:32768\n\t"
    "ds_read_b64_tr_b16 a[146:147], v16 offset:33792\n\t"
    "ds_read_b64_tr_b16 a[148:149], v17 offset:32768\n\t"
    "ds_read_b64_tr_b16 a[150:151], v17 offset:33792\n\t"
    "s_waitcnt vmcnt(22)\n\t"
    "v_mfma_f32_32x32x16_bf16 v[56:71], a[152:155], a[52:55], v[56:71]\n\t"
    "v_mfma_f32_32x32x16_bf16 v[72:87], a[156:159], a[52:55], v[72:87]\n\t"
    "ds_read_b64_tr_b16 a[152:153], v16 offset:34816\n\t"
    "ds_read_b64_tr_b16 a[154:155], v16 offset:35840\n\t"
    "ds_read_b64_tr_b16 a[156:157], v17 offset:34816\n\t"
    "ds_read_b64_tr_b16 a[158:159], v17 offset:35840\n\t"
    "s_waitcnt vmcnt(21) lgkmcnt(8)\n\t"
    "v_mfma_f32_32x32x16_bf16 v[56:71], a[160:163], a[56:59], v[56:71]\n\t"
    "v_mfma_f32_32x32x16_bf16 v[72:87], a[164:167], a[56:59], v[72:87]\n\t"
    "ds_read_b64_tr_b16 a[160:161], v18 offset:0\n\t"
    "ds_read_b64_tr_b16 a[162:163], v18 offset:1024\n\t"
    "ds_read_b64_tr_b16 a[164:165], v19 offset:0\n\t"
    "ds_read_b64_tr_b16 a[166:167], v19 offset:1024\n\t"
    "s_waitcnt vmcnt(20)\n\t"
    "v_mfma_f32_32x32x16_bf16 v[56:71], a[168:171], a[60:63], v[56:71]\n\t"
    "v_mfma_f32_32x32x16_bf16 v[72:87], a[172:175], a[60:63], v[72:87]\n\t"
    "ds_read_b64_tr_b16 a[168:169], v18 offset:2048\n\t"
    "ds_read_b64_tr_b16 a[170:171], v18 offset:3072\n\t"
    "ds_read_b64_tr_b16 a[172:173], v19 offset:2048\n\t"
    "ds_read_b64_tr_b16 a[174:175], v19 offset:3072\n\t"
    "s_waitcnt vmcnt(19) lgkmcnt(8)\n\t"
    "v_mfma_f32_32x32x16_bf16 v[56:71], a[144:147], a[64:67], v[56:71]\n\t"
    "v_mfma_f32_32x32x16_bf16 v[72:87], a[148:151], a[64:67], v[72:87]\n\t"
    "ds_read_b64_tr_b16 a[144:145], v18 offset:4096\n\t"
    "ds_read_b64_tr_b16 a[146:147], v18 offset:5120\n\t"
    "ds_read_b64_tr_b16 a[148:149], v19 offset:4096\n\t"
    "ds_read_b64_tr_b16 a[150:151], v19 offset:5120\n\t"
    "s_waitcnt vmcnt(18)\n\t"
    "v_mfma_f32_32x32x16_bf16 v[56:71], a[152:155], a[68:71], v[56:71]\n\t"
    "v_mfma_f32_32x32x16_bf16 v[72:87], a[156:159], a[68:71], v[72:87]\n\t"
    "ds_read_b64_tr_b16 a[152:153], v18 offset:6144\n\t"
    "ds_read_b64_tr_b16 a[154:155], v18 offset:7168\n\t"
    "ds_read_b64_tr_b16 a[156:157], v19 offset:6144\n\t"
    "ds_read_b64_tr_b16 a[158:159], v19 offset:7168\n\t"
    "s_waitcnt vmcnt(17) lgkmcnt(8)\n\t"
    "v_mfma_f32_32x32x16_bf16 v[56:71], a[160:163], a[72:75], v[56:71]\n\t"
    "v_mfma_f32_32x32x16_bf16 v[72:87], a[164:167], a[72:75], v[72:87]\n\t"
    "ds_read_b64_tr_b16 a[160:161], v18 offset:8192\n\t"
    "ds_read_b64_tr_b16 a[162:163], v18 offset:9216\n\t"
    "ds_read_b64_tr_b16 a[164:165], v19 offset:8192\n\t"
    "ds_read_b64_tr_b16 a[166:167], v19 offset:9216\n\t"
    "s_waitcnt vmcnt(16)\n\t"
    "v_mfma_f32_32x32x16_bf16 v[56:71], a[168:171], a[76:79], v[56:71]\n\t"
    "v_mfma_f32_32x32x16_bf16 v[72:87], a[172:175], a[76:79], v[72:87]\n\t"
    "ds_read_b64_tr_b16 a[168:169], v18 offset:10240\n\t"
    "ds_read_b64_tr_b16 a[170:171], v18 offset:11264\n\t"
    "ds_read_b64_tr_b16 a[172:173], v19 offset:10240\n\t"
    "ds_read_b64_tr_b16 a[174:175], v19 offset:11264\n\t"
    "s_waitcnt vmcnt(15) lgkmcnt(8)\n\t"
    "v_mfma_f32_32x32x16_bf16 v[56:71], a[144:147], a[80:83], v[56:71]\n\t"
    "buffer_store_dword v129, %[vrs], %[wsr], 0 offen sc1\n\t"
    "v_mfma_f32_32x32x16_bf16 v[72:87], a[148:151], a[80:83], v[72:87]\n\t"
    "ds_read_b64_tr_b16 a[144:145], v18 offset:12288\n\t"
    "ds_read_b64_tr_b16 a[146:147], v18 offset:13312\n\t"
    "ds_read_b64_tr_b16 a[148:149], v19 offset:12288\n\t"
    "ds_read_b64_tr_b16 a[150:151], v19 offset:13312\n\t"
    "s_waitcnt vmcnt(14)\n\t"
    "v_mfma_f32_32x32x16_bf16 v[56:71], a[152:155], a[84:87], v[56:71]\n\t"
    "v_mfma_f32_32x32x16_bf16 v[72:87], a[156:159], a[84:87], v[72:87]\n\t"
    "ds_read_b64_tr_b16 a[152:153], v18 offset:14336\n\t"
    "ds_read_b64_tr_b16 a[154:155], v18 offset:15360\n\t"
    "ds_read_b64_tr_b16 a[156:157], v19 offset:14336\n\t"
    "ds_read_b64_tr_b16 a[158:159], v19 offset:15360\n\t"
    "s_waitcnt vmcnt(13) lgkmcnt(8)\n\t"
    "v_mfma_f32_32x32x16_bf16 v[56:71], a[160:163], a[88:91], v[56:71]\n\t"
    "v_mfma_f32_32x32x16_bf16 v[72:87], a[164:167], a[88:91], v[72:87]\n\t"
    "ds_read_b64_tr_b16 a[160:161], v18 offset:16384\n\t"
    "ds_read_b64_tr_b16 a[162:163], v18 offset:17408\n\t"
    "ds_read_b64_tr_b16 a[164:165], v19 offset:16384\n\t"
    "ds_read_b64_tr_b16 a[166:167], v19 offset:17408\n\t"
    "s_waitcnt vmcnt(12)\n\t"
    "v_mfma_f32_32x32x16_bf16 v[56:71], a[168:171], a[92:95], v[56:71]\n\t"
    "v_mfma_f32_32x32x16_bf16 v[72:87], a[172:175], a[92:95], v[72:87]\n\t"
    "ds_read_b64_tr_b16 a[168:169], v18 offset:18432\n\t"
    "ds_read_b64_tr_b16 a[170:171], v18 offset:19456\n\t"
    "ds_read_b64_tr_b16 a[172:173], v19 offset:18432\n\t"
    "ds_read_b64_tr_b16 a[174:175], v19 offset:19456\n\t"
    "s_waitcnt vmcnt(11) lgkmcnt(8)\n\t"
    "v_mfma_f32_32x32x16_bf16 v[56:71], a[144:147], a[96:99], v[56:71]\n\t"
    "v_mfma_f32_32x32x16_bf16 v[72:87], a[148:151], a[96:99], v[72:87]\n\t"
    "ds_read_b64_tr_b16 a[144:145], v18 offset:20480\n\t"
    "ds_read_b64_tr_b16 a[146:147], v18 offset:21504\n\t"
    "ds_read_b64_tr_b16 a[148:149], v19 offset:20480\n\t"
    "ds_read_b64_tr_b16 a[150:151], v19 offset:21504\n\t"
    "s_waitcnt vmcnt(10)\n\t"
    "v_mfma_f32_32x32x16_bf16 v[56:71], a[152:155], a[100:103], v[56:71]\n\t"
    "v_mfma_f32_32x32x16_bf16 v[72:87], a[156:159], a[100:103], v[72:87]\n\t"
    "ds_read_b64_tr_b16 a[152:153], v18 offset:22528\n\t"
    "ds_read_b64_tr_b16 a[154:155], v18 offset:23552\n\t"
    "ds_read_b64_tr_b16 a[156:157], v19 offset:22528\n\t"
    "ds_read_b64_tr_b16 a[158:159], v19 offset:23552\n\t"
    "s_waitcnt vmcnt(9) lgkmcnt(8)\n\t"
    "v_mfma_f32_32x32x16_bf16 v[56:71], a[160:163], a[104:107], v[56:71]\n\t"
    "v_mfma_f32_32x32x16_bf16 v[72:87], a[164:167], a[104:107], v[72:87]\n\t"
    "ds_read_b64_tr_b16 a[160:161], v18 offset:24576\n\t"
    "ds_read_b64_tr_b16 a[162:163], v18 offset:25600\n\t"
    "ds_read_b64_tr_b16 a[164:165], v19 offset:24576\n\t"
    "ds_read_b64_tr_b16 a[166:167], v19 offset:25600\n\t"
    "s_waitcnt vmcnt(8)\n\t"
    "v_mfma_f32_32x32x16_bf16 v[56:71], a[168:171], a[108:111], v[56:71]\n\t"
    "v_mfma_f32_32x32x16_bf16 v[72:87], a[172:175], a[108:111], v[72:87]\n\t"
    "ds_read_b64_tr_b16 a[168:169], v18 offset:26624\n\t"
    "ds_read_b64_tr_b16 a[170:171], v18 offset:27648\n\t"
    "ds_read_b64_tr_b16 a[172:173], v19 offset:26624\n\t"
    "ds_read_b64_tr_b16 a[174:175], v19 offset:27648\n\t"
    "s_waitcnt vmcnt(7) lgkmcnt(8)\n\t"
    "v_mfma_f32_32x32x16_bf16 v[56:71], a[144:147], a[112:115], v[56:71]\n\t"
    "v_mfma_f32_32x32x16_bf16 v[72:87], a[148:151], a[112:115], v[72:87]\n\t"
    "ds_read_b64_tr_b16 a[144:145], v18 offset:28672\n\t"
    "ds_read_b64_tr_b16 a[146:147], v18 offset:29696\n\t"
    "ds_read_b64_tr_b16 a[148:149], v19 offset:28672\n\t"
    "ds_read_b64_tr_b16 a[150:151], v19 offset:29696\n\t"
    "s_waitcnt vmcnt(6)\n\t"
    "v_mfma_f32_32x32x16_bf16 v[56:71], a[152:155], a[116:119], v[56:71]\n\t"
    "v_mfma_f32_32x32x16_bf16 v[72:87], a[156:159], a[116:119], v[72:87]\n\t"
    "ds_read_b64_tr_b16 a[152:153], v18 offset:30720\n\t"
    "ds_read_b64_tr_b16 a[154:155], v18 offset:31744\n\t"
    "ds_read_b64_tr_b16 a[156:157], v19 offset:30720\n\t"
    "ds_read_b64_tr_b16 a[158:159], v19 offset:31744\n\t"
    "s_waitcnt vmcnt(5) lgkmcnt(8)\n\t"
    "v_mfma_f32_32x32x16_bf16 v[56:71], a[160:163], a[120:123], v[56:71]\n\t"
    "ds_read_b128 v[24:27], v12 offset:0\n\t"
    "ds_read_b128 v[28:31], v12 offset:32\n\t"
    "ds_read_b128 v[32:35], v12 offset:64\n\t"
    "ds_read_b128 v[36:39], v12 offset:96\n\t"
    "v_mfma_f32_32x32x16_bf16 v[72:87], a[164:167], a[120:123], v[72:87]\n\t"
    "ds_read_b64_tr_b16 a[160:161], v18 offset:32768\n\t"
    "ds_read_b64_tr_b16 a[162:163], v18 offset:33792\n\t"
    "ds_read_b64_tr_b16 a[164:165], v19 offset:32768\n\t"
    "ds_read_b64_tr_b16 a[166:167], v19 offset:33792\n\t"
    "s_waitcnt vmcnt(4)\n\t"
    "v_mfma_f32_32x32x16_bf16 v[56:71], a[168:171], a[124:127], v[56:71]\n\t"
    "ds_read_b128 v[40:43], v12 offset:128\n\t"
    "ds_read_b128 v[44:47], v12 offset:160\n\t"
    "ds_read_b128 v[48:51], v12 offset:192\n\t"
    "ds_read_b128 v[52:55], v12 offset:224\n\t"
    "v_mfma_f32_32x32x16_bf16 v[72:87], a[172:175], a[124:127], v[72:87]\n\t"
    "ds_read_b64_tr_b16 a[168:169], v18 offset:34816\n\t"
    "ds_read_b64_tr_b16 a[170:171], v18 offset:35840\n\t"
    "ds_read_b64_tr_b16 a[172:173], v19 offset:34816\n\t"
    "ds_read_b64_tr_b16 a[174:175], v19 offset:35840\n\t"
    "s_waitcnt vmcnt(3) lgkmcnt(8)\n\t"
    "v_mfma_f32_32x32x16_bf16 v[56:71], a[144:147], a[128:131], v[56:71]\n\t"
    "s_waitcnt vmcnt(0) lgkmcnt(0)\n\t"
    "s_barrier\n\t"
    "v_mfma_f32_32x32x16_bf16 v[72:87], a[148:151], a[128:131], v[72:87]\n\t"
    "ds_read_b64_tr_b16 a[144:145], v20 offset:0\n\t"
    "ds_read_b64_tr_b16 a[146:147], v20 offset:1024\n\t"
    "ds_read_b64_tr_b16 a[148:149], v21 offset:0\n\t"
    "ds_read_b64_tr_b16 a[150:151], v21 offset:1024\n\t"
    "s_waitcnt vmcnt(2)\n\t"
    "v_mfma_f32_32x32x16_bf16 v[56:71], a[152:155], a[132:135], v[56:71]\n\t"
    "s_add_u32 m0, %[m0b], 0\n\t"
    "s_add_u32 s64, s64, s65\n\t"
    "buffer_load_dwordx4 v13, s[60:63], s64 offen lds\n\t"
    "v_mfma_f32_32x32x16_bf16 v[72:87], a[156:159], a[132:135], v[72:87]\n\t"
    "ds_read_b64_tr_b16 a[152:153], v20 offset:2048\n\t"
    "ds_read_b64_tr_b16 a[154:155], v20 offset:3072\n\t"
    "ds_read_b64_tr_b16 a[156:157], v21 offset:2048\n\t"
    "ds_read_b64_tr_b16 a[158:159], v21 offset:3072\n\t"
    "s_waitcnt vmcnt(1) lgkmcnt(8)\n\t"
    "v_mfma_f32_32x32x16_bf16 v[56:71], a[160:163], a[136:139], v[56:71]\n\t"
    "s_add_u32 m0, %[m0b], 1024\n\t"
    "s_add_u32 s64, s64, s65\n\t"
    "buffer_load_dwordx4 v13, s[60:63], s64 offen lds\n\t"
    "v_mfma_f32_32x32x16_bf16 v[72:87], a[164:167], a[136:139], v[72:87]\n\t"
    "ds_read_b64_tr_b16 a[160:161], v20 offset:4096\n\t"
    "ds_read_b64_tr_b16 a[162:163], v20 offset:5120\n\t"
    "ds_read_b64_tr_b16 a[164:165], v21 offset:4096\n\t"
    "ds_read_b64_tr_b16 a[166:167], v21 offset:5120\n\t"
    "s_waitcnt vmcnt(0)\n\t"
    "v_mfma_f32_32x32x16_bf16 v[56:71], a[168:171], a[140:143], v[56:71]\n\t"
    "s_add_u32 m0, %[m0b], 2048\n\t"
    "s_add_u32 s64, s64, s65\n\t"
    "buffer_load_dwordx4 v13, s[60:63], s64 offen lds\n\t"
    "v_mfma_f32_32x32x16_bf16 v[72:87], a[172:175], a[140:143], v[72:87]\n\t"
    "s_nop 15\n\t"
    "s_nop 3\n\t"
    "v_pk_fma_f32 v[56:57], v[124:125], v[24:25], v[56:57] op_sel_hi:[0,1,1]\n\t"
    "v_pk_fma_f32 v[58:59], v[124:125], v[26:27], v[58:59] op_sel_hi:[0,1,1]\n\t"
    "v_pk_fma_f32 v[60:61], v[124:125], v[28:29], v[60:61] op_sel_hi:[0,1,1]\n\t"
    "v_pk_fma_f32 v[62:63], v[124:125], v[30:31], v[62:63] op_sel_hi:[0,1,1]\n\t"
    "v_pk_fma_f32 v[64:65], v[124:125], v[32:33], v[64:65] op_sel_hi:[0,1,1]\n\t"
    "v_pk_fma_f32 v[66:67], v[124:125], v[34:35], v[66:67] op_sel_hi:[0,1,1]\n\t"
    "v_pk_fma_f32 v[68:69], v[124:125], v[36:37], v[68:69] op_sel_hi:[0,1,1]\n\t"
    "v_pk_fma_f32 v[70:71], v[124:125], v[38:39], v[70:71] op_sel_hi:[0,1,1]\n\t"
    "v_pk_fma_f32 v[72:73], v[124:125], v[40:41], v[72:73] op_sel_hi:[0,1,1]\n\t"
    "v_pk_fma_f32 v[74:75], v[124:125], v[42:43], v[74:75] op_sel_hi:[0,1,1]\n\t"
    "v_pk_fma_f32 v[76:77], v[124:125], v[44:45], v[76:77] op_sel_hi:[0,1,1]\n\t"
    "v_pk_fma_f32 v[78:79], v[124:125], v[46:47], v[78:79] op_sel_hi:[0,1,1]\n\t"
    "v_pk_fma_f32 v[80:81], v[124:125], v[48:49], v[80:81] op_sel_hi:[0,1,1]\n\t"
    "v_pk_fma_f32 v[82:83], v[124:125], v[50:51], v[82:83] op_sel_hi:[0,1,1]\n\t"
    "v_pk_fma_f32 v[84:85], v[124:125], v[52:53], v[84:85] op_sel_hi:[0,1,1]\n\t"
    "v_pk_fma_f32 v[86:87], v[124:125], v[54:55], v[86:87] op_sel_hi:[0,1,1]\n\t"
    "v_pk_mul_f32 v[24:25], v[56:57], v[56:57]\n\t"
    "v_pk_mul_f32 v[26:27], v[58:59], v[58:59]\n\t"
    "v_pk_mul_f32 v[28:29], v[60:61], v[60:61]\n\t"
    "v_pk_mul_f32 v[30:31], v[62:63], v[62:63]\n\t"
    "v_pk_mul_f32 v[32:33], v[64:65], v[64:65]\n\t"
    "v_pk_mul_f32 v[34:35], v[66:67], v[66:67]\n\t"
    "v_pk_mul_f32 v[36:37], v[68:69], v[68:69]\n\t"
    "v_pk_mul_f32 v[38:39], v[70:71], v[70:71]\n\t"
    "v_pk_mul_f32 v[40:41], v[72:73], v[72:73]\n\t"
    "v_pk_mul_f32 v[42:43], v[74:75], v[74:75]\n\t"
    "v_pk_mul_f32 v[44:45], v[76:77], v[76:77]\n\t"
    "v_pk_mul_f32 v[46:47], v[78:79], v[78:79]\n\t"
    "v_pk_mul_f32 v[48:49], v[80:81], v[80:81]\n\t"
    "v_pk_mul_f32 v[50:51], v[82:83], v[82:83]\n\t"
    "v_pk_mul_f32 v[52:53], v[84:85], v[84:85]\n\t"
    "v_pk_mul_f32 v[54:55], v[86:87], v[86:87]\n\t"
    "v_pk_fma_f32 v[24:25], v[24:25], v[122:123], v[120:121] op_sel_hi:[1,0,0]\n\t"
    "v_pk_fma_f32 v[26:27], v[26:27], v[122:123], v[120:121] op_sel_hi:[1,0,0]\n\t"
    "v_pk_fma_f32 v[28:29], v[28:29], v[122:123], v[120:121] op_sel_hi:[1,0,0]\n\t"
    "v_pk_fma_f32 v[30:31], v[30:31], v[122:123], v[120:121] op_sel_hi:[1,0,0]\n\t"
    "v_pk_fma_f32 v[32:33], v[32:33], v[122:123], v[120:121] op_sel_hi:[1,0,0]\n\t"
    "v_pk_fma_f32 v[34:35], v[34:35], v[122:123], v[120:121] op_sel_hi:[1,0,0]\n\t"
    "v_pk_fma_f32 v[36:37], v[36:37], v[122:123], v[120:121] op_sel_hi:[1,0,0]\n\t"
    "v_pk_fma_f32 v[38:39], v[38:39], v[122:123], v[120:121] op_sel_hi:[1,0,0]\n\t"
    "v_pk_fma_f32 v[40:41], v[40:41], v[122:123], v[120:121] op_sel_hi:[1,0,0]\n\t"
    "v_pk_fma_f32 v[42:43], v[42:43], v[122:123], v[120:121] op_sel_hi:[1,0,0]\n\t"
    "v_pk_fma_f32 v[44:45], v[44:45], v[122:123], v[120:121] op_sel_hi:[1,0,0]\n\t"
    "v_pk_fma_f32 v[46:47], v[46:47], v[122:123], v[120:121] op_sel_hi:[1,0,0]\n\t"
    "v_pk_fma_f32 v[48:49], v[48:49], v[122:123], v[120:121] op_sel_hi:[1,0,0]\n\t"
    "v_pk_fma_f32 v[50:51], v[50:51], v[122:123], v[120:121] op_sel_hi:[1,0,0]\n\t"
    "v_pk_fma_f32 v[52:53], v[52:53], v[122:123], v[120:121] op_sel_hi:[1,0,0]\n\t"
    "v_pk_fma_f32 v[54:55], v[54:55], v[122:123], v[120:121] op_sel_hi:[1,0,0]\n\t"
    "v_pk_mul_f32 v[24:25], v[56:57], v[24:25]\n\t"
    "v_pk_mul_f32 v[26:27], v[58:59], v[26:27]\n\t"
    "v_pk_mul_f32 v[28:29], v[60:61], v[28:29]\n\t"
    "v_pk_mul_f32 v[30:31], v[62:63], v[30:31]\n\t"
    "v_pk_mul_f32 v[32:33], v[64:65], v[32:33]\n\t"
    "v_pk_mul_f32 v[34:35], v[66:67], v[34:35]\n\t"
    "v_pk_mul_f32 v[36:37], v[68:69], v[36:37]\n\t"
    "v_pk_mul_f32 v[38:39], v[70:71], v[38:39]\n\t"
    "v_pk_mul_f32 v[40:41], v[72:73], v[40:41]\n\t"
    "v_pk_mul_f32 v[42:43], v[74:75], v[42:43]\n\t"
    "v_pk_mul_f32 v[44:45], v[76:77], v[44:45]\n\t"
    "v_pk_mul_f32 v[46:47], v[78:79], v[46:47]\n\t"
    "v_pk_mul_f32 v[48:49], v[80:81], v[48:49]\n\t"
    "v_pk_mul_f32 v[50:51], v[82:83], v[50:51]\n\t"
    "v_pk_mul_f32 v[52:53], v[84:85], v[52:53]\n\t"
    "v_pk_mul_f32 v[54:55], v[86:87], v[54:55]\n\t"
    "ds_read_b64_tr_b16 a[168:169], v20 offset:6144\n\t"
    "ds_read_b64_tr_b16 a[170:171], v20 offset:7168\n\t"
    "ds_read_b64_tr_b16 a[172:173], v21 offset:6144\n\t"
    "ds_read_b64_tr_b16 a[174:175], v21 offset:7168\n\t"
    "s_waitcnt lgkmcnt(8)\n\t"
    "v_mfma_f32_32x32x16_bf16 v[88:103], a[144:147], a[0:3], 0\n\t"
    "s_add_u32 m0, %[m0b], 3072\n\t"
    "s_add_u32 s64, s64, s65\n\t"
    "buffer_load_dwordx4 v13, s[60:63], s64 offen lds\n\t"
    "v_exp_f32 v24, v24\n\t"
    "v_mfma_f32_32x32x16_bf16 v[104:119], a[148:151], a[0:3], 0\n\t"
    "ds_read_b64_tr_b16 a[144:145], v20 offset:8192\n\t"
    "ds_read_b64_tr_b16 a[146:147], v20 offset:9216\n\t"
    "ds_read_b64_tr_b16 a[148:149], v21 offset:8192\n\t"
    "ds_read_b64_tr_b16 a[150:151], v21 offset:9216\n\t"
    "v_mfma_f32_32x32x16_bf16 v[88:103], a[152:155], a[4:7], v[88:103]\n\t"
    "s_add_u32 m0, %[m0b], 4096\n\t"
    "s_add_u32 s64, s64, s65\n\t"
    "buffer_load_dwordx4 v13, s[60:63], s64 offen lds\n\t"
    "v_exp_f32 v25, v25\n\t"
    "v_mfma_f32_32x32x16_bf16 v[104:119], a[156:159], a[4:7], v[104:119]\n\t"
    "ds_read_b64_tr_b16 a[152:153], v20 offset:10240\n\t"
    "ds_read_b64_tr_b16 a[154:155], v20 offset:11264\n\t"
    "ds_read_b64_tr_b16 a[156:157], v21 offset:10240\n\t"
    "ds_read_b64_tr_b16 a[158:159], v21 offset:11264\n\t"
    "s_waitcnt lgkmcnt(8)\n\t"
    "v_mfma_f32_32x32x16_bf16 v[88:103], a[160:163], a[8:11], v[88:103]\n\t"
    "s_add_u32 m0, %[m0b], 5120\n\t"
    "s_add_u32 s64, s64, s65\n\t"
    "buffer_load_dwordx4 v13, s[60:63], s64 offen lds\n\t"
    "v_exp_f32 v26, v26\n\t"
    "v_mfma_f32_32x32x16_bf16 v[104:119], a[164:167], a[8:11], v[104:119]\n\t"
    "ds_read_b64_tr_b16 a[160:161], v20 offset:12288\n\t"
    "ds_read_b64_tr_b16 a[162:163], v20 offset:13312\n\t"
    "ds_read_b64_tr_b16 a[164:165], v21 offset:12288\n\t"
    "ds_read_b64_tr_b16 a[166:167], v21 offset:13312\n\t"
    "v_mfma_f32_32x32x16_bf16 v[88:103], a[168:171], a[12:15], v[88:103]\n\t"
    "s_add_u32 m0, %[m0b], 6144\n\t"
    "s_add_u32 s64, s64, s65\n\t"
    "buffer_load_dwordx4 v13, s[60:63], s64 offen lds\n\t"
    "v_exp_f32 v27, v27\n\t"
    "v_mfma_f32_32x32x16_bf16 v[104:119], a[172:175], a[12:15], v[104:119]\n\t"
    "ds_read_b64_tr_b16 a[168:169], v20 offset:14336\n\t"
    "ds_read_b64_tr_b16 a[170:171], v20 offset:15360\n\t"
    "ds_read_b64_tr_b16 a[172:173], v21 offset:14336\n\t"
    "ds_read_b64_tr_b16 a[174:175], v21 offset:15360\n\t"
    "s_waitcnt lgkmcnt(8)\n\t"
    "v_mfma_f32_32x32x16_bf16 v[88:103], a[144:147], a[16:19], v[88:103]\n\t"
    "s_add_u32 m0, %[m0b], 7168\n\t"
    "s_add_u32 s64, s64, s65\n\t"
    "buffer_load_dwordx4 v13, s[60:63], s64 offen lds\n\t"
    "v_exp_f32 v28, v28\n\t"
    "v_mfma_f32_32x32x16_bf16 v[104:119], a[148:151], a[16:19], v[104:119]\n\t"
    "ds_read_b64_tr_b16 a[144:145], v20 offset:16384\n\t"
    "ds_read_b64_tr_b16 a[146:147], v20 offset:17408\n\t"
    "ds_read_b64_tr_b16 a[148:149], v21 offset:16384\n\t"
    "ds_read_b64_tr_b16 a[150:151], v21 offset:17408\n\t"
    "v_mfma_f32_32x32x16_bf16 v[88:103], a[152:155], a[20:23], v[88:103]\n\t"
    "s_add_u32 m0, %[m0b], 8192\n\t"
    "s_add_u32 s64, s64, s65\n\t"
    "buffer_load_dwordx4 v13, s[60:63], s64 offen lds\n\t"
    "s_add_u32 s64, s64, s66\n\t"
    "v_mfma_f32_32x32x16_bf16 v[104:119], a[156:159], a[20:23], v[104:119]\n\t"
    "ds_read_b64_tr_b16 a[152:153], v20 offset:18432\n\t"
    "ds_read_b64_tr_b16 a[154:155], v20 offset:19456\n\t"
    "ds_read_b64_tr_b16 a[156:157], v21 offset:18432\n\t"
    "ds_read_b64_tr_b16 a[158:159], v21 offset:19456\n\t"
    "s_waitcnt lgkmcnt(8)\n\t"
    "v_mfma_f32_32x32x16_bf16 v[88:103], a[160:163], a[24:27], v[88:103]\n\t"
    "s_add_u32 m0, %[m0b], 36864\n\t"
    "s_add_u32 s64, s64, s65\n\t"
    "buffer_load_dwordx4 v13, s[60:63], s64 offen lds\n\t"
    "v_exp_f32 v29, v29\n\t"
    "v_mfma_f32_32x32x16_bf16 v[104:119], a[164:167], a[24:27], v[104:119]\n\t"
    "ds_read_b64_tr_b16 a[160:161], v20 offset:20480\n\t"
    "ds_read_b64_tr_b16 a[162:163], v20 offset:21504\n\t"
    "ds_read_b64_tr_b16 a[164:165], v21 offset:20480\n\t"
    "ds_read_b64_tr_b16 a[166:167], v21 offset:21504\n\t"
    "v_mfma_f32_32x32x16_bf16 v[88:103], a[168:171], a[28:31], v[88:103]\n\t"
    "s_add_u32 m0, %[m0b], 37888\n\t"
    "s_add_u32 s64, s64, s65\n\t"
    "buffer_load_dwordx4 v13, s[60:63], s64 offen lds\n\t"
    "v_exp_f32 v30, v30\n\t"
    "v_mfma_f32_32x32x16_bf16 v[104:119], a[172:175], a[28:31], v[104:119]\n\t"
    "ds_read_b64_tr_b16 a[168:169], v20 offset:22528\n\t"
    "ds_read_b64_tr_b16 a[170:171], v20 offset:23552\n\t"
    "ds_read_b64_tr_b16 a[172:173], v21 offset:22528\n\t"
    "ds_read_b64_tr_b16 a[174:175], v21 offset:23552\n\t"
    "s_waitcnt lgkmcnt(8)\n\t"
    "v_mfma_f32_32x32x16_bf16 v[88:103], a[144:147], a[32:35], v[88:103]\n\t"
    "s_add_u32 m0, %[m0b], 38912\n\t"
    "s_add_u32 s64, s64, s65\n\t"
    "buffer_load_dwordx4 v13, s[60:63], s64 offen lds\n\t"
    "v_exp_f32 v31, v31\n\t"
    "v_mfma_f32_32x32x16_bf16 v[104:119], a[148:151], a[32:35], v[104:119]\n\t"
    "ds_read_b64_tr_b16 a[144:145], v20 offset:24576\n\t"
    "ds_read_b64_tr_b16 a[146:147], v20 offset:25600\n\t"
    "ds_read_b64_tr_b16 a[148:149], v21 offset:24576\n\t"
    "ds_read_b64_tr_b16 a[150:151], v21 offset:25600\n\t"
    "v_mfma_f32_32x32x16_bf16 v[88:103], a[152:155], a[36:39], v[88:103]\n\t"
    "s_add_u32 m0, %[m0b], 39936\n\t"
    "s_add_u32 s64, s64, s65\n\t"
    "buffer_load_dwordx4 v13, s[60:63], s64 offen lds\n\t"
    "v_exp_f32 v32, v32\n\t"
    "v_mfma_f32_32x32x16_bf16 v[104:119], a[156:159], a[36:39], v[104:119]\n\t"
    "ds_read_b64_tr_b16 a[152:153], v20 offset:26624\n\t"
    "ds_read_b64_tr_b16 a[154:155], v20 offset:27648\n\t"
    "ds_read_b64_tr_b16 a[156:157], v21 offset:26624\n\t"
    "ds_read_b64_tr_b16 a[158:159], v21 offset:27648\n\t"
    "s_waitcnt lgkmcnt(8)\n\t"
    "v_mfma_f32_32x32x16_bf16 v[88:103], a[160:163], a[40:43], v[88:103]\n\t"
    "s_add_u32 m0, %[m0b], 40960\n\t"
    "s_add_u32 s64, s64, s65\n\t"
    "buffer_load_dwordx4 v13, s[60:63], s64 offen lds\n\t"
    "v_exp_f32 v33, v33\n\t"
    "v_mfma_f32_32x32x16_bf16 v[104:119], a[164:167], a[40:43], v[104:119]\n\t"
    "ds_read_b64_tr_b16 a[160:161], v20 offset:28672\n\t"
    "ds_read_b64_tr_b16 a[162:163], v20 offset:29696\n\t"
    "ds_read_b64_tr_b16 a[164:165], v21 offset:28672\n\t"
    "ds_read_b64_tr_b16 a[166:167], v21 offset:29696\n\t"
    "v_mfma_f32_32x32x16_bf16 v[88:103], a[168:171], a[44:47], v[88:103]\n\t"
    "s_add_u32 m0, %[m0b], 41984\n\t"
    "s_add_u32 s64, s64, s65\n\t"
    "buffer_load_dwordx4 v13, s[60:63], s64 offen lds\n\t"
    "v_exp_f32 v34, v34\n\t"
    "v_mfma_f32_32x32x16_bf16 v[104:119], a[172:175], a[44:47], v[104:119]\n\t"
    "ds_read_b64_tr_b16 a[168:169], v20 offset:30720\n\t"
    "ds_read_b64_tr_b16 a[170:171], v20 offset:31744\n\t"
    "ds_read_b64_tr_b16 a[172:173], v21 offset:30720\n\t"
    "ds_read_b64_tr_b16 a[174:175], v21 offset:31744\n\t"
    "s_waitcnt lgkmcnt(8)\n\t"
    "v_mfma_f32_32x32x16_bf16 v[88:103], a[144:147], a[48:51], v[88:103]\n\t"
    "s_add_u32 m0, %[m0b], 43008\n\t"
    "s_add_u32 s64, s64, s65\n\t"
    "buffer_load_dwordx4 v13, s[60:63], s64 offen lds\n\t"
    "v_exp_f32 v35, v35\n\t"
    "v_mfma_f32_32x32x16_bf16 v[104:119], a[148:151], a[48:51], v[104:119]\n\t"
    "ds_read_b64_tr_b16 a[144:145], v20 offset:32768\n\t"
    "ds_read_b64_tr_b16 a[146:147], v20 offset:33792\n\t"
    "ds_read_b64_tr_b16 a[148:149], v21 offset:32768\n\t"
    "ds_read_b64_tr_b16 a[150:151], v21 offset:33792\n\t"
    "v_mfma_f32_32x32x16_bf16 v[88:103], a[152:155], a[52:55], v[88:103]\n\t"
    "s_add_u32 m0, %[m0b], 44032\n\t"
    "s_add_u32 s64, s64, s65\n\t"
    "buffer_load_dwordx4 v13, s[60:63], s64 offen lds\n\t"
    "v_exp_f32 v36, v36\n\t"
    "v_mfma_f32_32x32x16_bf16 v[104:119], a[156:159], a[52:55], v[104:119]\n\t"
    "ds_read_b64_tr_b16 a[152:153], v20 offset:34816\n\t"
    "ds_read_b64_tr_b16 a[154:155], v20 offset:35840\n\t"
    "ds_read_b64_tr_b16 a[156:157], v21 offset:34816\n\t"
    "ds_read_b64_tr_b16 a[158:159], v21 offset:35840\n\t"
    "s_waitcnt lgkmcnt(8)\n\t"
    "v_mfma_f32_32x32x16_bf16 v[88:103], a[160:163], a[56:59], v[88:103]\n\t"
    "s_add_u32 m0, %[m0b], 45056\n\t"
    "s_add_u32 s64, s64, s65\n\t"
    "buffer_load_dwordx4 v13, s[60:63], s64 offen lds\n\t"
    "s_add_u32 s64, s64, s67\n\t"
    "v_mfma_f32_32x32x16_bf16 v[104:119], a[164:167], a[56:59], v[104:119]\n\t"
    "ds_read_b64_tr_b16 a[160:161], v22 offset:0\n\t"
    "ds_read_b64_tr_b16 a[162:163], v22 offset:1024\n\t"
    "ds_read_b64_tr_b16 a[164:165], v23 offset:0\n\t"
    "ds_read_b64_tr_b16 a[166:167], v23 offset:1024\n\t"
    "v_mfma_f32_32x32x16_bf16 v[88:103], a[168:171], a[60:63], v[88:103]\n\t"
    "v_exp_f32 v37, v37\n\t"
    "v_exp_f32 v38, v38\n\t"
    "v_exp_f32 v39, v39\n\t"
    "v_mfma_f32_32x32x16_bf16 v[104:119], a[172:175], a[60:63], v[104:119]\n\t"
    "ds_read_b64_tr_b16 a[168:169], v22 offset:2048\n\t"
    "ds_read_b64_tr_b16 a[170:171], v22 offset:3072\n\t"
    "ds_read_b64_tr_b16 a[172:173], v23 offset:2048\n\t"
    "ds_read_b64_tr_b16 a[174:175], v23 offset:3072\n\t"
    "s_waitcnt lgkmcnt(8)\n\t"
    "v_mfma_f32_32x32x16_bf16 v[88:103], a[144:147], a[64:67], v[88:103]\n\t"
    "v_exp_f32 v40, v40\n\t"
    "v_exp_f32 v41, v41\n\t"
    "v_exp_f32 v42, v42\n\t"
    "v_mfma_f32_32x32x16_bf16 v[104:119], a[148:151], a[64:67], v[104:119]\n\t"
    "ds_read_b64_tr_b16 a[144:145], v22 offset:4096\n\t"
    "ds_read_b64_tr_b16 a[146:147], v22 offset:5120\n\t"
    "ds_read_b64_tr_b16 a[148:149], v23 offset:4096\n\t"
    "ds_read_b64_tr_b16 a[150:151], v23 offset:5120\n\t"
    "v_mfma_f32_32x32x16_bf16 v[88:103], a[152:155], a[68:71], v[88:103]\n\t"
    "v_exp_f32 v43, v43\n\t"
    "v_exp_f32 v44, v44\n\t"
    "v_exp_f32 v45, v45\n\t"
    "v_mfma_f32_32x32x16_bf16 v[104:119], a[156:159], a[68:71], v[104:119]\n\t"
    "ds_read_b64_tr_b16 a[152:153], v22 offset:6144\n\t"
    "ds_read_b64_tr_b16 a[154:155], v22 offset:7168\n\t"
    "ds_read_b64_tr_b16 a[156:157], v23 offset:6144\n\t"
    "ds_read_b64_tr_b16 a[158:159], v23 offset:7168\n\t"
    "s_waitcnt lgkmcnt(8)\n\t"
    "v_mfma_f32_32x32x16_bf16 v[88:103], a[160:163], a[72:75], v[88:103]\n\t"
    "v_exp_f32 v46, v46\n\t"
    "v_exp_f32 v47, v47\n\t"
    "v_exp_f32 v48, v48\n\t"
    "v_mfma_f32_32x32x16_bf16 v[104:119], a[164:167], a[72:75], v[104:119]\n\t"
    "ds_read_b64_tr_b16 a[160:161], v22 offset:8192\n\t"
    "ds_read_b64_tr_b16 a[162:163], v22 offset:9216\n\t"
    "ds_read_b64_tr_b16 a[164:165], v23 offset:8192\n\t"
    "ds_read_b64_tr_b16 a[166:167], v23 offset:9216\n\t"
    "v_mfma_f32_32x32x16_bf16 v[88:103], a[168:171], a[76:79], v[88:103]\n\t"
    "v_exp_f32 v49, v49\n\t"
    "v_exp_f32 v50, v50\n\t"
    "v_exp_f32 v51, v51\n\t"
    "v_mfma_f32_32x32x16_bf16 v[104:119], a[172:175], a[76:79], v[104:119]\n\t"
    "ds_read_b64_tr_b16 a[168:169], v22 offset:10240\n\t"
    "ds_read_b64_tr_b16 a[170:171], v22 offset:11264\n\t"
    "ds_read_b64_tr_b16 a[172:173], v23 offset:10240\n\t"
    "ds_read_b64_tr_b16 a[174:175], v23 offset:11264\n\t"
    "s_waitcnt lgkmcnt(8)\n\t"
    "v_mfma_f32_32x32x16_bf16 v[88:103], a[144:147], a[80:83], v[88:103]\n\t"
    "v_exp_f32 v52, v52\n\t"
    "v_exp_f32 v53, v53\n\t"
    "v_exp_f32 v54, v54\n\t"
    "v_mfma_f32_32x32x16_bf16 v[104:119], a[148:151], a[80:83], v[104:119]\n\t"
    "ds_read_b64_tr_b16 a[144:145], v22 offset:12288\n\t"
    "ds_read_b64_tr_b16 a[146:147], v22 offset:13312\n\t"
    "ds_read_b64_tr_b16 a[148:149], v23 offset:12288\n\t"
    "ds_read_b64_tr_b16 a[150:151], v23 offset:13312\n\t"
    "v_mfma_f32_32x32x16_bf16 v[88:103], a[152:155], a[84:87], v[88:103]\n\t"
    "v_exp_f32 v55, v55\n\t"
    "s_nop 0\n\t"
    "v_pk_add_f32 v[24:25], v[24:25], v[126:127] op_sel_hi:[1,0]\n\t"
    "v_pk_add_f32 v[26:27], v[26:27], v[126:127] op_sel_hi:[1,0]\n\t"
    "v_pk_add_f32 v[28:29], v[28:29], v[126:127] op_sel_hi:[1,0]\n\t"
    "v_pk_add_f32 v[30:31], v[30:31], v[126:127] op_sel_hi:[1,0]\n\t"
    "v_pk_add_f32 v[32:33], v[32:33], v[126:127] op_sel_hi:[1,0]\n\t"
    "v_pk_add_f32 v[34:35], v[34:35], v[126:127] op_sel_hi:[1,0]\n\t"
    "v_pk_add_f32 v[36:37], v[36:37], v[126:127] op_sel_hi:[1,0]\n\t"
    "v_pk_add_f32 v[38:39], v[38:39], v[126:127] op_sel_hi:[1,0]\n\t"
    "v_pk_add_f32 v[40:41], v[40:41], v[126:127] op_sel_hi:[1,0]\n\t"
    "v_pk_add_f32 v[42:43], v[42:43], v[126:127] op_sel_hi:[1,0]\n\t"
    "v_pk_add_f32 v[44:45], v[44:45], v[126:127] op_sel_hi:[1,0]\n\t"
    "v_pk_add_f32 v[46:47], v[46:47], v[126:127] op_sel_hi:[1,0]\n\t"
    "v_pk_add_f32 v[48:49], v[48:49], v[126:127] op_sel_hi:[1,0]\n\t"
    "v_pk_add_f32 v[50:51], v[50:51], v[126:127] op_sel_hi:[1,0]\n\t"
    "v_pk_add_f32 v[52:53], v[52:53], v[126:127] op_sel_hi:[1,0]\n\t"
    "v_pk_add_f32 v[54:55], v[54:55], v[126:127] op_sel_hi:[1,0]\n\t"
    "v_mfma_f32_32x32x16_bf16 v[104:119], a[156:159], a[84:87], v[104:119]\n\t"
    "ds_read_b64_tr_b16 a[152:153], v22 offset:14336\n\t"
    "ds_read_b64_tr_b16 a[154:155], v22 offset:15360\n\t"
    "ds_read_b64_tr_b16 a[156:157], v23 offset:14336\n\t"
    "ds_read_b64_tr_b16 a[158:159], v23 offset:15360\n\t"
    "s_waitcnt lgkmcnt(8)\n\t"
    "v_mfma_f32_32x32x16_bf16 v[88:103], a[160:163], a[88:91], v[88:103]\n\t"
    "v_rcp_f32 v24, v24\n\t"
    "v_rcp_f32 v25, v25\n\t"
    "v_rcp_f32 v26, v26\n\t"
    "v_mfma_f32_32x32x16_bf16 v[104:119], a[164:167], a[88:91], v[104:119]\n\t"
    "ds_read_b64_tr_b16 a[160:161], v22 offset:16384\n\t"
    "ds_read_b64_tr_b16 a[162:163], v22 offset:17408\n\t"
    "ds_read_b64_tr_b16 a[164:165], v23 offset:16384\n\t"
    "ds_read_b64_tr_b16 a[166:167], v23 offset:17408\n\t"
    "v_mfma_f32_32x32x16_bf16 v[88:103], a[168:171], a[92:95], v[88:103]\n\t"
    "v_rcp_f32 v27, v27\n\t"
    "v_rcp_f32 v28, v28\n\t"
    "v_rcp_f32 v29, v29\n\t"
    "v_mfma_f32_32x32x16_bf16 v[104:119], a[172:175], a[92:95], v[104:119]\n\t"
    "ds_read_b64_tr_b16 a[168:169], v22 offset:18432\n\t"
    "ds_read_b64_tr_b16 a[170:171], v22 offset:19456\n\t"
    "ds_read_b64_tr_b16 a[172:173], v23 offset:18432\n\t"
    "ds_read_b64_tr_b16 a[174:175], v23 offset:19456\n\t"
    "s_waitcnt lgkmcnt(8)\n\t"
    "v_mfma_f32_32x32x16_bf16 v[88:103], a[144:147], a[96:99], v[88:103]\n\t"
    "v_rcp_f32 v30, v30\n\t"
    "v_rcp_f32 v31, v31\n\t"
    "v_rcp_f32 v32, v32\n\t"
    "v_mfma_f32_32x32x16_bf16 v[104:119], a[148:151], a[96:99], v[104:119]\n\t"
    "ds_read_b64_tr_b16 a[144:145], v22 offset:20480\n\t"
    "ds_read_b64_tr_b16 a[146:147], v22 offset:21504\n\t"
    "ds_read_b64_tr_b16 a[148:149], v23 offset:20480\n\t"
    "ds_read_b64_tr_b16 a[150:151], v23 offset:21504\n\t"
    "v_mfma_f32_32x32x16_bf16 v[88:103], a[152:155], a[100:103], v[88:103]\n\t"
    "v_rcp_f32 v33, v33\n\t"
    "v_rcp_f32 v34, v34\n\t"
    "v_rcp_f32 v35, v35\n\t"
    "v_mfma_f32_32x32x16_bf16 v[104:119], a[156:159], a[100:103], v[104:119]\n\t"
    "ds_read_b64_tr_b16 a[152:153], v22 offset:22528\n\t"
    "ds_read_b64_tr_b16 a[154:155], v22 offset:23552\n\t"
    "ds_read_b64_tr_b16 a[156:157], v23 offset:22528\n\t"
    "ds_read_b64_tr_b16 a[158:159], v23 offset:23552\n\t"
    "s_waitcnt lgkmcnt(8)\n\t"
    "v_mfma_f32_32x32x16_bf16 v[88:103], a[160:163], a[104:107], v[88:103]\n\t"
    "v_rcp_f32 v36, v36\n\t"
    "v_rcp_f32 v37, v37\n\t"
    "v_rcp_f32 v38, v38\n\t"
    "v_mfma_f32_32x32x16_bf16 v[104:119], a[164:167], a[104:107], v[104:119]\n\t"
    "ds_read_b64_tr_b16 a[160:161], v22 offset:24576\n\t"
    "ds_read_b64_tr_b16 a[162:163], v22 offset:25600\n\t"
    "ds_read_b64_tr_b16 a[164:165], v23 offset:24576\n\t"
    "ds_read_b64_tr_b16 a[166:167], v23 offset:25600\n\t"
    "v_mfma_f32_32x32x16_bf16 v[88:103], a[168:171], a[108:111], v[88:103]\n\t"
    "v_rcp_f32 v39, v39\n\t"
    "v_rcp_f32 v40, v40\n\t"
    "v_rcp_f32 v41, v41\n\t"
    "v_mfma_f32_32x32x16_bf16 v[104:119], a[172:175], a[108:111], v[104:119]\n\t"
    "ds_read_b64_tr_b16 a[168:169], v22 offset:26624\n\t"
    "ds_read_b64_tr_b16 a[170:171], v22 offset:27648\n\t"
    "ds_read_b64_tr_b16 a[172:173], v23 offset:26624\n\t"
    "ds_read_b64_tr_b16 a[174:175], v23 offset:27648\n\t"
    "s_waitcnt lgkmcnt(8)\n\t"
    "v_mfma_f32_32x32x16_bf16 v[88:103], a[144:147], a[112:115], v[88:103]\n\t"
    "v_rcp_f32 v42, v42\n\t"
    "v_rcp_f32 v43, v43\n\t"
    "v_rcp_f32 v44, v44\n\t"
    "v_mfma_f32_32x32x16_bf16 v[104:119], a[148:151], a[112:115], v[104:119]\n\t"
    "ds_read_b64_tr_b16 a[144:145], v22 offset:28672\n\t"
    "ds_read_b64_tr_b16 a[146:147], v22 offset:29696\n\t"
    "ds_read_b64_tr_b16 a[148:149], v23 offset:28672\n\t"
    "ds_read_b64_tr_b16 a[150:151], v23 offset:29696\n\t"
    "v_mfma_f32_32x32x16_bf16 v[88:103], a[152:155], a[116:119], v[88:103]\n\t"
    "v_rcp_f32 v45, v45\n\t"
    "v_rcp_f32 v46, v46\n\t"
    "v_rcp_f32 v47, v47\n\t"
    "v_mfma_f32_32x32x16_bf16 v[104:119], a[156:159], a[116:119], v[104:119]\n\t"
    "ds_read_b64_tr_b16 a[152:153], v22 offset:30720\n\t"
    "ds_read_b64_tr_b16 a[154:155], v22 offset:31744\n\t"
    "ds_read_b64_tr_b16 a[156:157], v23 offset:30720\n\t"
    "ds_read_b64_tr_b16 a[158:159], v23 offset:31744\n\t"
    "s_waitcnt lgkmcnt(8)\n\t"
    "v_mfma_f32_32x32x16_bf16 v[88:103], a[160:163], a[120:123], v[88:103]\n\t"
    "v_rcp_f32 v48, v48\n\t"
    "v_rcp_f32 v49, v49\n\t"
    "v_rcp_f32 v50, v50\n\t"
    "v_mfma_f32_32x32x16_bf16 v[104:119], a[164:167], a[120:123], v[104:119]\n\t"
    "ds_read_b64_tr_b16 a[160:161], v22 offset:32768\n\t"
    "ds_read_b64_tr_b16 a[162:163], v22 offset:33792\n\t"
    "ds_read_b64_tr_b16 a[164:165], v23 offset:32768\n\t"
    "ds_read_b64_tr_b16 a[166:167], v23 offset:33792\n\t"
    "v_mfma_f32_32x32x16_bf16 v[88:103], a[168:171], a[124:127], v[88:103]\n\t"
    "v_rcp_f32 v51, v51\n\t"
    "v_rcp_f32 v52, v52\n\t"
    "v_rcp_f32 v53, v53\n\t"
    "v_mfma_f32_32x32x16_bf16 v[104:119], a[172:175], a[124:127], v[104:119]\n\t"
    "ds_read_b64_tr_b16 a[168:169], v22 offset:34816\n\t"
    "ds_read_b64_tr_b16 a[170:171], v22 offset:35840\n\t"
    "ds_read_b64_tr_b16 a[172:173], v23 offset:34816\n\t"
    "ds_read_b64_tr_b16 a[174:175], v23 offset:35840\n\t"
    "s_waitcnt lgkmcnt(8)\n\t"
    "v_mfma_f32_32x32x16_bf16 v[88:103], a[144:147], a[128:131], v[88:103]\n\t"
    "s_waitcnt vmcnt(0) lgkmcnt(0)\n\t"
    "s_barrier\n\t"
    "v_mfma_f32_32x32x16_bf16 v[104:119], a[148:151], a[128:131], v[104:119]\n\t"
    "ds_read_b64_tr_b16 a[144:145], v16 offset:0\n\t"
    "ds_read_b64_tr_b16 a[146:147], v16 offset:1024\n\t"
    "ds_read_b64_tr_b16 a[148:149], v17 offset:0\n\t"
    "ds_read_b64_tr_b16 a[150:151], v17 offset:1024\n\t"
    "v_mfma_f32_32x32x16_bf16 v[88:103], a[152:155], a[132:135], v[88:103]\n\t"
    "s_add_u32 m0, %[m0b], 73728\n\t"
    "s_add_u32 s64, s64, s65\n\t"
    "buffer_load_dwordx4 v13, s[60:63], s64 offen lds\n\t"
    "v_rcp_f32 v54, v54\n\t"
    "v_mfma_f32_32x32x16_bf16 v[104:119], a[156:159], a[132:135], v[104:119]\n\t"
    "ds_read_b64_tr_b16 a[152:153], v16 offset:2048\n\t"
    "ds_read_b64_tr_b16 a[154:155], v16 offset:3072\n\t"
    "ds_read_b64_tr_b16 a[156:157], v17 offset:2048\n\t"
    "ds_read_b64_tr_b16 a[158:159], v17 offset:3072\n\t"
    "s_waitcnt lgkmcnt(8)\n\t"
    "v_mfma_f32_32x32x16_bf16 v[88:103], a[160:163], a[136:139], v[88:103]\n\t"
    "s_add_u32 m0, %[m0b], 74752\n\t"
    "s_add_u32 s64, s64, s65\n\t"
    "buffer_load_dwordx4 v13, s[60:63], s64 offen lds\n\t"
    "v_rcp_f32 v55, v55\n\t"
    "v_mfma_f32_32x32x16_bf16 v[104:119], a[164:167], a[136:139], v[104:119]\n\t"
    "ds_read_b64_tr_b16 a[160:161], v16 offset:4096\n\t"
    "ds_read_b64_tr_b16 a[162:163], v16 offset:5120\n\t"
    "ds_read_b64_tr_b16 a[164:165], v17 offset:4096\n\t"
    "ds_read_b64_tr_b16 a[166:167], v17 offset:5120\n\t"
    "v_mfma_f32_32x32x16_bf16 v[88:103], a[168:171], a[140:143], v[88:103]\n\t"
    "s_add_u32 m0, %[m0b], 75776\n\t"
    "s_add_u32 s64, s64, s65\n\t"
    "buffer_load_dwordx4 v13, s[60:63], s64 offen lds\n\t"
    "v_mfma_f32_32x32x16_bf16 v[104:119], a[172:175], a[140:143], v[104:119]\n\t"
    "s_mov_b32 s68, 3\n\t"
    ".Lml1_%=:\n"
    "v_add_u32 v12, 0x100, v12\n\t"
    "v_pk_mul_f32 v[56:57], v[56:57], v[24:25]\n\t"
    "v_pk_mul_f32 v[58:59], v[58:59], v[26:27]\n\t"
    "ds_read_b128 v[24:27], v12 offset:0\n\t"
    "v_pk_mul_f32 v[60:61], v[60:61], v[28:29]\n\t"
    "v_pk_mul_f32 v[62:63], v[62:63], v[30:31]\n\t"
    "ds_read_b128 v[28:31], v12 offset:32\n\t"
    "v_pk_mul_f32 v[64:65], v[64:65], v[32:33]\n\t"
    "v_pk_mul_f32 v[66:67], v[66:67], v[34:35]\n\t"
    "ds_read_b128 v[32:35], v12 offset:64\n\t"
    "v_pk_mul_f32 v[68:69], v[68:69], v[36:37]\n\t"
    "v_pk_mul_f32 v[70:71], v[70:71], v[38:39]\n\t"
    "ds_read_b128 v[36:39], v12 offset:96\n\t"
    "v_pk_mul_f32 v[72:73], v[72:73], v[40:41]\n\t"
    "v_pk_mul_f32 v[74:75], v[74:75], v[42:43]\n\t"
    "ds_read_b128 v[40:43], v12 offset:128\n\t"
    "v_pk_mul_f32 v[76:77], v[76:77], v[44:45]\n\t"
    "v_pk_mul_f32 v[78:79], v[78:79], v[46:47]\n\t"
    "ds_read_b128 v[44:47], v12 offset:160\n\t"
    "v_pk_mul_f32 v[80:81], v[80:81], v[48:49]\n\t"
    "v_pk_mul_f32 v[82:83], v[82:83], v[50:51]\n\t"
    "ds_read_b128 v[48:51], v12 offset:192\n\t"
    "v_pk_mul_f32 v[84:85], v[84:85], v[52:53]\n\t"
    "v_pk_mul_f32 v[86:87], v[86:87], v[54:55]\n\t"
    "ds_read_b128 v[52:55], v12 offset:224\n\t"
    "s_set_gpr_idx_on s69, gpr_idx(DST)\n\t"
    "s_nop 0\n\t"
    "v_cvt_pk_bf16_f32 v128, v56, v57\n\t"
    "v_cvt_pk_bf16_f32 v129, v58, v59\n\t"
    "v_cvt_pk_bf16_f32 v130, v60, v61\n\t"
    "v_cvt_pk_bf16_f32 v131, v62, v63\n\t"
    "v_cvt_pk_bf16_f32 v132, v64, v65\n\t"
    "v_cvt_pk_bf16_f32 v133, v66, v67\n\t"
    "v_cvt_pk_bf16_f32 v134, v68, v69\n\t"
    "v_cvt_pk_bf16_f32 v135, v70, v71\n\t"
    "v_cvt_pk_bf16_f32 v136, v72, v73\n\t"
    "v_cvt_pk_bf16_f32 v137, v74, v75\n\t"
    "v_cvt_pk_bf16_f32 v138, v76, v77\n\t"
    "v_cvt_pk_bf16_f32 v139, v78, v79\n\t"
    "v_cvt_pk_bf16_f32 v140, v80, v81\n\t"
    "v_cvt_pk_bf16_f32 v141, v82, v83\n\t"
    "v_cvt_pk_bf16_f32 v142, v84, v85\n\t"
    "v_cvt_pk_bf16_f32 v143, v86, v87\n\t"
    "s_set_gpr_idx_off\n\t"
    "s_add_u32 s69, s69, 16\n\t"
    "s_waitcnt lgkmcnt(0)\n\t"
    "v_pk_fma_f32 v[88:89], v[124:125], v[24:25], v[88:89] op_sel_hi:[0,1,1]\n\t"
    "v_pk_fma_f32 v[90:91], v[124:125], v[26:27], v[90:91] op_sel_hi:[0,1,1]\n\t"
    "v_pk_fma_f32 v[92:93], v[124:125], v[28:29], v[92:93] op_sel_hi:[0,1,1]\n\t"
    "v_pk_fma_f32 v[94:95], v[124:125], v[30:31], v[94:95] op_sel_hi:[0,1,1]\n\t"
    "v_pk_fma_f32 v[96:97], v[124:125], v[32:33], v[96:97] op_sel_hi:[0,1,1]\n\t"
    "v_pk_fma_f32 v[98:99], v[124:125], v[34:35], v[98:99] op_sel_hi:[0,1,1]\n\t"
    "v_pk_fma_f32 v[100:101], v[124:125], v[36:37], v[100:101] op_sel_hi:[0,1,1]\n\t"
    "v_pk_fma_f32 v[102:103], v[124:125], v[38:39], v[102:103] op_sel_hi:[0,1,1]\n\t"
    "v_pk_fma_f32 v[104:105], v[124:125], v[40:41], v[104:105] op_sel_hi:[0,1,1]\n\t"
    "v_pk_fma_f32 v[106:107], v[124:125], v[42:43], v[106:107] op_sel_hi:[0,1,1]\n\t"
    "v_pk_fma_f32 v[108:109], v[124:125], v[44:45], v[108:109] op_sel_hi:[0,1,1]\n\t"
    "v_pk_fma_f32 v[110:111], v[124:125], v[46:47], v[110:111] op_sel_hi:[0,1,1]\n\t"
    "v_pk_fma_f32 v[112:113], v[124:125], v[48:49], v[112:113] op_sel_hi:[0,1,1]\n\t"
    "v_pk_fma_f32 v[114:115], v[124:125], v[50:51], v[114:115] op_sel_hi:[0,1,1]\n\t"
    "v_pk_fma_f32 v[116:117], v[124:125], v[52:53], v[116:117] op_sel_hi:[0,1,1]\n\t"
    "v_pk_fma_f32 v[118:119], v[124:125], v[54:55], v[118:119] op_sel_hi:[0,1,1]\n\t"
    "v_pk_mul_f32 v[24:25], v[88:89], v[88:89]\n\t"
    "v_pk_mul_f32 v[26:27], v[90:91], v[90:91]\n\t"
    "v_pk_mul_f32 v[28:29], v[92:93], v[92:93]\n\t"
    "v_pk_mul_f32 v[30:31], v[94:95], v[94:95]\n\t"
    "v_pk_mul_f32 v[32:33], v[96:97], v[96:97]\n\t"
    "v_pk_mul_f32 v[34:35], v[98:99], v[98:99]\n\t"
    "v_pk_mul_f32 v[36:37], v[100:101], v[100:101]\n\t"
    "v_pk_mul_f32 v[38:39], v[102:103], v[102:103]\n\t"
    "v_pk_mul_f32 v[40:41], v[104:105], v[104:105]\n\t"
    "v_pk_mul_f32 v[42:43], v[106:107], v[106:107]\n\t"
    "v_pk_mul_f32 v[44:45], v[108:109], v[108:109]\n\t"
    "v_pk_mul_f32 v[46:47], v[110:111], v[110:111]\n\t"
    "v_pk_mul_f32 v[48:49], v[112:113], v[112:113]\n\t"
    "v_pk_mul_f32 v[50:51], v[114:115], v[114:115]\n\t"
    "v_pk_mul_f32 v[52:53], v[116:117], v[116:117]\n\t"
    "v_pk_mul_f32 v[54:55], v[118:119], v[118:119]\n\t"
    "v_pk_fma_f32 v[24:25], v[24:25], v[122:123], v[120:121] op_sel_hi:[1,0,0]\n\t"
    "v_pk_fma_f32 v[26:27], v[26:27], v[122:123], v[120:121] op_sel_hi:[1,0,0]\n\t"
    "v_pk_fma_f32 v[28:29], v[28:29], v[122:123], v[120:121] op_sel_hi:[1,0,0]\n\t"
    "v_pk_fma_f32 v[30:31], v[30:31], v[122:123], v[120:121] op_sel_hi:[1,0,0]\n\t"
    "v_pk_fma_f32 v[32:33], v[32:33], v[122:123], v[120:121] op_sel_hi:[1,0,0]\n\t"
    "v_pk_fma_f32 v[34:35], v[34:35], v[122:123], v[120:121] op_sel_hi:[1,0,0]\n\t"
    "v_pk_fma_f32 v[36:37], v[36:37], v[122:123], v[120:121] op_sel_hi:[1,0,0]\n\t"
    "v_pk_fma_f32 v[38:39], v[38:39], v[122:123], v[120:121] op_sel_hi:[1,0,0]\n\t"
    "v_pk_fma_f32 v[40:41], v[40:41], v[122:123], v[120:121] op_sel_hi:[1,0,0]\n\t"
    "v_pk_fma_f32 v[42:43], v[42:43], v[122:123], v[120:121] op_sel_hi:[1,0,0]\n\t"
    "v_pk_fma_f32 v[44:45], v[44:45], v[122:123], v[120:121] op_sel_hi:[1,0,0]\n\t"
    "v_pk_fma_f32 v[46:47], v[46:47], v[122:123], v[120:121] op_sel_hi:[1,0,0]\n\t"
    "v_pk_fma_f32 v[48:49], v[48:49], v[122:123], v[120:121] op_sel_hi:[1,0,0]\n\t"
    "v_pk_fma_f32 v[50:51], v[50:51], v[122:123], v[120:121] op_sel_hi:[1,0,0]\n\t"
    "v_pk_fma_f32 v[52:53], v[52:53], v[122:123], v[120:121] op_sel_hi:[1,0,0]\n\t"
    "v_pk_fma_f32 v[54:55], v[54:55], v[122:123], v[120:121] op_sel_hi:[1,0,0]\n\t"
    "v_pk_mul_f32 v[24:25], v[88:89], v[24:25]\n\t"
    "v_pk_mul_f32 v[26:27], v[90:91], v[26:27]\n\t"
    "v_pk_mul_f32 v[28:29], v[92:93], v[28:29]\n\t"
    "v_pk_mul_f32 v[30:31], v[94:95], v[30:31]\n\t"
    "v_pk_mul_f32 v[32:33], v[96:97], v[32:33]\n\t"
    "v_pk_mul_f32 v[34:35], v[98:99], v[34:35]\n\t"
    "v_pk_mul_f32 v[36:37], v[100:101], v[36:37]\n\t"
    "v_pk_mul_f32 v[38:39], v[102:103], v[38:39]\n\t"
    "v_pk_mul_f32 v[40:41], v[104:105], v[40:41]\n\t"
    "v_pk_mul_f32 v[42:43], v[106:107], v[42:43]\n\t"
    "v_pk_mul_f32 v[44:45], v[108:109], v[44:45]\n\t"
    "v_pk_mul_f32 v[46:47], v[110:111], v[46:47]\n\t"
    "v_pk_mul_f32 v[48:49], v[112:113], v[48:49]\n\t"
    "v_pk_mul_f32 v[50:51], v[114:115], v[50:51]\n\t"
    "v_pk_mul_f32 v[52:53], v[116:117], v[52:53]\n\t"
    "v_pk_mul_f32 v[54:55], v[118:119], v[54:55]\n\t"
    "ds_read_b64_tr_b16 a[168:169], v16 offset:6144\n\t"
    "ds_read_b64_tr_b16 a[170:171], v16 offset:7168\n\t"
    "ds_read_b64_tr_b16 a[172:173], v17 offset:6144\n\t"
    "ds_read_b64_tr_b16 a[174:175], v17 offset:7168\n\t"
    "s_waitcnt lgkmcnt(8)\n\t"
    "v_mfma_f32_32x32x16_bf16 v[56:71], a[144:147], a[0:3], 0\n\t"
    "s_add_u32 m0, %[m0b], 76800\n\t"
    "s_add_u32 s64, s64, s65\n\t"
    "buffer_load_dwordx4 v13, s[60:63], s64 offen lds\n\t"
    "v_exp_f32 v24, v24\n\t"
    "v_mfma_f32_32x32x16_bf16 v[72:87], a[148:151], a[0:3], 0\n\t"
    "ds_read_b64_tr_b16 a[144:145], v16 offset:8192\n\t"
    "ds_read_b64_tr_b16 a[146:147], v16 offset:9216\n\t"
    "ds_read_b64_tr_b16 a[148:149], v17 offset:8192\n\t"
    "ds_read_b64_tr_b16 a[150:151], v17 offset:9216\n\t"
    "v_mfma_f32_32x32x16_bf16 v[56:71], a[152:155], a[4:7], v[56:71]\n\t"
    "s_add_u32 m0, %[m0b], 77824\n\t"
    "s_add_u32 s64, s64, s65\n\t"
    "buffer_load_dwordx4 v13, s[60:63], s64 offen lds\n\t"
    "v_exp_f32 v25, v25\n\t"
    "v_mfma_f32_32x32x16_bf16 v[72:87], a[156:159], a[4:7], v[72:87]\n\t"
    "ds_read_b64_tr_b16 a[152:153], v16 offset:10240\n\t"
    "ds_read_b64_tr_b16 a[154:155], v16 offset:11264\n\t"
    "ds_read_b64_tr_b16 a[156:157], v17 offset:10240\n\t"
    "ds_read_b64_tr_b16 a[158:159], v17 offset:11264\n\t"
    "s_waitcnt lgkmcnt(8)\n\t"
    "v_mfma_f32_32x32x16_bf16 v[56:71], a[160:163], a[8:11], v[56:71]\n\t"
    "s_add_u32 m0, %[m0b], 78848\n\t"
    "s_add_u32 s64, s64, s65\n\t"
    "buffer_load_dwordx4 v13, s[60:63], s64 offen lds\n\t"
    "v_exp_f32 v26, v26\n\t"
    "v_mfma_f32_32x32x16_bf16 v[72:87], a[164:167], a[8:11], v[72:87]\n\t"
    "ds_read_b64_tr_b16 a[160:161], v16 offset:12288\n\t"
    "ds_read_b64_tr_b16 a[162:163], v16 offset:13312\n\t"
    "ds_read_b64_tr_b16 a[164:165], v17 offset:12288\n\t"
    "ds_read_b64_tr_b16 a[166:167], v17 offset:13312\n\t"
    "v_mfma_f32_32x32x16_bf16 v[56:71], a[168:171], a[12:15], v[56:71]\n\t"
    "s_add_u32 m0, %[m0b], 79872\n\t"
    "s_add_u32 s64, s64, s65\n\t"
    "buffer_load_dwordx4 v13, s[60:63], s64 offen lds\n\t"
    "v_exp_f32 v27, v27\n\t"
    "v_mfma_f32_32x32x16_bf16 v[72:87], a[172:175], a[12:15], v[72:87]\n\t"
    "ds_read_b64_tr_b16 a[168:169], v16 offset:14336\n\t"
    "ds_read_b64_tr_b16 a[170:171], v16 offset:15360\n\t"
    "ds_read_b64_tr_b16 a[172:173], v17 offset:14336\n\t"
    "ds_read_b64_tr_b16 a[174:175], v17 offset:15360\n\t"
    "s_waitcnt lgkmcnt(8)\n\t"
    "v_mfma_f32_32x32x16_bf16 v[56:71], a[144:147], a[16:19], v[56:71]\n\t"
    "s_add_u32 m0, %[m0b], 80896\n\t"
    "s_add_u32 s64, s64, s65\n\t"
    "buffer_load_dwordx4 v13, s[60:63], s64 offen lds\n\t"
    "v_exp_f32 v28, v28\n\t"
    "v_mfma_f32_32x32x16_bf16 v[72:87], a[148:151], a[16:19], v[72:87]\n\t"
    "ds_read_b64_tr_b16 a[144:145], v16 offset:16384\n\t"
    "ds_read_b64_tr_b16 a[146:147], v16 offset:17408\n\t"
    "ds_read_b64_tr_b16 a[148:149], v17 offset:16384\n\t"
    "ds_read_b64_tr_b16 a[150:151], v17 offset:17408\n\t"
    "v_mfma_f32_32x32x16_bf16 v[56:71], a[152:155], a[20:23], v[56:71]\n\t"
    "s_add_u32 m0, %[m0b], 81920\n\t"
    "s_add_u32 s64, s64, s65\n\t"
    "buffer_load_dwordx4 v13, s[60:63], s64 offen lds\n\t"
    "s_add_u32 s64, s64, s66\n\t"
    "v_mfma_f32_32x32x16_bf16 v[72:87], a[156:159], a[20:23], v[72:87]\n\t"
    "ds_read_b64_tr_b16 a[152:153], v16 offset:18432\n\t"
    "ds_read_b64_tr_b16 a[154:155], v16 offset:19456\n\t"
    "ds_read_b64_tr_b16 a[156:157], v17 offset:18432\n\t"
    "ds_read_b64_tr_b16 a[158:159], v17 offset:19456\n\t"
    "s_waitcnt lgkmcnt(8)\n\t"
    "v_mfma_f32_32x32x16_bf16 v[56:71], a[160:163], a[24:27], v[56:71]\n\t"
    "s_add_u32 m0, %[m0b], 110592\n\t"
    "s_add_u32 s64, s64, s65\n\t"
    "buffer_load_dwordx4 v13, s[60:63], s64 offen lds\n\t"
    "v_exp_f32 v29, v29\n\t"
    "v_mfma_f32_32x32x16_bf16 v[72:87], a[164:167], a[24:27], v[72:87]\n\t"
    "ds_read_b64_tr_b16 a[160:161], v16 offset:20480\n\t"
    "ds_read_b64_tr_b16 a[162:163], v16 offset:21504\n\t"
    "ds_read_b64_tr_b16 a[164:165], v17 offset:20480\n\t"
    "ds_read_b64_tr_b16 a[166:167], v17 offset:21504\n\t"
    "v_mfma_f32_32x32x16_bf16 v[56:71], a[168:171], a[28:31], v[56:71]\n\t"
    "s_add_u32 m0, %[m0b], 111616\n\t"
    "s_add_u32 s64, s64, s65\n\t"
    "buffer_load_dwordx4 v13, s[60:63], s64 offen lds\n\t"
    "v_exp_f32 v30, v30\n\t"
    "v_mfma_f32_32x32x16_bf16 v[72:87], a[172:175], a[28:31], v[72:87]\n\t"
    "ds_read_b64_tr_b16 a[168:169], v16 offset:22528\n\t"
    "ds_read_b64_tr_b16 a[170:171], v16 offset:23552\n\t"
    "ds_read_b64_tr_b16 a[172:173], v17 offset:22528\n\t"
    "ds_read_b64_tr_b16 a[174:175], v17 offset:23552\n\t"
    "s_waitcnt lgkmcnt(8)\n\t"
    "v_mfma_f32_32x32x16_bf16 v[56:71], a[144:147], a[32:35], v[56:71]\n\t"
    "s_add_u32 m0, %[m0b], 112640\n\t"
    "s_add_u32 s64, s64, s65\n\t"
    "buffer_load_dwordx4 v13, s[60:63], s64 offen lds\n\t"
    "v_exp_f32 v31, v31\n\t"
    "v_mfma_f32_32x32x16_bf16 v[72:87], a[148:151], a[32:35], v[72:87]\n\t"
    "ds_read_b64_tr_b16 a[144:145], v16 offset:24576\n\t"
    "ds_read_b64_tr_b16 a[146:147], v16 offset:25600\n\t"
    "ds_read_b64_tr_b16 a[148:149], v17 offset:24576\n\t"
    "ds_read_b64_tr_b16 a[150:151], v17 offset:25600\n\t"
    "v_mfma_f32_32x32x16_bf16 v[56:71], a[152:155], a[36:39], v[56:71]\n\t"
    "s_add_u32 m0, %[m0b], 113664\n\t"
    "s_add_u32 s64, s64, s65\n\t"
    "buffer_load_dwordx4 v13, s[60:63], s64 offen lds\n\t"
    "v_exp_f32 v32, v32\n\t"
    "v_mfma_f32_32x32x16_bf16 v[72:87], a[156:159], a[36:39], v[72:87]\n\t"
    "ds_read_b64_tr_b16 a[152:153], v16 offset:26624\n\t"
    "ds_read_b64_tr_b16 a[154:155], v16 offset:27648\n\t"
    "ds_read_b64_tr_b16 a[156:157], v17 offset:26624\n\t"
    "ds_read_b64_tr_b16 a[158:159], v17 offset:27648\n\t"
    "s_waitcnt lgkmcnt(8)\n\t"
    "v_mfma_f32_32x32x16_bf16 v[56:71], a[160:163], a[40:43], v[56:71]\n\t"
    "s_add_u32 m0, %[m0b], 114688\n\t"
    "s_add_u32 s64, s64, s65\n\t"
    "buffer_load_dwordx4 v13, s[60:63], s64 offen lds\n\t"
    "v_exp_f32 v33, v33\n\t"
    "v_mfma_f32_32x32x16_bf16 v[72:87], a[164:167], a[40:43], v[72:87]\n\t"
    "ds_read_b64_tr_b16 a[160:161], v16 offset:28672\n\t"
    "ds_read_b64_tr_b16 a[162:163], v16 offset:29696\n\t"
    "ds_read_b64_tr_b16 a[164:165], v17 offset:28672\n\t"
    "ds_read_b64_tr_b16 a[166:167], v17 offset:29696\n\t"
    "v_mfma_f32_32x32x16_bf16 v[56:71], a[168:171], a[44:47], v[56:71]\n\t"
    "s_add_u32 m0, %[m0b], 115712\n\t"
    "s_add_u32 s64, s64, s65\n\t"
    "buffer_load_dwordx4 v13, s[60:63], s64 offen lds\n\t"
    "v_exp_f32 v34, v34\n\t"
    "v_mfma_f32_32x32x16_bf16 v[72:87], a[172:175], a[44:47], v[72:87]\n\t"
    "ds_read_b64_tr_b16 a[168:169], v16 offset:30720\n\t"
    "ds_read_b64_tr_b16 a[170:171], v16 offset:31744\n\t"
    "ds_read_b64_tr_b16 a[172:173], v17 offset:30720\n\t"
    "ds_read_b64_tr_b16 a[174:175], v17 offset:31744\n\t"
    "s_waitcnt lgkmcnt(8)\n\t"
    "v_mfma_f32_32x32x16_bf16 v[56:71], a[144:147], a[48:51], v[56:71]\n\t"
    "s_add_u32 m0, %[m0b], 116736\n\t"
    "s_add_u32 s64, s64, s65\n\t"
    "buffer_load_dwordx4 v13, s[60:63], s64 offen lds\n\t"
    "v_exp_f32 v35, v35\n\t"
    "v_mfma_f32_32x32x16_bf16 v[72:87], a[148:151], a[48:51], v[72:87]\n\t"
    "ds_read_b64_tr_b16 a[144:145], v16 offset:32768\n\t"
    "ds_read_b64_tr_b16 a[146:147], v16 offset:33792\n\t"
    "ds_read_b64_tr_b16 a[148:149], v17 offset:32768\n\t"
    "ds_read_b64_tr_b16 a[150:151], v17 offset:33792\n\t"
    "v_mfma_f32_32x32x16_bf16 v[56:71], a[152:155], a[52:55], v[56:71]\n\t"
    "s_add_u32 m0, %[m0b], 117760\n\t"
    "s_add_u32 s64, s64, s65\n\t"
    "buffer_load_dwordx4 v13, s[60:63], s64 offen lds\n\t"
    "v_exp_f32 v36, v36\n\t"
    "v_mfma_f32_32x32x16_bf16 v[72:87], a[156:159], a[52:55], v[72:87]\n\t"
    "ds_read_b64_tr_b16 a[152:153], v16 offset:34816\n\t"
    "ds_read_b64_tr_b16 a[154:155], v16 offset:35840\n\t"
    "ds_read_b64_tr_b16 a[156:157], v17 offset:34816\n\t"
    "ds_read_b64_tr_b16 a[158:159], v17 offset:35840\n\t"
    "s_waitcnt lgkmcnt(8)\n\t"
    "v_mfma_f32_32x32x16_bf16 v[56:71], a[160:163], a[56:59], v[56:71]\n\t"
    "s_add_u32 m0, %[m0b], 118784\n\t"
    "s_add_u32 s64, s64, s65\n\t"
    "buffer_load_dwordx4 v13, s[60:63], s64 offen lds\n\t"
    "s_add_u32 s64, s64, s67\n\t"
    "v_mfma_f32_32x32x16_bf16 v[72:87], a[164:167], a[56:59], v[72:87]\n\t"
    "ds_read_b64_tr_b16 a[160:161], v18 offset:0\n\t"
    "ds_read_b64_tr_b16 a[162:163], v18 offset:1024\n\t"
    "ds_read_b64_tr_b16 a[164:165], v19 offset:0\n\t"
    "ds_read_b64_tr_b16 a[166:167], v19 offset:1024\n\t"
    "v_mfma_f32_32x32x16_bf16 v[56:71], a[168:171], a[60:63], v[56:71]\n\t"
    "v_exp_f32 v37, v37\n\t"
    "v_exp_f32 v38, v38\n\t"
    "v_exp_f32 v39, v39\n\t"
    "v_mfma_f32_32x32x16_bf16 v[72:87], a[172:175], a[60:63], v[72:87]\n\t"
    "ds_read_b64_tr_b16 a[168:169], v18 offset:2048\n\t"
    "ds_read_b64_tr_b16 a[170:171], v18 offset:3072\n\t"
    "ds_read_b64_tr_b16 a[172:173], v19 offset:2048\n\t"
    "ds_read_b64_tr_b16 a[174:175], v19 offset:3072\n\t"
    "s_waitcnt lgkmcnt(8)\n\t"
    "v_mfma_f32_32x32x16_bf16 v[56:71], a[144:147], a[64:67], v[56:71]\n\t"
    "v_exp_f32 v40, v40\n\t"
    "v_exp_f32 v41, v41\n\t"
    "v_exp_f32 v42, v42\n\t"
    "v_mfma_f32_32x32x16_bf16 v[72:87], a[148:151], a[64:67], v[72:87]\n\t"
    "ds_read_b64_tr_b16 a[144:145], v18 offset:4096\n\t"
    "ds_read_b64_tr_b16 a[146:147], v18 offset:5120\n\t"
    "ds_read_b64_tr_b16 a[148:149], v19 offset:4096\n\t"
    "ds_read_b64_tr_b16 a[150:151], v19 offset:5120\n\t"
    "v_mfma_f32_32x32x16_bf16 v[56:71], a[152:155], a[68:71], v[56:71]\n\t"
    "v_exp_f32 v43, v43\n\t"
    "v_exp_f32 v44, v44\n\t"
    "v_exp_f32 v45, v45\n\t"
    "v_mfma_f32_32x32x16_bf16 v[72:87], a[156:159], a[68:71], v[72:87]\n\t"
    "ds_read_b64_tr_b16 a[152:153], v18 offset:6144\n\t"
    "ds_read_b64_tr_b16 a[154:155], v18 offset:7168\n\t"
    "ds_read_b64_tr_b16 a[156:157], v19 offset:6144\n\t"
    "ds_read_b64_tr_b16 a[158:159], v19 offset:7168\n\t"
    "s_waitcnt lgkmcnt(8)\n\t"
    "v_mfma_f32_32x32x16_bf16 v[56:71], a[160:163], a[72:75], v[56:71]\n\t"
    "v_exp_f32 v46, v46\n\t"
    "v_exp_f32 v47, v47\n\t"
    "v_exp_f32 v48, v48\n\t"
    "v_mfma_f32_32x32x16_bf16 v[72:87], a[164:167], a[72:75], v[72:87]\n\t"
    "ds_read_b64_tr_b16 a[160:161], v18 offset:8192\n\t"
    "ds_read_b64_tr_b16 a[162:163], v18 offset:9216\n\t"
    "ds_read_b64_tr_b16 a[164:165], v19 offset:8192\n\t"
    "ds_read_b64_tr_b16 a[166:167], v19 offset:9216\n\t"
    "v_mfma_f32_32x32x16_bf16 v[56:71], a[168:171], a[76:79], v[56:71]\n\t"
    "v_exp_f32 v49, v49\n\t"
    "v_exp_f32 v50, v50\n\t"
    "v_exp_f32 v51, v51\n\t"
    "v_mfma_f32_32x32x16_bf16 v[72:87], a[172:175], a[76:79], v[72:87]\n\t"
    "ds_read_b64_tr_b16 a[168:169], v18 offset:10240\n\t"
    "ds_read_b64_tr_b16 a[170:171], v18 offset:11264\n\t"
    "ds_read_b64_tr_b16 a[172:173], v19 offset:10240\n\t"
    "ds_read_b64_tr_b16 a[174:175], v19 offset:11264\n\t"
    "s_waitcnt lgkmcnt(8)\n\t"
    "v_mfma_f32_32x32x16_bf16 v[56:71], a[144:147], a[80:83], v[56:71]\n\t"
    "v_exp_f32 v52, v52\n\t"
    "v_exp_f32 v53, v53\n\t"
    "v_exp_f32 v54, v54\n\t"
    "v_mfma_f32_32x32x16_bf16 v[72:87], a[148:151], a[80:83], v[72:87]\n\t"
    "ds_read_b64_tr_b16 a[144:145], v18 offset:12288\n\t"
    "ds_read_b64_tr_b16 a[146:147], v18 offset:13312\n\t"
    "ds_read_b64_tr_b16 a[148:149], v19 offset:12288\n\t"
    "ds_read_b64_tr_b16 a[150:151], v19 offset:13312\n\t"
    "v_mfma_f32_32x32x16_bf16 v[56:71], a[152:155], a[84:87], v[56:71]\n\t"
    "v_exp_f32 v55, v55\n\t"
    "s_nop 0\n\t"
    "v_pk_add_f32 v[24:25], v[24:25], v[126:127] op_sel_hi:[1,0]\n\t"
    "v_pk_add_f32 v[26:27], v[26:27], v[126:127] op_sel_hi:[1,0]\n\t"
    "v_pk_add_f32 v[28:29], v[28:29], v[126:127] op_sel_hi:[1,0]\n\t"
    "v_pk_add_f32 v[30:31], v[30:31], v[126:127] op_sel_hi:[1,0]\n\t"
    "v_pk_add_f32 v[32:33], v[32:33], v[126:127] op_sel_hi:[1,0]\n\t"
    "v_pk_add_f32 v[34:35], v[34:35], v[126:127] op_sel_hi:[1,0]\n\t"
    "v_pk_add_f32 v[36:37], v[36:37], v[126:127] op_sel_hi:[1,0]\n\t"
    "v_pk_add_f32 v[38:39], v[38:39], v[126:127] op_sel_hi:[1,0]\n\t"
    "v_pk_add_f32 v[40:41], v[40:41], v[126:127] op_sel_hi:[1,0]\n\t"
    "v_pk_add_f32 v[42:43], v[42:43], v[126:127] op_sel_hi:[1,0]\n\t"
    "v_pk_add_f32 v[44:45], v[44:45], v[126:127] op_sel_hi:[1,0]\n\t"
    "v_pk_add_f32 v[46:47], v[46:47], v[126:127] op_sel_hi:[1,0]\n\t"
    "v_pk_add_f32 v[48:49], v[48:49], v[126:127] op_sel_hi:[1,0]\n\t"
    "v_pk_add_f32 v[50:51], v[50:51], v[126:127] op_sel_hi:[1,0]\n\t"
    "v_pk_add_f32 v[52:53], v[52:53], v[126:127] op_sel_hi:[1,0]\n\t"
    "v_pk_add_f32 v[54:55], v[54:55], v[126:127] op_sel_hi:[1,0]\n\t"
    "v_mfma_f32_32x32x16_bf16 v[72:87], a[156:159], a[84:87], v[72:87]\n\t"
    "ds_read_b64_tr_b16 a[152:153], v18 offset:14336\n\t"
    "ds_read_b64_tr_b16 a[154:155], v18 offset:15360\n\t"
    "ds_read_b64_tr_b16 a[156:157], v19 offset:14336\n\t"
    "ds_read_b64_tr_b16 a[158:159], v19 offset:15360\n\t"
    "s_waitcnt lgkmcnt(8)\n\t"
    "v_mfma_f32_32x32x16_bf16 v[56:71], a[160:163], a[88:91], v[56:71]\n\t"
    "v_rcp_f32 v24, v24\n\t"
    "v_rcp_f32 v25, v25\n\t"
    "v_rcp_f32 v26, v26\n\t"
    "v_mfma_f32_32x32x16_bf16 v[72:87], a[164:167], a[88:91], v[72:87]\n\t"
    "ds_read_b64_tr_b16 a[160:161], v18 offset:16384\n\t"
    "ds_read_b64_tr_b16 a[162:163], v18 offset:17408\n\t"
    "ds_read_b64_tr_b16 a[164:165], v19 offset:16384\n\t"
    "ds_read_b64_tr_b16 a[166:167], v19 offset:17408\n\t"
    "v_mfma_f32_32x32x16_bf16 v[56:71], a[168:171], a[92:95], v[56:71]\n\t"
    "v_rcp_f32 v27, v27\n\t"
    "v_rcp_f32 v28, v28\n\t"
    "v_rcp_f32 v29, v29\n\t"
    "v_mfma_f32_32x32x16_bf16 v[72:87], a[172:175], a[92:95], v[72:87]\n\t"
    "ds_read_b64_tr_b16 a[168:169], v18 offset:18432\n\t"
    "ds_read_b64_tr_b16 a[170:171], v18 offset:19456\n\t"
    "ds_read_b64_tr_b16 a[172:173], v19 offset:18432\n\t"
    "ds_read_b64_tr_b16 a[174:175], v19 offset:19456\n\t"
    "s_waitcnt lgkmcnt(8)\n\t"
    "v_mfma_f32_32x32x16_bf16 v[56:71], a[144:147], a[96:99], v[56:71]\n\t"
    "v_rcp_f32 v30, v30\n\t"
    "v_rcp_f32 v31, v31\n\t"
    "v_rcp_f32 v32, v32\n\t"
    "v_mfma_f32_32x32x16_bf16 v[72:87], a[148:151], a[96:99], v[72:87]\n\t"
    "ds_read_b64_tr_b16 a[144:145], v18 offset:20480\n\t"
    "ds_read_b64_tr_b16 a[146:147], v18 offset:21504\n\t"
    "ds_read_b64_tr_b16 a[148:149], v19 offset:20480\n\t"
    "ds_read_b64_tr_b16 a[150:151], v19 offset:21504\n\t"
    "v_mfma_f32_32x32x16_bf16 v[56:71], a[152:155], a[100:103], v[56:71]\n\t"
    "v_rcp_f32 v33, v33\n\t"
    "v_rcp_f32 v34, v34\n\t"
    "v_rcp_f32 v35, v35\n\t"
    "v_mfma_f32_32x32x16_bf16 v[72:87], a[156:159], a[100:103], v[72:87]\n\t"
    "ds_read_b64_tr_b16 a[152:153], v18 offset:22528\n\t"
    "ds_read_b64_tr_b16 a[154:155], v18 offset:23552\n\t"
    "ds_read_b64_tr_b16 a[156:157], v19 offset:22528\n\t"
    "ds_read_b64_tr_b16 a[158:159], v19 offset:23552\n\t"
    "s_waitcnt lgkmcnt(8)\n\t"
    "v_mfma_f32_32x32x16_bf16 v[56:71], a[160:163], a[104:107], v[56:71]\n\t"
    "v_rcp_f32 v36, v36\n\t"
    "v_rcp_f32 v37, v37\n\t"
    "v_rcp_f32 v38, v38\n\t"
    "v_mfma_f32_32x32x16_bf16 v[72:87], a[164:167], a[104:107], v[72:87]\n\t"
    "ds_read_b64_tr_b16 a[160:161], v18 offset:24576\n\t"
    "ds_read_b64_tr_b16 a[162:163], v18 offset:25600\n\t"
    "ds_read_b64_tr_b16 a[164:165], v19 offset:24576\n\t"
    "ds_read_b64_tr_b16 a[166:167], v19 offset:25600\n\t"
    "v_mfma_f32_32x32x16_bf16 v[56:71], a[168:171], a[108:111], v[56:71]\n\t"
    "v_rcp_f32 v39, v39\n\t"
    "v_rcp_f32 v40, v40\n\t"
    "v_rcp_f32 v41, v41\n\t"
    "v_mfma_f32_32x32x16_bf16 v[72:87], a[172:175], a[108:111], v[72:87]\n\t"
    "ds_read_b64_tr_b16 a[168:169], v18 offset:26624\n\t"
    "ds_read_b64_tr_b16 a[170:171], v18 offset:27648\n\t"
    "ds_read_b64_tr_b16 a[172:173], v19 offset:26624\n\t"
    "ds_read_b64_tr_b16 a[174:175], v19 offset:27648\n\t"
    "s_waitcnt lgkmcnt(8)\n\t"
    "v_mfma_f32_32x32x16_bf16 v[56:71], a[144:147], a[112:115], v[56:71]\n\t"
    "v_rcp_f32 v42, v42\n\t"
    "v_rcp_f32 v43, v43\n\t"
    "v_rcp_f32 v44, v44\n\t"
    "v_mfma_f32_32x32x16_bf16 v[72:87], a[148:151], a[112:115], v[72:87]\n\t"
    "ds_read_b64_tr_b16 a[144:145], v18 offset:28672\n\t"
    "ds_read_b64_tr_b16 a[146:147], v18 offset:29696\n\t"
    "ds_read_b64_tr_b16 a[148:149], v19 offset:28672\n\t"
    "ds_read_b64_tr_b16 a[150:151], v19 offset:29696\n\t"
    "v_mfma_f32_32x32x16_bf16 v[56:71], a[152:155], a[116:119], v[56:71]\n\t"
    "v_rcp_f32 v45, v45\n\t"
    "v_rcp_f32 v46, v46\n\t"
    "v_rcp_f32 v47, v47\n\t"
    "v_mfma_f32_32x32x16_bf16 v[72:87], a[156:159], a[116:119], v[72:87]\n\t"
    "ds_read_b64_tr_b16 a[152:153], v18 offset:30720\n\t"
    "ds_read_b64_tr_b16 a[154:155], v18 offset:31744\n\t"
    "ds_read_b64_tr_b16 a[156:157], v19 offset:30720\n\t"
    "ds_read_b64_tr_b16 a[158:159], v19 offset:31744\n\t"
    "s_waitcnt lgkmcnt(8)\n\t"
    "v_mfma_f32_32x32x16_bf16 v[56:71], a[160:163], a[120:123], v[56:71]\n\t"
    "v_rcp_f32 v48, v48\n\t"
    "v_rcp_f32 v49, v49\n\t"
    "v_rcp_f32 v50, v50\n\t"
    "v_mfma_f32_32x32x16_bf16 v[72:87], a[164:167], a[120:123], v[72:87]\n\t"
    "ds_read_b64_tr_b16 a[160:161], v18 offset:32768\n\t"
    "ds_read_b64_tr_b16 a[162:163], v18 offset:33792\n\t"
    "ds_read_b64_tr_b16 a[164:165], v19 offset:32768\n\t"
    "ds_read_b64_tr_b16 a[166:167], v19 offset:33792\n\t"
    "v_mfma_f32_32x32x16_bf16 v[56:71], a[168:171], a[124:127], v[56:71]\n\t"
    "v_rcp_f32 v51, v51\n\t"
    "v_rcp_f32 v52, v52\n\t"
    "v_rcp_f32 v53, v53\n\t"
    "v_mfma_f32_32x32x16_bf16 v[72:87], a[172:175], a[124:127], v[72:87]\n\t"
    "ds_read_b64_tr_b16 a[168:169], v18 offset:34816\n\t"
    "ds_read_b64_tr_b16 a[170:171], v18 offset:35840\n\t"
    "ds_read_b64_tr_b16 a[172:173], v19 offset:34816\n\t"
    "ds_read_b64_tr_b16 a[174:175], v19 offset:35840\n\t"
    "s_waitcnt lgkmcnt(8)\n\t"
    "v_mfma_f32_32x32x16_bf16 v[56:71], a[144:147], a[128:131], v[56:71]\n\t"
    "s_waitcnt vmcnt(0) lgkmcnt(0)\n\t"
    "s_barrier\n\t"
    "v_mfma_f32_32x32x16_bf16 v[72:87], a[148:151], a[128:131], v[72:87]\n\t"
    "ds_read_b64_tr_b16 a[144:145], v20 offset:0\n\t"
    "ds_read_b64_tr_b16 a[146:147], v20 offset:1024\n\t"
    "ds_read_b64_tr_b16 a[148:149], v21 offset:0\n\t"
    "ds_read_b64_tr_b16 a[150:151], v21 offset:1024\n\t"
    "v_mfma_f32_32x32x16_bf16 v[56:71], a[152:155], a[132:135], v[56:71]\n\t"
    "s_add_u32 m0, %[m0b], 0\n\t"
    "s_add_u32 s64, s64, s65\n\t"
    "buffer_load_dwordx4 v13, s[60:63], s64 offen lds\n\t"
    "v_rcp_f32 v54, v54\n\t"
    "v_mfma_f32_32x32x16_bf16 v[72:87], a[156:159], a[132:135], v[72:87]\n\t"
    "ds_read_b64_tr_b16 a[152:153], v20 offset:2048\n\t"
    "ds_read_b64_tr_b16 a[154:155], v20 offset:3072\n\t"
    "ds_read_b64_tr_b16 a[156:157], v21 offset:2048\n\t"
    "ds_read_b64_tr_b16 a[158:159], v21 offset:3072\n\t"
    "s_waitcnt lgkmcnt(8)\n\t"
    "v_mfma_f32_32x32x16_bf16 v[56:71], a[160:163], a[136:139], v[56:71]\n\t"
    "s_add_u32 m0, %[m0b], 1024\n\t"
    "s_add_u32 s64, s64, s65\n\t"
    "buffer_load_dwordx4 v13, s[60:63], s64 offen lds\n\t"
    "v_rcp_f32 v55, v55\n\t"
    "v_mfma_f32_32x32x16_bf16 v[72:87], a[164:167], a[136:139], v[72:87]\n\t"
    "ds_read_b64_tr_b16 a[160:161], v20 offset:4096\n\t"
    "ds_read_b64_tr_b16 a[162:163], v20 offset:5120\n\t"
    "ds_read_b64_tr_b16 a[164:165], v21 offset:4096\n\t"
    "ds_read_b64_tr_b16 a[166:167], v21 offset:5120\n\t"
    "v_mfma_f32_32x32x16_bf16 v[56:71], a[168:171], a[140:143], v[56:71]\n\t"
    "s_add_u32 m0, %[m0b], 2048\n\t"
    "s_add_u32 s64, s64, s65\n\t"
    "buffer_load_dwordx4 v13, s[60:63], s64 offen lds\n\t"
    "v_mfma_f32_32x32x16_bf16 v[72:87], a[172:175], a[140:143], v[72:87]\n\t"
    "v_add_u32 v12, 0x100, v12\n\t"
    "v_pk_mul_f32 v[88:89], v[88:89], v[24:25]\n\t"
    "v_pk_mul_f32 v[90:91], v[90:91], v[26:27]\n\t"
    "ds_read_b128 v[24:27], v12 offset:0\n\t"
    "v_pk_mul_f32 v[92:93], v[92:93], v[28:29]\n\t"
    "v_pk_mul_f32 v[94:95], v[94:95], v[30:31]\n\t"
    "ds_read_b128 v[28:31], v12 offset:32\n\t"
    "v_pk_mul_f32 v[96:97], v[96:97], v[32:33]\n\t"
    "v_pk_mul_f32 v[98:99], v[98:99], v[34:35]\n\t"
    "ds_read_b128 v[32:35], v12 offset:64\n\t"
    "v_pk_mul_f32 v[100:101], v[100:101], v[36:37]\n\t"
    "v_pk_mul_f32 v[102:103], v[102:103], v[38:39]\n\t"
    "ds_read_b128 v[36:39], v12 offset:96\n\t"
    "v_pk_mul_f32 v[104:105], v[104:105], v[40:41]\n\t"
    "v_pk_mul_f32 v[106:107], v[106:107], v[42:43]\n\t"
    "ds_read_b128 v[40:43], v12 offset:128\n\t"
    "v_pk_mul_f32 v[108:109], v[108:109], v[44:45]\n\t"
    "v_pk_mul_f32 v[110:111], v[110:111], v[46:47]\n\t"
    "ds_read_b128 v[44:47], v12 offset:160\n\t"
    "v_pk_mul_f32 v[112:113], v[112:113], v[48:49]\n\t"
    "v_pk_mul_f32 v[114:115], v[114:115], v[50:51]\n\t"
    "ds_read_b128 v[48:51], v12 offset:192\n\t"
    "v_pk_mul_f32 v[116:117], v[116:117], v[52:53]\n\t"
    "v_pk_mul_f32 v[118:119], v[118:119], v[54:55]\n\t"
    "ds_read_b128 v[52:55], v12 offset:224\n\t"
    "s_set_gpr_idx_on s69, gpr_idx(DST)\n\t"
    "s_nop 0\n\t"
    "v_cvt_pk_bf16_f32 v128, v88, v89\n\t"
    "v_cvt_pk_bf16_f32 v129, v90, v91\n\t"
    "v_cvt_pk_bf16_f32 v130, v92, v93\n\t"
    "v_cvt_pk_bf16_f32 v131, v94, v95\n\t"
    "v_cvt_pk_bf16_f32 v132, v96, v97\n\t"
    "v_cvt_pk_bf16_f32 v133, v98, v99\n\t"
    "v_cvt_pk_bf16_f32 v134, v100, v101\n\t"
    "v_cvt_pk_bf16_f32 v135, v102, v103\n\t"
    "v_cvt_pk_bf16_f32 v136, v104, v105\n\t"
    "v_cvt_pk_bf16_f32 v137, v106, v107\n\t"
    "v_cvt_pk_bf16_f32 v138, v108, v109\n\t"
    "v_cvt_pk_bf16_f32 v139, v110, v111\n\t"
    "v_cvt_pk_bf16_f32 v140, v112, v113\n\t"
    "v_cvt_pk_bf16_f32 v141, v114, v115\n\t"
    "v_cvt_pk_bf16_f32 v142, v116, v117\n\t"
    "v_cvt_pk_bf16_f32 v143, v118, v119\n\t"
    "s_set_gpr_idx_off\n\t"
    "s_add_u32 s69, s69, 16\n\t"
    "s_waitcnt lgkmcnt(0)\n\t"
    "v_pk_fma_f32 v[56:57], v[124:125], v[24:25], v[56:57] op_sel_hi:[0,1,1]\n\t"
    "v_pk_fma_f32 v[58:59], v[124:125], v[26:27], v[58:59] op_sel_hi:[0,1,1]\n\t"
    "v_pk_fma_f32 v[60:61], v[124:125], v[28:29], v[60:61] op_sel_hi:[0,1,1]\n\t"
    "v_pk_fma_f32 v[62:63], v[124:125], v[30:31], v[62:63] op_sel_hi:[0,1,1]\n\t"
    "v_pk_fma_f32 v[64:65], v[124:125], v[32:33], v[64:65] op_sel_hi:[0,1,1]\n\t"
    "v_pk_fma_f32 v[66:67], v[124:125], v[34:35], v[66:67] op_sel_hi:[0,1,1]\n\t"
    "v_pk_fma_f32 v[68:69], v[124:125], v[36:37], v[68:69] op_sel_hi:[0,1,1]\n\t"
    "v_pk_fma_f32 v[70:71], v[124:125], v[38:39], v[70:71] op_sel_hi:[0,1,1]\n\t"
    "v_pk_fma_f32 v[72:73], v[124:125], v[40:41], v[72:73] op_sel_hi:[0,1,1]\n\t"
    "v_pk_fma_f32 v[74:75], v[124:125], v[42:43], v[74:75] op_sel_hi:[0,1,1]\n\t"
    "v_pk_fma_f32 v[76:77], v[124:125], v[44:45], v[76:77] op_sel_hi:[0,1,1]\n\t"
    "v_pk_fma_f32 v[78:79], v[124:125], v[46:47], v[78:79] op_sel_hi:[0,1,1]\n\t"
    "v_pk_fma_f32 v[80:81], v[124:125], v[48:49], v[80:81] op_sel_hi:[0,1,1]\n\t"
    "v_pk_fma_f32 v[82:83], v[124:125], v[50:51], v[82:83] op_sel_hi:[0,1,1]\n\t"
    "v_pk_fma_f32 v[84:85], v[124:125], v[52:53], v[84:85] op_sel_hi:[0,1,1]\n\t"
    "v_pk_fma_f32 v[86:87], v[124:125], v[54:55], v[86:87] op_sel_hi:[0,1,1]\n\t"
    "v_pk_mul_f32 v[24:25], v[56:57], v[56:57]\n\t"
    "v_pk_mul_f32 v[26:27], v[58:59], v[58:59]\n\t"
    "v_pk_mul_f32 v[28:29], v[60:61], v[60:61]\n\t"
    "v_pk_mul_f32 v[30:31], v[62:63], v[62:63]\n\t"
    "v_pk_mul_f32 v[32:33], v[64:65], v[64:65]\n\t"
    "v_pk_mul_f32 v[34:35], v[66:67], v[66:67]\n\t"
    "v_pk_mul_f32 v[36:37], v[68:69], v[68:69]\n\t"
    "v_pk_mul_f32 v[38:39], v[70:71], v[70:71]\n\t"
    "v_pk_mul_f32 v[40:41], v[72:73], v[72:73]\n\t"
    "v_pk_mul_f32 v[42:43], v[74:75], v[74:75]\n\t"
    "v_pk_mul_f32 v[44:45], v[76:77], v[76:77]\n\t"
    "v_pk_mul_f32 v[46:47], v[78:79], v[78:79]\n\t"
    "v_pk_mul_f32 v[48:49], v[80:81], v[80:81]\n\t"
    "v_pk_mul_f32 v[50:51], v[82:83], v[82:83]\n\t"
    "v_pk_mul_f32 v[52:53], v[84:85], v[84:85]\n\t"
    "v_pk_mul_f32 v[54:55], v[86:87], v[86:87]\n\t"
    "v_pk_fma_f32 v[24:25], v[24:25], v[122:123], v[120:121] op_sel_hi:[1,0,0]\n\t"
    "v_pk_fma_f32 v[26:27], v[26:27], v[122:123], v[120:121] op_sel_hi:[1,0,0]\n\t"
    "v_pk_fma_f32 v[28:29], v[28:29], v[122:123], v[120:121] op_sel_hi:[1,0,0]\n\t"
    "v_pk_fma_f32 v[30:31], v[30:31], v[122:123], v[120:121] op_sel_hi:[1,0,0]\n\t"
    "v_pk_fma_f32 v[32:33], v[32:33], v[122:123], v[120:121] op_sel_hi:[1,0,0]\n\t"
    "v_pk_fma_f32 v[34:35], v[34:35], v[122:123], v[120:121] op_sel_hi:[1,0,0]\n\t"
    "v_pk_fma_f32 v[36:37], v[36:37], v[122:123], v[120:121] op_sel_hi:[1,0,0]\n\t"
    "v_pk_fma_f32 v[38:39], v[38:39], v[122:123], v[120:121] op_sel_hi:[1,0,0]\n\t"
    "v_pk_fma_f32 v[40:41], v[40:41], v[122:123], v[120:121] op_sel_hi:[1,0,0]\n\t"
    "v_pk_fma_f32 v[42:43], v[42:43], v[122:123], v[120:121] op_sel_hi:[1,0,0]\n\t"
    "v_pk_fma_f32 v[44:45], v[44:45], v[122:123], v[120:121] op_sel_hi:[1,0,0]\n\t"
    "v_pk_fma_f32 v[46:47], v[46:47], v[122:123], v[120:121] op_sel_hi:[1,0,0]\n\t"
    "v_pk_fma_f32 v[48:49], v[48:49], v[122:123], v[120:121] op_sel_hi:[1,0,0]\n\t"
    "v_pk_fma_f32 v[50:51], v[50:51], v[122:123], v[120:121] op_sel_hi:[1,0,0]\n\t"
    "v_pk_fma_f32 v[52:53], v[52:53], v[122:123], v[120:121] op_sel_hi:[1,0,0]\n\t"
    "v_pk_fma_f32 v[54:55], v[54:55], v[122:123], v[120:121] op_sel_hi:[1,0,0]\n\t"
    "v_pk_mul_f32 v[24:25], v[56:57], v[24:25]\n\t"
    "v_pk_mul_f32 v[26:27], v[58:59], v[26:27]\n\t"
    "v_pk_mul_f32 v[28:29], v[60:61], v[28:29]\n\t"
    "v_pk_mul_f32 v[30:31], v[62:63], v[30:31]\n\t"
    "v_pk_mul_f32 v[32:33], v[64:65], v[32:33]\n\t"
    "v_pk_mul_f32 v[34:35], v[66:67], v[34:35]\n\t"
    "v_pk_mul_f32 v[36:37], v[68:69], v[36:37]\n\t"
    "v_pk_mul_f32 v[38:39], v[70:71], v[38:39]\n\t"
    "v_pk_mul_f32 v[40:41], v[72:73], v[40:41]\n\t"
    "v_pk_mul_f32 v[42:43], v[74:75], v[42:43]\n\t"
    "v_pk_mul_f32 v[44:45], v[76:77], v[44:45]\n\t"
    "v_pk_mul_f32 v[46:47], v[78:79], v[46:47]\n\t"
    "v_pk_mul_f32 v[48:49], v[80:81], v[48:49]\n\t"
    "v_pk_mul_f32 v[50:51], v[82:83], v[50:51]\n\t"
    "v_pk_mul_f32 v[52:53], v[84:85], v[52:53]\n\t"
    "v_pk_mul_f32 v[54:55], v[86:87], v[54:55]\n\t"
    "ds_read_b64_tr_b16 a[168:169], v20 offset:6144\n\t"
    "ds_read_b64_tr_b16 a[170:171], v20 offset:7168\n\t"
    "ds_read_b64_tr_b16 a[172:173], v21 offset:6144\n\t"
    "ds_read_b64_tr_b16 a[174:175], v21 offset:7168\n\t"
    "s_waitcnt lgkmcnt(8)\n\t"
    "v_mfma_f32_32x32x16_bf16 v[88:103], a[144:147], a[0:3], 0\n\t"
    "s_add_u32 m0, %[m0b], 3072\n\t"
    "s_add_u32 s64, s64, s65\n\t"
    "buffer_load_dwordx4 v13, s[60:63], s64 offen lds\n\t"
    "v_exp_f32 v24, v24\n\t"
    "v_mfma_f32_32x32x16_bf16 v[104:119], a[148:151], a[0:3], 0\n\t"
    "ds_read_b64_tr_b16 a[144:145], v20 offset:8192\n\t"
    "ds_read_b64_tr_b16 a[146:147], v20 offset:9216\n\t"
    "ds_read_b64_tr_b16 a[148:149], v21 offset:8192\n\t"
    "ds_read_b64_tr_b16 a[150:151], v21 offset:9216\n\t"
    "v_mfma_f32_32x32x16_bf16 v[88:103], a[152:155], a[4:7], v[88:103]\n\t"
    "s_add_u32 m0, %[m0b], 4096\n\t"
    "s_add_u32 s64, s64, s65\n\t"
    "buffer_load_dwordx4 v13, s[60:63], s64 offen lds\n\t"
    "v_exp_f32 v25, v25\n\t"
    "v_mfma_f32_32x32x16_bf16 v[104:119], a[156:159], a[4:7], v[104:119]\n\t"
    "ds_read_b64_tr_b16 a[152:153], v20 offset:10240\n\t"
    "ds_read_b64_tr_b16 a[154:155], v20 offset:11264\n\t"
    "ds_read_b64_tr_b16 a[156:157], v21 offset:10240\n\t"
    "ds_read_b64_tr_b16 a[158:159], v21 offset:11264\n\t"
    "s_waitcnt lgkmcnt(8)\n\t"
    "v_mfma_f32_32x32x16_bf16 v[88:103], a[160:163], a[8:11], v[88:103]\n\t"
    "s_add_u32 m0, %[m0b], 5120\n\t"
    "s_add_u32 s64, s64, s65\n\t"
    "buffer_load_dwordx4 v13, s[60:63], s64 offen lds\n\t"
    "v_exp_f32 v26, v26\n\t"
    "v_mfma_f32_32x32x16_bf16 v[104:119], a[164:167], a[8:11], v[104:119]\n\t"
    "ds_read_b64_tr_b16 a[160:161], v20 offset:12288\n\t"
    "ds_read_b64_tr_b16 a[162:163], v20 offset:13312\n\t"
    "ds_read_b64_tr_b16 a[164:165], v21 offset:12288\n\t"
    "ds_read_b64_tr_b16 a[166:167], v21 offset:13312\n\t"
    "v_mfma_f32_32x32x16_bf16 v[88:103], a[168:171], a[12:15], v[88:103]\n\t"
    "s_add_u32 m0, %[m0b], 6144\n\t"
    "s_add_u32 s64, s64, s65\n\t"
    "buffer_load_dwordx4 v13, s[60:63], s64 offen lds\n\t"
    "v_exp_f32 v27, v27\n\t"
    "v_mfma_f32_32x32x16_bf16 v[104:119], a[172:175], a[12:15], v[104:119]\n\t"
    "ds_read_b64_tr_b16 a[168:169], v20 offset:14336\n\t"
    "ds_read_b64_tr_b16 a[170:171], v20 offset:15360\n\t"
    "ds_read_b64_tr_b16 a[172:173], v21 offset:14336\n\t"
    "ds_read_b64_tr_b16 a[174:175], v21 offset:15360\n\t"
    "s_waitcnt lgkmcnt(8)\n\t"
    "v_mfma_f32_32x32x16_bf16 v[88:103], a[144:147], a[16:19], v[88:103]\n\t"
    "s_add_u32 m0, %[m0b], 7168\n\t"
    "s_add_u32 s64, s64, s65\n\t"
    "buffer_load_dwordx4 v13, s[60:63], s64 offen lds\n\t"
    "v_exp_f32 v28, v28\n\t"
    "v_mfma_f32_32x32x16_bf16 v[104:119], a[148:151], a[16:19], v[104:119]\n\t"
    "ds_read_b64_tr_b16 a[144:145], v20 offset:16384\n\t"
    "ds_read_b64_tr_b16 a[146:147], v20 offset:17408\n\t"
    "ds_read_b64_tr_b16 a[148:149], v21 offset:16384\n\t"
    "ds_read_b64_tr_b16 a[150:151], v21 offset:17408\n\t"
    "v_mfma_f32_32x32x16_bf16 v[88:103], a[152:155], a[20:23], v[88:103]\n\t"
    "s_add_u32 m0, %[m0b], 8192\n\t"
    "s_add_u32 s64, s64, s65\n\t"
    "buffer_load_dwordx4 v13, s[60:63], s64 offen lds\n\t"
    "s_add_u32 s64, s64, s66\n\t"
    "v_mfma_f32_32x32x16_bf16 v[104:119], a[156:159], a[20:23], v[104:119]\n\t"
    "ds_read_b64_tr_b16 a[152:153], v20 offset:18432\n\t"
    "ds_read_b64_tr_b16 a[154:155], v20 offset:19456\n\t"
    "ds_read_b64_tr_b16 a[156:157], v21 offset:18432\n\t"
    "ds_read_b64_tr_b16 a[158:159], v21 offset:19456\n\t"
    "s_waitcnt lgkmcnt(8)\n\t"
    "v_mfma_f32_32x32x16_bf16 v[88:103], a[160:163], a[24:27], v[88:103]\n\t"
    "s_add_u32 m0, %[m0b], 36864\n\t"
    "s_add_u32 s64, s64, s65\n\t"
    "buffer_load_dwordx4 v13, s[60:63], s64 offen lds\n\t"
    "v_exp_f32 v29, v29\n\t"
    "v_mfma_f32_32x32x16_bf16 v[104:119], a[164:167], a[24:27], v[104:119]\n\t"
    "ds_read_b64_tr_b16 a[160:161], v20 offset:20480\n\t"
    "ds_read_b64_tr_b16 a[162:163], v20 offset:21504\n\t"
    "ds_read_b64_tr_b16 a[164:165], v21 offset:20480\n\t"
    "ds_read_b64_tr_b16 a[166:167], v21 offset:21504\n\t"
    "v_mfma_f32_32x32x16_bf16 v[88:103], a[168:171], a[28:31], v[88:103]\n\t"
    "s_add_u32 m0, %[m0b], 37888\n\t"
    "s_add_u32 s64, s64, s65\n\t"
    "buffer_load_dwordx4 v13, s[60:63], s64 offen lds\n\t"
    "v_exp_f32 v30, v30\n\t"
    "v_mfma_f32_32x32x16_bf16 v[104:119], a[172:175], a[28:31], v[104:119]\n\t"
    "ds_read_b64_tr_b16 a[168:169], v20 offset:22528\n\t"
    "ds_read_b64_tr_b16 a[170:171], v20 offset:23552\n\t"
    "ds_read_b64_tr_b16 a[172:173], v21 offset:22528\n\t"
    "ds_read_b64_tr_b16 a[174:175], v21 offset:23552\n\t"
    "s_waitcnt lgkmcnt(8)\n\t"
    "v_mfma_f32_32x32x16_bf16 v[88:103], a[144:147], a[32:35], v[88:103]\n\t"
    "s_add_u32 m0, %[m0b], 38912\n\t"
    "s_add_u32 s64, s64, s65\n\t"
    "buffer_load_dwordx4 v13, s[60:63], s64 offen lds\n\t"
    "v_exp_f32 v31, v31\n\t"
    "v_mfma_f32_32x32x16_bf16 v[104:119], a[148:151], a[32:35], v[104:119]\n\t"
    "ds_read_b64_tr_b16 a[144:145], v20 offset:24576\n\t"
    "ds_read_b64_tr_b16 a[146:147], v20 offset:25600\n\t"
    "ds_read_b64_tr_b16 a[148:149], v21 offset:24576\n\t"
    "ds_read_b64_tr_b16 a[150:151], v21 offset:25600\n\t"
    "v_mfma_f32_32x32x16_bf16 v[88:103], a[152:155], a[36:39], v[88:103]\n\t"
    "s_add_u32 m0, %[m0b], 39936\n\t"
    "s_add_u32 s64, s64, s65\n\t"
    "buffer_load_dwordx4 v13, s[60:63], s64 offen lds\n\t"
    "v_exp_f32 v32, v32\n\t"
    "v_mfma_f32_32x32x16_bf16 v[104:119], a[156:159], a[36:39], v[104:119]\n\t"
    "ds_read_b64_tr_b16 a[152:153], v20 offset:26624\n\t"
    "ds_read_b64_tr_b16 a[154:155], v20 offset:27648\n\t"
    "ds_read_b64_tr_b16 a[156:157], v21 offset:26624\n\t"
    "ds_read_b64_tr_b16 a[158:159], v21 offset:27648\n\t"
    "s_waitcnt lgkmcnt(8)\n\t"
    "v_mfma_f32_32x32x16_bf16 v[88:103], a[160:163], a[40:43], v[88:103]\n\t"
    "s_add_u32 m0, %[m0b], 40960\n\t"
    "s_add_u32 s64, s64, s65\n\t"
    "buffer_load_dwordx4 v13, s[60:63], s64 offen lds\n\t"
    "v_exp_f32 v33, v33\n\t"
    "v_mfma_f32_32x32x16_bf16 v[104:119], a[164:167], a[40:43], v[104:119]\n\t"
    "ds_read_b64_tr_b16 a[160:161], v20 offset:28672\n\t"
    "ds_read_b64_tr_b16 a[162:163], v20 offset:29696\n\t"
    "ds_read_b64_tr_b16 a[164:165], v21 offset:28672\n\t"
    "ds_read_b64_tr_b16 a[166:167], v21 offset:29696\n\t"
    "v_mfma_f32_32x32x16_bf16 v[88:103], a[168:171], a[44:47], v[88:103]\n\t"
    "s_add_u32 m0, %[m0b], 41984\n\t"
    "s_add_u32 s64, s64, s65\n\t"
    "buffer_load_dwordx4 v13, s[60:63], s64 offen lds\n\t"
    "v_exp_f32 v34, v34\n\t"
    "v_mfma_f32_32x32x16_bf16 v[104:119], a[172:175], a[44:47], v[104:119]\n\t"
    "ds_read_b64_tr_b16 a[168:169], v20 offset:30720\n\t"
    "ds_read_b64_tr_b16 a[170:171], v20 offset:31744\n\t"
    "ds_read_b64_tr_b16 a[172:173], v21 offset:30720\n\t"
    "ds_read_b64_tr_b16 a[174:175], v21 offset:31744\n\t"
    "s_waitcnt lgkmcnt(8)\n\t"
    "v_mfma_f32_32x32x16_bf16 v[88:103], a[144:147], a[48:51], v[88:103]\n\t"
    "s_add_u32 m0, %[m0b], 43008\n\t"
    "s_add_u32 s64, s64, s65\n\t"
    "buffer_load_dwordx4 v13, s[60:63], s64 offen lds\n\t"
    "v_exp_f32 v35, v35\n\t"
    "v_mfma_f32_32x32x16_bf16 v[104:119], a[148:151], a[48:51], v[104:119]\n\t"
    "ds_read_b64_tr_b16 a[144:145], v20 offset:32768\n\t"
    "ds_read_b64_tr_b16 a[146:147], v20 offset:33792\n\t"
    "ds_read_b64_tr_b16 a[148:149], v21 offset:32768\n\t"
    "ds_read_b64_tr_b16 a[150:151], v21 offset:33792\n\t"
    "v_mfma_f32_32x32x16_bf16 v[88:103], a[152:155], a[52:55], v[88:103]\n\t"
    "s_add_u32 m0, %[m0b], 44032\n\t"
    "s_add_u32 s64, s64, s65\n\t"
    "buffer_load_dwordx4 v13, s[60:63], s64 offen lds\n\t"
    "v_exp_f32 v36, v36\n\t"
    "v_mfma_f32_32x32x16_bf16 v[104:119], a[156:159], a[52:55], v[104:119]\n\t"
    "ds_read_b64_tr_b16 a[152:153], v20 offset:34816\n\t"
    "ds_read_b64_tr_b16 a[154:155], v20 offset:35840\n\t"
    "ds_read_b64_tr_b16 a[156:157], v21 offset:34816\n\t"
    "ds_read_b64_tr_b16 a[158:159], v21 offset:35840\n\t"
    "s_waitcnt lgkmcnt(8)\n\t"
    "v_mfma_f32_32x32x16_bf16 v[88:103], a[160:163], a[56:59], v[88:103]\n\t"
    "s_add_u32 m0, %[m0b], 45056\n\t"
    "s_add_u32 s64, s64, s65\n\t"
    "buffer_load_dwordx4 v13, s[60:63], s64 offen lds\n\t"
    "s_add_u32 s64, s64, s67\n\t"
    "v_mfma_f32_32x32x16_bf16 v[104:119], a[164:167], a[56:59], v[104:119]\n\t"
    "ds_read_b64_tr_b16 a[160:161], v22 offset:0\n\t"
    "ds_read_b64_tr_b16 a[162:163], v22 offset:1024\n\t"
    "ds_read_b64_tr_b16 a[164:165], v23 offset:0\n\t"
    "ds_read_b64_tr_b16 a[166:167], v23 offset:1024\n\t"
    "v_mfma_f32_32x32x16_bf16 v[88:103], a[168:171], a[60:63], v[88:103]\n\t"
    "v_exp_f32 v37, v37\n\t"
    "v_exp_f32 v38, v38\n\t"
    "v_exp_f32 v39, v39\n\t"
    "v_mfma_f32_32x32x16_bf16 v[104:119], a[172:175], a[60:63], v[104:119]\n\t"
    "ds_read_b64_tr_b16 a[168:169], v22 offset:2048\n\t"
    "ds_read_b64_tr_b16 a[170:171], v22 offset:3072\n\t"
    "ds_read_b64_tr_b16 a[172:173], v23 offset:2048\n\t"
    "ds_read_b64_tr_b16 a[174:175], v23 offset:3072\n\t"
    "s_waitcnt lgkmcnt(8)\n\t"
    "v_mfma_f32_32x32x16_bf16 v[88:103], a[144:147], a[64:67], v[88:103]\n\t"
    "v_exp_f32 v40, v40\n\t"
    "v_exp_f32 v41, v41\n\t"
    "v_exp_f32 v42, v42\n\t"
    "v_mfma_f32_32x32x16_bf16 v[104:119], a[148:151], a[64:67], v[104:119]\n\t"
    "ds_read_b64_tr_b16 a[144:145], v22 offset:4096\n\t"
    "ds_read_b64_tr_b16 a[146:147], v22 offset:5120\n\t"
    "ds_read_b64_tr_b16 a[148:149], v23 offset:4096\n\t"
    "ds_read_b64_tr_b16 a[150:151], v23 offset:5120\n\t"
    "v_mfma_f32_32x32x16_bf16 v[88:103], a[152:155], a[68:71], v[88:103]\n\t"
    "v_exp_f32 v43, v43\n\t"
    "v_exp_f32 v44, v44\n\t"
    "v_exp_f32 v45, v45\n\t"
    "v_mfma_f32_32x32x16_bf16 v[104:119], a[156:159], a[68:71], v[104:119]\n\t"
    "ds_read_b64_tr_b16 a[152:153], v22 offset:6144\n\t"
    "ds_read_b64_tr_b16 a[154:155], v22 offset:7168\n\t"
    "ds_read_b64_tr_b16 a[156:157], v23 offset:6144\n\t"
    "ds_read_b64_tr_b16 a[158:159], v23 offset:7168\n\t"
    "s_waitcnt lgkmcnt(8)\n\t"
    "v_mfma_f32_32x32x16_bf16 v[88:103], a[160:163], a[72:75], v[88:103]\n\t"
    "v_exp_f32 v46, v46\n\t"
    "v_exp_f32 v47, v47\n\t"
    "v_exp_f32 v48, v48\n\t"
    "v_mfma_f32_32x32x16_bf16 v[104:119], a[164:167], a[72:75], v[104:119]\n\t"
    "ds_read_b64_tr_b16 a[160:161], v22 offset:8192\n\t"
    "ds_read_b64_tr_b16 a[162:163], v22 offset:9216\n\t"
    "ds_read_b64_tr_b16 a[164:165], v23 offset:8192\n\t"
    "ds_read_b64_tr_b16 a[166:167], v23 offset:9216\n\t"
    "v_mfma_f32_32x32x16_bf16 v[88:103], a[168:171], a[76:79], v[88:103]\n\t"
    "v_exp_f32 v49, v49\n\t"
    "v_exp_f32 v50, v50\n\t"
    "v_exp_f32 v51, v51\n\t"
    "v_mfma_f32_32x32x16_bf16 v[104:119], a[172:175], a[76:79], v[104:119]\n\t"
    "ds_read_b64_tr_b16 a[168:169], v22 offset:10240\n\t"
    "ds_read_b64_tr_b16 a[170:171], v22 offset:11264\n\t"
    "ds_read_b64_tr_b16 a[172:173], v23 offset:10240\n\t"
    "ds_read_b64_tr_b16 a[174:175], v23 offset:11264\n\t"
    "s_waitcnt lgkmcnt(8)\n\t"
    "v_mfma_f32_32x32x16_bf16 v[88:103], a[144:147], a[80:83], v[88:103]\n\t"
    "v_exp_f32 v52, v52\n\t"
    "v_exp_f32 v53, v53\n\t"
    "v_exp_f32 v54, v54\n\t"
    "v_mfma_f32_32x32x16_bf16 v[104:119], a[148:151], a[80:83], v[104:119]\n\t"
    "ds_read_b64_tr_b16 a[144:145], v22 offset:12288\n\t"
    "ds_read_b64_tr_b16 a[146:147], v22 offset:13312\n\t"
    "ds_read_b64_tr_b16 a[148:149], v23 offset:12288\n\t"
    "ds_read_b64_tr_b16 a[150:151], v23 offset:13312\n\t"
    "v_mfma_f32_32x32x16_bf16 v[88:103], a[152:155], a[84:87], v[88:103]\n\t"
    "v_exp_f32 v55, v55\n\t"
    "s_nop 0\n\t"
    "v_pk_add_f32 v[24:25], v[24:25], v[126:127] op_sel_hi:[1,0]\n\t"
    "v_pk_add_f32 v[26:27], v[26:27], v[126:127] op_sel_hi:[1,0]\n\t"
    "v_pk_add_f32 v[28:29], v[28:29], v[126:127] op_sel_hi:[1,0]\n\t"
    "v_pk_add_f32 v[30:31], v[30:31], v[126:127] op_sel_hi:[1,0]\n\t"
    "v_pk_add_f32 v[32:33], v[32:33], v[126:127] op_sel_hi:[1,0]\n\t"
    "v_pk_add_f32 v[34:35], v[34:35], v[126:127] op_sel_hi:[1,0]\n\t"
    "v_pk_add_f32 v[36:37], v[36:37], v[126:127] op_sel_hi:[1,0]\n\t"
    "v_pk_add_f32 v[38:39], v[38:39], v[126:127] op_sel_hi:[1,0]\n\t"
    "v_pk_add_f32 v[40:41], v[40:41], v[126:127] op_sel_hi:[1,0]\n\t"
    "v_pk_add_f32 v[42:43], v[42:43], v[126:127] op_sel_hi:[1,0]\n\t"
    "v_pk_add_f32 v[44:45], v[44:45], v[126:127] op_sel_hi:[1,0]\n\t"
    "v_pk_add_f32 v[46:47], v[46:47], v[126:127] op_sel_hi:[1,0]\n\t"
    "v_pk_add_f32 v[48:49], v[48:49], v[126:127] op_sel_hi:[1,0]\n\t"
    "v_pk_add_f32 v[50:51], v[50:51], v[126:127] op_sel_hi:[1,0]\n\t"
    "v_pk_add_f32 v[52:53], v[52:53], v[126:127] op_sel_hi:[1,0]\n\t"
    "v_pk_add_f32 v[54:55], v[54:55], v[126:127] op_sel_hi:[1,0]\n\t"
    "v_mfma_f32_32x32x16_bf16 v[104:119], a[156:159], a[84:87], v[104:119]\n\t"
    "ds_read_b64_tr_b16 a[152:153], v22 offset:14336\n\t"
    "ds_read_b64_tr_b16 a[154:155], v22 offset:15360\n\t"
    "ds_read_b64_tr_b16 a[156:157], v23 offset:14336\n\t"
    "ds_read_b64_tr_b16 a[158:159], v23 offset:15360\n\t"
    "s_waitcnt lgkmcnt(8)\n\t"
    "v_mfma_f32_32x32x16_bf16 v[88:103], a[160:163], a[88:91], v[88:103]\n\t"
    "v_rcp_f32 v24, v24\n\t"
    "v_rcp_f32 v25, v25\n\t"
    "v_rcp_f32 v26, v26\n\t"
    "v_mfma_f32_32x32x16_bf16 v[104:119], a[164:167], a[88:91], v[104:119]\n\t"
    "ds_read_b64_tr_b16 a[160:161], v22 offset:16384\n\t"
    "ds_read_b64_tr_b16 a[162:163], v22 offset:17408\n\t"
    "ds_read_b64_tr_b16 a[164:165], v23 offset:16384\n\t"
    "ds_read_b64_tr_b16 a[166:167], v23 offset:17408\n\t"
    "v_mfma_f32_32x32x16_bf16 v[88:103], a[168:171], a[92:95], v[88:103]\n\t"
    "v_rcp_f32 v27, v27\n\t"
    "v_rcp_f32 v28, v28\n\t"
    "v_rcp_f32 v29, v29\n\t"
    "v_mfma_f32_32x32x16_bf16 v[104:119], a[172:175], a[92:95], v[104:119]\n\t"
    "ds_read_b64_tr_b16 a[168:169], v22 offset:18432\n\t"
    "ds_read_b64_tr_b16 a[170:171], v22 offset:19456\n\t"
    "ds_read_b64_tr_b16 a[172:173], v23 offset:18432\n\t"
    "ds_read_b64_tr_b16 a[174:175], v23 offset:19456\n\t"
    "s_waitcnt lgkmcnt(8)\n\t"
    "v_mfma_f32_32x32x16_bf16 v[88:103], a[144:147], a[96:99], v[88:103]\n\t"
    "v_rcp_f32 v30, v30\n\t"
    "v_rcp_f32 v31, v31\n\t"
    "v_rcp_f32 v32, v32\n\t"
    "v_mfma_f32_32x32x16_bf16 v[104:119], a[148:151], a[96:99], v[104:119]\n\t"
    "ds_read_b64_tr_b16 a[144:145], v22 offset:20480\n\t"
    "ds_read_b64_tr_b16 a[146:147], v22 offset:21504\n\t"
    "ds_read_b64_tr_b16 a[148:149], v23 offset:20480\n\t"
    "ds_read_b64_tr_b16 a[150:151], v23 offset:21504\n\t"
    "v_mfma_f32_32x32x16_bf16 v[88:103], a[152:155], a[100:103], v[88:103]\n\t"
    "v_rcp_f32 v33, v33\n\t"
    "v_rcp_f32 v34, v34\n\t"
    "v_rcp_f32 v35, v35\n\t"
    "v_mfma_f32_32x32x16_bf16 v[104:119], a[156:159], a[100:103], v[104:119]\n\t"
    "ds_read_b64_tr_b16 a[152:153], v22 offset:22528\n\t"
    "ds_read_b64_tr_b16 a[154:155], v22 offset:23552\n\t"
    "ds_read_b64_tr_b16 a[156:157], v23 offset:22528\n\t"
    "ds_read_b64_tr_b16 a[158:159], v23 offset:23552\n\t"
    "s_waitcnt lgkmcnt(8)\n\t"
    "v_mfma_f32_32x32x16_bf16 v[88:103], a[160:163], a[104:107], v[88:103]\n\t"
    "v_rcp_f32 v36, v36\n\t"
    "v_rcp_f32 v37, v37\n\t"
    "v_rcp_f32 v38, v38\n\t"
    "v_mfma_f32_32x32x16_bf16 v[104:119], a[164:167], a[104:107], v[104:119]\n\t"
    "ds_read_b64_tr_b16 a[160:161], v22 offset:24576\n\t"
    "ds_read_b64_tr_b16 a[162:163], v22 offset:25600\n\t"
    "ds_read_b64_tr_b16 a[164:165], v23 offset:24576\n\t"
    "ds_read_b64_tr_b16 a[166:167], v23 offset:25600\n\t"
    "v_mfma_f32_32x32x16_bf16 v[88:103], a[168:171], a[108:111], v[88:103]\n\t"
    "v_rcp_f32 v39, v39\n\t"
    "v_rcp_f32 v40, v40\n\t"
    "v_rcp_f32 v41, v41\n\t"
    "v_mfma_f32_32x32x16_bf16 v[104:119], a[172:175], a[108:111], v[104:119]\n\t"
    "ds_read_b64_tr_b16 a[168:169], v22 offset:26624\n\t"
    "ds_read_b64_tr_b16 a[170:171], v22 offset:27648\n\t"
    "ds_read_b64_tr_b16 a[172:173], v23 offset:26624\n\t"
    "ds_read_b64_tr_b16 a[174:175], v23 offset:27648\n\t"
    "s_waitcnt lgkmcnt(8)\n\t"
    "v_mfma_f32_32x32x16_bf16 v[88:103], a[144:147], a[112:115], v[88:103]\n\t"
    "v_rcp_f32 v42, v42\n\t"
    "v_rcp_f32 v43, v43\n\t"
    "v_rcp_f32 v44, v44\n\t"
    "v_mfma_f32_32x32x16_bf16 v[104:119], a[148:151], a[112:115], v[104:119]\n\t"
    "ds_read_b64_tr_b16 a[144:145], v22 offset:28672\n\t"
    "ds_read_b64_tr_b16 a[146:147], v22 offset:29696\n\t"
    "ds_read_b64_tr_b16 a[148:149], v23 offset:28672\n\t"
    "ds_read_b64_tr_b16 a[150:151], v23 offset:29696\n\t"
    "v_mfma_f32_32x32x16_bf16 v[88:103], a[152:155], a[116:119], v[88:103]\n\t"
    "v_rcp_f32 v45, v45\n\t"
    "v_rcp_f32 v46, v46\n\t"
    "v_rcp_f32 v47, v47\n\t"
    "v_mfma_f32_32x32x16_bf16 v[104:119], a[156:159], a[116:119], v[104:119]\n\t"
    "ds_read_b64_tr_b16 a[152:153], v22 offset:30720\n\t"
    "ds_read_b64_tr_b16 a[154:155], v22 offset:31744\n\t"
    "ds_read_b64_tr_b16 a[156:157], v23 offset:30720\n\t"
    "ds_read_b64_tr_b16 a[158:159], v23 offset:31744\n\t"
    "s_waitcnt lgkmcnt(8)\n\t"
    "v_mfma_f32_32x32x16_bf16 v[88:103], a[160:163], a[120:123], v[88:103]\n\t"
    "v_rcp_f32 v48, v48\n\t"
    "v_rcp_f32 v49, v49\n\t"
    "v_rcp_f32 v50, v50\n\t"
    "v_mfma_f32_32x32x16_bf16 v[104:119], a[164:167], a[120:123], v[104:119]\n\t"
    "ds_read_b64_tr_b16 a[160:161], v22 offset:32768\n\t"
    "ds_read_b64_tr_b16 a[162:163], v22 offset:33792\n\t"
    "ds_read_b64_tr_b16 a[164:165], v23 offset:32768\n\t"
    "ds_read_b64_tr_b16 a[166:167], v23 offset:33792\n\t"
    "v_mfma_f32_32x32x16_bf16 v[88:103], a[168:171], a[124:127], v[88:103]\n\t"
    "v_rcp_f32 v51, v51\n\t"
    "v_rcp_f32 v52, v52\n\t"
    "v_rcp_f32 v53, v53\n\t"
    "v_mfma_f32_32x32x16_bf16 v[104:119], a[172:175], a[124:127], v[104:119]\n\t"
    "ds_read_b64_tr_b16 a[168:169], v22 offset:34816\n\t"
    "ds_read_b64_tr_b16 a[170:171], v22 offset:35840\n\t"
    "ds_read_b64_tr_b16 a[172:173], v23 offset:34816\n\t"
    "ds_read_b64_tr_b16 a[174:175], v23 offset:35840\n\t"
    "s_waitcnt lgkmcnt(8)\n\t"
    "v_mfma_f32_32x32x16_bf16 v[88:103], a[144:147], a[128:131], v[88:103]\n\t"
    "s_waitcnt vmcnt(0) lgkmcnt(0)\n\t"
    "s_barrier\n\t"
    "v_mfma_f32_32x32x16_bf16 v[104:119], a[148:151], a[128:131], v[104:119]\n\t"
    "ds_read_b64_tr_b16 a[144:145], v16 offset:0\n\t"
    "ds_read_b64_tr_b16 a[146:147], v16 offset:1024\n\t"
    "ds_read_b64_tr_b16 a[148:149], v17 offset:0\n\t"
    "ds_read_b64_tr_b16 a[150:151], v17 offset:1024\n\t"
    "v_mfma_f32_32x32x16_bf16 v[88:103], a[152:155], a[132:135], v[88:103]\n\t"
    "s_cmp_lg_u32 s68, 1\n\t"
    "s_cbranch_scc1 .Lmlsw_%=\n\t"
    "s_mov_b32 s60, %[w4a]\n\t"
    "s_mov_b32 s61, %[w4b]\n\t"
    "v_mov_b32 v13, %[vo4]\n\t"
    "s_sub_u32 s64, %[so4], 8192\n\t"
    "s_mov_b32 s65, 9216\n\t"
    "s_mov_b32 s66, 248832\n\t"
    "s_mov_b32 s67, 0xfff9ac80\n\t"
    ".Lmlsw_%=:\n"
    "s_add_u32 m0, %[m0b], 73728\n\t"
    "s_add_u32 s64, s64, s65\n\t"
    "buffer_load_dwordx4 v13, s[60:63], s64 offen lds\n\t"
    "v_mfma_f32_32x32x16_bf16 v[104:119], a[156:159], a[132:135], v[104:119]\n\t"
    "ds_read_b64_tr_b16 a[152:153], v16 offset:2048\n\t"
    "ds_read_b64_tr_b16 a[154:155], v16 offset:3072\n\t"
    "ds_read_b64_tr_b16 a[156:157], v17 offset:2048\n\t"
    "ds_read_b64_tr_b16 a[158:159], v17 offset:3072\n\t"
    "s_waitcnt lgkmcnt(8)\n\t"
    "v_mfma_f32_32x32x16_bf16 v[88:103], a[160:163], a[136:139], v[88:103]\n\t"
    "s_add_u32 m0, %[m0b], 74752\n\t"
    "s_add_u32 s64, s64, s65\n\t"
    "buffer_load_dwordx4 v13, s[60:63], s64 offen lds\n\t"
    "v_rcp_f32 v54, v54\n\t"
    "v_mfma_f32_32x32x16_bf16 v[104:119], a[164:167], a[136:139], v[104:119]\n\t"
    "ds_read_b64_tr_b16 a[160:161], v16 offset:4096\n\t"
    "ds_read_b64_tr_b16 a[162:163], v16 offset:5120\n\t"
    "ds_read_b64_tr_b16 a[164:165], v17 offset:4096\n\t"
    "ds_read_b64_tr_b16 a[166:167], v17 offset:5120\n\t"
    "v_mfma_f32_32x32x16_bf16 v[88:103], a[168:171], a[140:143], v[88:103]\n\t"
    "s_add_u32 m0, %[m0b], 75776\n\t"
    "s_add_u32 s64, s64, s65\n\t"
    "buffer_load_dwordx4 v13, s[60:63], s64 offen lds\n\t"
    "v_rcp_f32 v55, v55\n\t"
    "v_mfma_f32_32x32x16_bf16 v[104:119], a[172:175], a[140:143], v[104:119]\n\t"
    "s_sub_u32 s68, s68, 1\n\t"
    "s_cmp_lg_u32 s68, 0\n\t"
    "s_cbranch_scc1 .Lml1_%=\n\t"
    "v_add_u32 v12, 0x100, v12\n\t"
    "v_pk_mul_f32 v[56:57], v[56:57], v[24:25]\n\t"
    "v_pk_mul_f32 v[58:59], v[58:59], v[26:27]\n\t"
    "ds_read_b128 v[24:27], v12 offset:0\n\t"
    "v_pk_mul_f32 v[60:61], v[60:61], v[28:29]\n\t"
    "v_pk_mul_f32 v[62:63], v[62:63], v[30:31]\n\t"
    "ds_read_b128 v[28:31], v12 offset:32\n\t"
    "v_pk_mul_f32 v[64:65], v[64:65], v[32:33]\n\t"
    "v_pk_mul_f32 v[66:67], v[66:67], v[34:35]\n\t"
    "ds_read_b128 v[32:35], v12 offset:64\n\t"
    "v_pk_mul_f32 v[68:69], v[68:69], v[36:37]\n\t"
    "v_pk_mul_f32 v[70:71], v[70:71], v[38:39]\n\t"
    "ds_read_b128 v[36:39], v12 offset:96\n\t"
    "v_pk_mul_f32 v[72:73], v[72:73], v[40:41]\n\t"
    "v_pk_mul_f32 v[74:75], v[74:75], v[42:43]\n\t"
    "ds_read_b128 v[40:43], v12 offset:128\n\t"
    "v_pk_mul_f32 v[76:77], v[76:77], v[44:45]\n\t"
    "v_pk_mul_f32 v[78:79], v[78:79], v[46:47]\n\t"
    "ds_read_b128 v[44:47], v12 offset:160\n\t"
    "v_pk_mul_f32 v[80:81], v[80:81], v[48:49]\n\t"
    "v_pk_mul_f32 v[82:83], v[82:83], v[50:51]\n\t"
    "ds_read_b128 v[48:51], v12 offset:192\n\t"
    "v_pk_mul_f32 v[84:85], v[84:85], v[52:53]\n\t"
    "v_pk_mul_f32 v[86:87], v[86:87], v[54:55]\n\t"
    "ds_read_b128 v[52:55], v12 offset:224\n\t"
    "s_set_gpr_idx_on s69, gpr_idx(DST)\n\t"
    "s_nop 0\n\t"
    "v_cvt_pk_bf16_f32 v128, v56, v57\n\t"
    "v_cvt_pk_bf16_f32 v129, v58, v59\n\t"
    "v_cvt_pk_bf16_f32 v130, v60, v61\n\t"
    "v_cvt_pk_bf16_f32 v131, v62, v63\n\t"
    "v_cvt_pk_bf16_f32 v132, v64, v65\n\t"
    "v_cvt_pk_bf16_f32 v133, v66, v67\n\t"
    "v_cvt_pk_bf16_f32 v134, v68, v69\n\t"
    "v_cvt_pk_bf16_f32 v135, v70, v71\n\t"
    "v_cvt_pk_bf16_f32 v136, v72, v73\n\t"
    "v_cvt_pk_bf16_f32 v137, v74, v75\n\t"
    "v_cvt_pk_bf16_f32 v138, v76, v77\n\t"
    "v_cvt_pk_bf16_f32 v139, v78, v79\n\t"
    "v_cvt_pk_bf16_f32 v140, v80, v81\n\t"
    "v_cvt_pk_bf16_f32 v141, v82, v83\n\t"
    "v_cvt_pk_bf16_f32 v142, v84, v85\n\t"
    "v_cvt_pk_bf16_f32 v143, v86, v87\n\t"
    "s_set_gpr_idx_off\n\t"
    "s_add_u32 s69, s69, 16\n\t"
    "s_waitcnt lgkmcnt(0)\n\t"
    "v_pk_fma_f32 v[88:89], v[124:125], v[24:25], v[88:89] op_sel_hi:[0,1,1]\n\t"
    "v_pk_fma_f32 v[90:91], v[124:125], v[26:27], v[90:91] op_sel_hi:[0,1,1]\n\t"
    "v_pk_fma_f32 v[92:93], v[124:125], v[28:29], v[92:93] op_sel_hi:[0,1,1]\n\t"
    "v_pk_fma_f32 v[94:95], v[124:125], v[30:31], v[94:95] op_sel_hi:[0,1,1]\n\t"
    "v_pk_fma_f32 v[96:97], v[124:125], v[32:33], v[96:97] op_sel_hi:[0,1,1]\n\t"
    "v_pk_fma_f32 v[98:99], v[124:125], v[34:35], v[98:99] op_sel_hi:[0,1,1]\n\t"
    "v_pk_fma_f32 v[100:101], v[124:125], v[36:37], v[100:101] op_sel_hi:[0,1,1]\n\t"
    "v_pk_fma_f32 v[102:103], v[124:125], v[38:39], v[102:103] op_sel_hi:[0,1,1]\n\t"
    "v_pk_fma_f32 v[104:105], v[124:125], v[40:41], v[104:105] op_sel_hi:[0,1,1]\n\t"
    "v_pk_fma_f32 v[106:107], v[124:125], v[42:43], v[106:107] op_sel_hi:[0,1,1]\n\t"
    "v_pk_fma_f32 v[108:109], v[124:125], v[44:45], v[108:109] op_sel_hi:[0,1,1]\n\t"
    "v_pk_fma_f32 v[110:111], v[124:125], v[46:47], v[110:111] op_sel_hi:[0,1,1]\n\t"
    "v_pk_fma_f32 v[112:113], v[124:125], v[48:49], v[112:113] op_sel_hi:[0,1,1]\n\t"
    "v_pk_fma_f32 v[114:115], v[124:125], v[50:51], v[114:115] op_sel_hi:[0,1,1]\n\t"
    "v_pk_fma_f32 v[116:117], v[124:125], v[52:53], v[116:117] op_sel_hi:[0,1,1]\n\t"
    "v_pk_fma_f32 v[118:119], v[124:125], v[54:55], v[118:119] op_sel_hi:[0,1,1]\n\t"
    "v_pk_mul_f32 v[24:25], v[88:89], v[88:89]\n\t"
    "v_pk_mul_f32 v[26:27], v[90:91], v[90:91]\n\t"
    "v_pk_mul_f32 v[28:29], v[92:93], v[92:93]\n\t"
    "v_pk_mul_f32 v[30:31], v[94:95], v[94:95]\n\t"
    "v_pk_mul_f32 v[32:33], v[96:97], v[96:97]\n\t"
    "v_pk_mul_f32 v[34:35], v[98:99], v[98:99]\n\t"
    "v_pk_mul_f32 v[36:37], v[100:101], v[100:101]\n\t"
    "v_pk_mul_f32 v[38:39], v[102:103], v[102:103]\n\t"
    "v_pk_mul_f32 v[40:41], v[104:105], v[104:105]\n\t"
    "v_pk_mul_f32 v[42:43], v[106:107], v[106:107]\n\t"
    "v_pk_mul_f32 v[44:45], v[108:109], v[108:109]\n\t"
    "v_pk_mul_f32 v[46:47], v[110:111], v[110:111]\n\t"
    "v_pk_mul_f32 v[48:49], v[112:113], v[112:113]\n\t"
    "v_pk_mul_f32 v[50:51], v[114:115], v[114:115]\n\t"
    "v_pk_mul_f32 v[52:53], v[116:117], v[116:117]\n\t"
    "v_pk_mul_f32 v[54:55], v[118:119], v[118:119]\n\t"
    "v_pk_fma_f32 v[24:25], v[24:25], v[122:123], v[120:121] op_sel_hi:[1,0,0]\n\t"
    "v_pk_fma_f32 v[26:27], v[26:27], v[122:123], v[120:121] op_sel_hi:[1,0,0]\n\t"
    "v_pk_fma_f32 v[28:29], v[28:29], v[122:123], v[120:121] op_sel_hi:[1,0,0]\n\t"
    "v_pk_fma_f32 v[30:31], v[30:31], v[122:123], v[120:121] op_sel_hi:[1,0,0]\n\t"
    "v_pk_fma_f32 v[32:33], v[32:33], v[122:123], v[120:121] op_sel_hi:[1,0,0]\n\t"
    "v_pk_fma_f32 v[34:35], v[34:35], v[122:123], v[120:121] op_sel_hi:[1,0,0]\n\t"
    "v_pk_fma_f32 v[36:37], v[36:37], v[122:123], v[120:121] op_sel_hi:[1,0,0]\n\t"
    "v_pk_fma_f32 v[38:39], v[38:39], v[122:123], v[120:121] op_sel_hi:[1,0,0]\n\t"
    "v_pk_fma_f32 v[40:41], v[40:41], v[122:123], v[120:121] op_sel_hi:[1,0,0]\n\t"
    "v_pk_fma_f32 v[42:43], v[42:43], v[122:123], v[120:121] op_sel_hi:[1,0,0]\n\t"
    "v_pk_fma_f32 v[44:45], v[44:45], v[122:123], v[120:121] op_sel_hi:[1,0,0]\n\t"
    "v_pk_fma_f32 v[46:47], v[46:47], v[122:123], v[120:121] op_sel_hi:[1,0,0]\n\t"
    "v_pk_fma_f32 v[48:49], v[48:49], v[122:123], v[120:121] op_sel_hi:[1,0,0]\n\t"
    "v_pk_fma_f32 v[50:51], v[50:51], v[122:123], v[120:121] op_sel_hi:[1,0,0]\n\t"
    "v_pk_fma_f32 v[52:53], v[52:53], v[122:123], v[120:121] op_sel_hi:[1,0,0]\n\t"
    "v_pk_fma_f32 v[54:55], v[54:55], v[122:123], v[120:121] op_sel_hi:[1,0,0]\n\t"
    "v_pk_mul_f32 v[24:25], v[88:89], v[24:25]\n\t"
    "v_pk_mul_f32 v[26:27], v[90:91], v[26:27]\n\t"
    "v_pk_mul_f32 v[28:29], v[92:93], v[28:29]\n\t"
    "v_pk_mul_f32 v[30:31], v[94:95], v[30:31]\n\t"
    "v_pk_mul_f32 v[32:33], v[96:97], v[32:33]\n\t"
    "v_pk_mul_f32 v[34:35], v[98:99], v[34:35]\n\t"
    "v_pk_mul_f32 v[36:37], v[100:101], v[36:37]\n\t"
    "v_pk_mul_f32 v[38:39], v[102:103], v[38:39]\n\t"
    "v_pk_mul_f32 v[40:41], v[104:105], v[40:41]\n\t"
    "v_pk_mul_f32 v[42:43], v[106:107], v[42:43]\n\t"
    "v_pk_mul_f32 v[44:45], v[108:109], v[44:45]\n\t"
    "v_pk_mul_f32 v[46:47], v[110:111], v[46:47]\n\t"
    "v_pk_mul_f32 v[48:49], v[112:113], v[48:49]\n\t"
    "v_pk_mul_f32 v[50:51], v[114:115], v[50:51]\n\t"
    "v_pk_mul_f32 v[52:53], v[116:117], v[52:53]\n\t"
    "v_pk_mul_f32 v[54:55], v[118:119], v[54:55]\n\t"
    "ds_read_b64_tr_b16 a[168:169], v16 offset:6144\n\t"
    "ds_read_b64_tr_b16 a[170:171], v16 offset:7168\n\t"
    "ds_read_b64_tr_b16 a[172:173], v17 offset:6144\n\t"
    "ds_read_b64_tr_b16 a[174:175], v17 offset:7168\n\t"
    "s_waitcnt lgkmcnt(8)\n\t"
    "v_mfma_f32_32x32x16_bf16 v[56:71], a[144:147], a[0:3], 0\n\t"
    "s_add_u32 m0, %[m0b], 76800\n\t"
    "s_add_u32 s64, s64, s65\n\t"
    "buffer_load_dwordx4 v13, s[60:63], s64 offen lds\n\t"
    "v_exp_f32 v24, v24\n\t"
    "v_mfma_f32_32x32x16_bf16 v[72:87], a[148:151], a[0:3], 0\n\t"
    "ds_read_b64_tr_b16 a[144:145], v16 offset:8192\n\t"
    "ds_read_b64_tr_b16 a[146:147], v16 offset:9216\n\t"
    "ds_read_b64_tr_b16 a[148:149], v17 offset:8192\n\t"
    "ds_read_b64_tr_b16 a[150:151], v17 offset:9216\n\t"
    "v_mfma_f32_32x32x16_bf16 v[56:71], a[152:155], a[4:7], v[56:71]\n\t"
    "s_add_u32 m0, %[m0b], 77824\n\t"
    "s_add_u32 s64, s64, s65\n\t"
    "buffer_load_dwordx4 v13, s[60:63], s64 offen lds\n\t"
    "v_exp_f32 v25, v25\n\t"
    "v_mfma_f32_32x32x16_bf16 v[72:87], a[156:159], a[4:7], v[72:87]\n\t"
    "ds_read_b64_tr_b16 a[152:153], v16 offset:10240\n\t"
    "ds_read_b64_tr_b16 a[154:155], v16 offset:11264\n\t"
    "ds_read_b64_tr_b16 a[156:157], v17 offset:10240\n\t"
    "ds_read_b64_tr_b16 a[158:159], v17 offset:11264\n\t"
    "s_waitcnt lgkmcnt(8)\n\t"
    "v_mfma_f32_32x32x16_bf16 v[56:71], a[160:163], a[8:11], v[56:71]\n\t"
    "s_add_u32 m0, %[m0b], 78848\n\t"
    "s_add_u32 s64, s64, s65\n\t"
    "buffer_load_dwordx4 v13, s[60:63], s64 offen lds\n\t"
    "v_exp_f32 v26, v26\n\t"
    "v_mfma_f32_32x32x16_bf16 v[72:87], a[164:167], a[8:11], v[72:87]\n\t"
    "ds_read_b64_tr_b16 a[160:161], v16 offset:12288\n\t"
    "ds_read_b64_tr_b16 a[162:163], v16 offset:13312\n\t"
    "ds_read_b64_tr_b16 a[164:165], v17 offset:12288\n\t"
    "ds_read_b64_tr_b16 a[166:167], v17 offset:13312\n\t"
    "v_mfma_f32_32x32x16_bf16 v[56:71], a[168:171], a[12:15], v[56:71]\n\t"
    "s_add_u32 m0, %[m0b], 79872\n\t"
    "s_add_u32 s64, s64, s65\n\t"
    "buffer_load_dwordx4 v13, s[60:63], s64 offen lds\n\t"
    "v_exp_f32 v27, v27\n\t"
    "v_mfma_f32_32x32x16_bf16 v[72:87], a[172:175], a[12:15], v[72:87]\n\t"
    "ds_read_b64_tr_b16 a[168:169], v16 offset:14336\n\t"
    "ds_read_b64_tr_b16 a[170:171], v16 offset:15360\n\t"
    "ds_read_b64_tr_b16 a[172:173], v17 offset:14336\n\t"
    "ds_read_b64_tr_b16 a[174:175], v17 offset:15360\n\t"
    "s_waitcnt lgkmcnt(8)\n\t"
    "v_mfma_f32_32x32x16_bf16 v[56:71], a[144:147], a[16:19], v[56:71]\n\t"
    "s_add_u32 m0, %[m0b], 80896\n\t"
    "s_add_u32 s64, s64, s65\n\t"
    "buffer_load_dwordx4 v13, s[60:63], s64 offen lds\n\t"
    "v_exp_f32 v28, v28\n\t"
    "v_mfma_f32_32x32x16_bf16 v[72:87], a[148:151], a[16:19], v[72:87]\n\t"
    "ds_read_b64_tr_b16 a[144:145], v16 offset:16384\n\t"
    "ds_read_b64_tr_b16 a[146:147], v16 offset:17408\n\t"
    "ds_read_b64_tr_b16 a[148:149], v17 offset:16384\n\t"
    "ds_read_b64_tr_b16 a[150:151], v17 offset:17408\n\t"
    "v_mfma_f32_32x32x16_bf16 v[56:71], a[152:155], a[20:23], v[56:71]\n\t"
    "s_add_u32 m0, %[m0b], 81920\n\t"
    "s_add_u32 s64, s64, s65\n\t"
    "buffer_load_dwordx4 v13, s[60:63], s64 offen lds\n\t"
    "s_add_u32 s64, s64, s66\n\t"
    "v_mfma_f32_32x32x16_bf16 v[72:87], a[156:159], a[20:23], v[72:87]\n\t"
    "ds_read_b64_tr_b16 a[152:153], v16 offset:18432\n\t"
    "ds_read_b64_tr_b16 a[154:155], v16 offset:19456\n\t"
    "ds_read_b64_tr_b16 a[156:157], v17 offset:18432\n\t"
    "ds_read_b64_tr_b16 a[158:159], v17 offset:19456\n\t"
    "s_waitcnt lgkmcnt(8)\n\t"
    "v_mfma_f32_32x32x16_bf16 v[56:71], a[160:163], a[24:27], v[56:71]\n\t"
    "s_add_u32 m0, %[m0b], 110592\n\t"
    "s_add_u32 s64, s64, s65\n\t"
    "buffer_load_dwordx4 v13, s[60:63], s64 offen lds\n\t"
    "v_exp_f32 v29, v29\n\t"
    "v_mfma_f32_32x32x16_bf16 v[72:87], a[164:167], a[24:27], v[72:87]\n\t"
    "ds_read_b64_tr_b16 a[160:161], v16 offset:20480\n\t"
    "ds_read_b64_tr_b16 a[162:163], v16 offset:21504\n\t"
    "ds_read_b64_tr_b16 a[164:165], v17 offset:20480\n\t"
    "ds_read_b64_tr_b16 a[166:167], v17 offset:21504\n\t"
    "v_mfma_f32_32x32x16_bf16 v[56:71], a[168:171], a[28:31], v[56:71]\n\t"
    "s_add_u32 m0, %[m0b], 111616\n\t"
    "s_add_u32 s64, s64, s65\n\t"
    "buffer_load_dwordx4 v13, s[60:63], s64 offen lds\n\t"
    "v_exp_f32 v30, v30\n\t"
    "v_mfma_f32_32x32x16_bf16 v[72:87], a[172:175], a[28:31], v[72:87]\n\t"
    "ds_read_b64_tr_b16 a[168:169], v16 offset:22528\n\t"
    "ds_read_b64_tr_b16 a[170:171], v16 offset:23552\n\t"
    "ds_read_b64_tr_b16 a[172:173], v17 offset:22528\n\t"
    "ds_read_b64_tr_b16 a[174:175], v17 offset:23552\n\t"
    "s_waitcnt lgkmcnt(8)\n\t"
    "v_mfma_f32_32x32x16_bf16 v[56:71], a[144:147], a[32:35], v[56:71]\n\t"
    "s_add_u32 m0, %[m0b], 112640\n\t"
    "s_add_u32 s64, s64, s65\n\t"
    "buffer_load_dwordx4 v13, s[60:63], s64 offen lds\n\t"
    "v_exp_f32 v31, v31\n\t"
    "v_mfma_f32_32x32x16_bf16 v[72:87], a[148:151], a[32:35], v[72:87]\n\t"
    "ds_read_b64_tr_b16 a[144:145], v16 offset:24576\n\t"
    "ds_read_b64_tr_b16 a[146:147], v16 offset:25600\n\t"
    "ds_read_b64_tr_b16 a[148:149], v17 offset:24576\n\t"
    "ds_read_b64_tr_b16 a[150:151], v17 offset:25600\n\t"
    "v_mfma_f32_32x32x16_bf16 v[56:71], a[152:155], a[36:39], v[56:71]\n\t"
    "s_add_u32 m0, %[m0b], 113664\n\t"
    "s_add_u32 s64, s64, s65\n\t"
    "buffer_load_dwordx4 v13, s[60:63], s64 offen lds\n\t"
    "v_exp_f32 v32, v32\n\t"
    "v_mfma_f32_32x32x16_bf16 v[72:87], a[156:159], a[36:39], v[72:87]\n\t"
    "ds_read_b64_tr_b16 a[152:153], v16 offset:26624\n\t"
    "ds_read_b64_tr_b16 a[154:155], v16 offset:27648\n\t"
    "ds_read_b64_tr_b16 a[156:157], v17 offset:26624\n\t"
    "ds_read_b64_tr_b16 a[158:159], v17 offset:27648\n\t"
    "s_waitcnt lgkmcnt(8)\n\t"
    "v_mfma_f32_32x32x16_bf16 v[56:71], a[160:163], a[40:43], v[56:71]\n\t"
    "s_add_u32 m0, %[m0b], 114688\n\t"
    "s_add_u32 s64, s64, s65\n\t"
    "buffer_load_dwordx4 v13, s[60:63], s64 offen lds\n\t"
    "v_exp_f32 v33, v33\n\t"
    "v_mfma_f32_32x32x16_bf16 v[72:87], a[164:167], a[40:43], v[72:87]\n\t"
    "ds_read_b64_tr_b16 a[160:161], v16 offset:28672\n\t"
    "ds_read_b64_tr_b16 a[162:163], v16 offset:29696\n\t"
    "ds_read_b64_tr_b16 a[164:165], v17 offset:28672\n\t"
    "ds_read_b64_tr_b16 a[166:167], v17 offset:29696\n\t"
    "v_mfma_f32_32x32x16_bf16 v[56:71], a[168:171], a[44:47], v[56:71]\n\t"
    "s_add_u32 m0, %[m0b], 115712\n\t"
    "s_add_u32 s64, s64, s65\n\t"
    "buffer_load_dwordx4 v13, s[60:63], s64 offen lds\n\t"
    "v_exp_f32 v34, v34\n\t"
    "v_mfma_f32_32x32x16_bf16 v[72:87], a[172:175], a[44:47], v[72:87]\n\t"
    "ds_read_b64_tr_b16 a[168:169], v16 offset:30720\n\t"
    "ds_read_b64_tr_b16 a[170:171], v16 offset:31744\n\t"
    "ds_read_b64_tr_b16 a[172:173], v17 offset:30720\n\t"
    "ds_read_b64_tr_b16 a[174:175], v17 offset:31744\n\t"
    "s_waitcnt lgkmcnt(8)\n\t"
    "v_mfma_f32_32x32x16_bf16 v[56:71], a[144:147], a[48:51], v[56:71]\n\t"
    "s_add_u32 m0, %[m0b], 116736\n\t"
    "s_add_u32 s64, s64, s65\n\t"
    "buffer_load_dwordx4 v13, s[60:63], s64 offen lds\n\t"
    "v_exp_f32 v35, v35\n\t"
    "v_mfma_f32_32x32x16_bf16 v[72:87], a[148:151], a[48:51], v[72:87]\n\t"
    "ds_read_b64_tr_b16 a[144:145], v16 offset:32768\n\t"
    "ds_read_b64_tr_b16 a[146:147], v16 offset:33792\n\t"
    "ds_read_b64_tr_b16 a[148:149], v17 offset:32768\n\t"
    "ds_read_b64_tr_b16 a[150:151], v17 offset:33792\n\t"
    "v_mfma_f32_32x32x16_bf16 v[56:71], a[152:155], a[52:55], v[56:71]\n\t"
    "s_add_u32 m0, %[m0b], 117760\n\t"
    "s_add_u32 s64, s64, s65\n\t"
    "buffer_load_dwordx4 v13, s[60:63], s64 offen lds\n\t"
    "v_exp_f32 v36, v36\n\t"
    "v_mfma_f32_32x32x16_bf16 v[72:87], a[156:159], a[52:55], v[72:87]\n\t"
    "ds_read_b64_tr_b16 a[152:153], v16 offset:34816\n\t"
    "ds_read_b64_tr_b16 a[154:155], v16 offset:35840\n\t"
    "ds_read_b64_tr_b16 a[156:157], v17 offset:34816\n\t"
    "ds_read_b64_tr_b16 a[158:159], v17 offset:35840\n\t"
    "s_waitcnt lgkmcnt(8)\n\t"
    "v_mfma_f32_32x32x16_bf16 v[56:71], a[160:163], a[56:59], v[56:71]\n\t"
    "s_add_u32 m0, %[m0b], 118784\n\t"
    "s_add_u32 s64, s64, s65\n\t"
    "buffer_load_dwordx4 v13, s[60:63], s64 offen lds\n\t"
    "s_add_u32 s64, s64, s67\n\t"
    "s_sub_u32 s64, s64, 1152\n\t"
    "v_mfma_f32_32x32x16_bf16 v[72:87], a[164:167], a[56:59], v[72:87]\n\t"
    "ds_read_b64_tr_b16 a[160:161], v18 offset:0\n\t"
    "ds_read_b64_tr_b16 a[162:163], v18 offset:1024\n\t"
    "ds_read_b64_tr_b16 a[164:165], v19 offset:0\n\t"
    "ds_read_b64_tr_b16 a[166:167], v19 offset:1024\n\t"
    "v_mfma_f32_32x32x16_bf16 v[56:71], a[168:171], a[60:63], v[56:71]\n\t"
    "v_exp_f32 v37, v37\n\t"
    "v_exp_f32 v38, v38\n\t"
    "v_exp_f32 v39, v39\n\t"
    "v_mfma_f32_32x32x16_bf16 v[72:87], a[172:175], a[60:63], v[72:87]\n\t"
    "ds_read_b64_tr_b16 a[168:169], v18 offset:2048\n\t"
    "ds_read_b64_tr_b16 a[170:171], v18 offset:3072\n\t"
    "ds_read_b64_tr_b16 a[172:173], v19 offset:2048\n\t"
    "ds_read_b64_tr_b16 a[174:175], v19 offset:3072\n\t"
    "s_waitcnt lgkmcnt(8)\n\t"
    "v_mfma_f32_32x32x16_bf16 v[56:71], a[144:147], a[64:67], v[56:71]\n\t"
    "v_exp_f32 v40, v40\n\t"
    "v_exp_f32 v41, v41\n\t"
    "v_exp_f32 v42, v42\n\t"
    "v_mfma_f32_32x32x16_bf16 v[72:87], a[148:151], a[64:67], v[72:87]\n\t"
    "ds_read_b64_tr_b16 a[144:145], v18 offset:4096\n\t"
    "ds_read_b64_tr_b16 a[146:147], v18 offset:5120\n\t"
    "ds_read_b64_tr_b16 a[148:149], v19 offset:4096\n\t"
    "ds_read_b64_tr_b16 a[150:151], v19 offset:5120\n\t"
    "v_mfma_f32_32x32x16_bf16 v[56:71], a[152:155], a[68:71], v[56:71]\n\t"
    "v_exp_f32 v43, v43\n\t"
    "v_exp_f32 v44, v44\n\t"
    "v_exp_f32 v45, v45\n\t"
    "v_mfma_f32_32x32x16_bf16 v[72:87], a[156:159], a[68:71], v[72:87]\n\t"
    "ds_read_b64_tr_b16 a[152:153], v18 offset:6144\n\t"
    "ds_read_b64_tr_b16 a[154:155], v18 offset:7168\n\t"
    "ds_read_b64_tr_b16 a[156:157], v19 offset:6144\n\t"
    "ds_read_b64_tr_b16 a[158:159], v19 offset:7168\n\t"
    "s_waitcnt lgkmcnt(8)\n\t"
    "v_mfma_f32_32x32x16_bf16 v[56:71], a[160:163], a[72:75], v[56:71]\n\t"
    "v_exp_f32 v46, v46\n\t"
    "v_exp_f32 v47, v47\n\t"
    "v_exp_f32 v48, v48\n\t"
    "v_mfma_f32_32x32x16_bf16 v[72:87], a[164:167], a[72:75], v[72:87]\n\t"
    "ds_read_b64_tr_b16 a[160:161], v18 offset:8192\n\t"
    "ds_read_b64_tr_b16 a[162:163], v18 offset:9216\n\t"
    "ds_read_b64_tr_b16 a[164:165], v19 offset:8192\n\t"
    "ds_read_b64_tr_b16 a[166:167], v19 offset:9216\n\t"
    "v_mfma_f32_32x32x16_bf16 v[56:71], a[168:171], a[76:79], v[56:71]\n\t"
    "v_exp_f32 v49, v49\n\t"
    "v_exp_f32 v50, v50\n\t"
    "v_exp_f32 v51, v51\n\t"
    "v_mfma_f32_32x32x16_bf16 v[72:87], a[172:175], a[76:79], v[72:87]\n\t"
    "ds_read_b64_tr_b16 a[168:169], v18 offset:10240\n\t"
    "ds_read_b64_tr_b16 a[170:171], v18 offset:11264\n\t"
    "ds_read_b64_tr_b16 a[172:173], v19 offset:10240\n\t"
    "ds_read_b64_tr_b16 a[174:175], v19 offset:11264\n\t"
    "s_waitcnt lgkmcnt(8)\n\t"
    "v_mfma_f32_32x32x16_bf16 v[56:71], a[144:147], a[80:83], v[56:71]\n\t"
    "v_exp_f32 v52, v52\n\t"
    "v_exp_f32 v53, v53\n\t"
    "v_exp_f32 v54, v54\n\t"
    "v_mfma_f32_32x32x16_bf16 v[72:87], a[148:151], a[80:83], v[72:87]\n\t"
    "ds_read_b64_tr_b16 a[144:145], v18 offset:12288\n\t"
    "ds_read_b64_tr_b16 a[146:147], v18 offset:13312\n\t"
    "ds_read_b64_tr_b16 a[148:149], v19 offset:12288\n\t"
    "ds_read_b64_tr_b16 a[150:151], v19 offset:13312\n\t"
    "v_mfma_f32_32x32x16_bf16 v[56:71], a[152:155], a[84:87], v[56:71]\n\t"
    "v_exp_f32 v55, v55\n\t"
    "s_nop 0\n\t"
    "v_pk_add_f32 v[24:25], v[24:25], v[126:127] op_sel_hi:[1,0]\n\t"
    "v_pk_add_f32 v[26:27], v[26:27], v[126:127] op_sel_hi:[1,0]\n\t"
    "v_pk_add_f32 v[28:29], v[28:29], v[126:127] op_sel_hi:[1,0]\n\t"
    "v_pk_add_f32 v[30:31], v[30:31], v[126:127] op_sel_hi:[1,0]\n\t"
    "v_pk_add_f32 v[32:33], v[32:33], v[126:127] op_sel_hi:[1,0]\n\t"
    "v_pk_add_f32 v[34:35], v[34:35], v[126:127] op_sel_hi:[1,0]\n\t"
    "v_pk_add_f32 v[36:37], v[36:37], v[126:127] op_sel_hi:[1,0]\n\t"
    "v_pk_add_f32 v[38:39], v[38:39], v[126:127] op_sel_hi:[1,0]\n\t"
    "v_pk_add_f32 v[40:41], v[40:41], v[126:127] op_sel_hi:[1,0]\n\t"
    "v_pk_add_f32 v[42:43], v[42:43], v[126:127] op_sel_hi:[1,0]\n\t"
    "v_pk_add_f32 v[44:45], v[44:45], v[126:127] op_sel_hi:[1,0]\n\t"
    "v_pk_add_f32 v[46:47], v[46:47], v[126:127] op_sel_hi:[1,0]\n\t"
    "v_pk_add_f32 v[48:49], v[48:49], v[126:127] op_sel_hi:[1,0]\n\t"
    "v_pk_add_f32 v[50:51], v[50:51], v[126:127] op_sel_hi:[1,0]\n\t"
    "v_pk_add_f32 v[52:53], v[52:53], v[126:127] op_sel_hi:[1,0]\n\t"
    "v_pk_add_f32 v[54:55], v[54:55], v[126:127] op_sel_hi:[1,0]\n\t"
    "v_mfma_f32_32x32x16_bf16 v[72:87], a[156:159], a[84:87], v[72:87]\n\t"
    "ds_read_b64_tr_b16 a[152:153], v18 offset:14336\n\t"
    "ds_read_b64_tr_b16 a[154:155], v18 offset:15360\n\t"
    "ds_read_b64_tr_b16 a[156:157], v19 offset:14336\n\t"
    "ds_read_b64_tr_b16 a[158:159], v19 offset:15360\n\t"
    "s_waitcnt lgkmcnt(8)\n\t"
    "v_mfma_f32_32x32x16_bf16 v[56:71], a[160:163], a[88:91], v[56:71]\n\t"
    "v_rcp_f32 v24, v24\n\t"
    "v_rcp_f32 v25, v25\n\t"
    "v_rcp_f32 v26, v26\n\t"
    "v_mfma_f32_32x32x16_bf16 v[72:87], a[164:167], a[88:91], v[72:87]\n\t"
    "ds_read_b64_tr_b16 a[160:161], v18 offset:16384\n\t"
    "ds_read_b64_tr_b16 a[162:163], v18 offset:17408\n\t"
    "ds_read_b64_tr_b16 a[164:165], v19 offset:16384\n\t"
    "ds_read_b64_tr_b16 a[166:167], v19 offset:17408\n\t"
    "v_mfma_f32_32x32x16_bf16 v[56:71], a[168:171], a[92:95], v[56:71]\n\t"
    "v_rcp_f32 v27, v27\n\t"
    "v_rcp_f32 v28, v28\n\t"
    "v_rcp_f32 v29, v29\n\t"
    "v_mfma_f32_32x32x16_bf16 v[72:87], a[172:175], a[92:95], v[72:87]\n\t"
    "ds_read_b64_tr_b16 a[168:169], v18 offset:18432\n\t"
    "ds_read_b64_tr_b16 a[170:171], v18 offset:19456\n\t"
    "ds_read_b64_tr_b16 a[172:173], v19 offset:18432\n\t"
    "ds_read_b64_tr_b16 a[174:175], v19 offset:19456\n\t"
    "s_waitcnt lgkmcnt(8)\n\t"
    "v_mfma_f32_32x32x16_bf16 v[56:71], a[144:147], a[96:99], v[56:71]\n\t"
    "v_rcp_f32 v30, v30\n\t"
    "v_rcp_f32 v31, v31\n\t"
    "v_rcp_f32 v32, v32\n\t"
    "v_mfma_f32_32x32x16_bf16 v[72:87], a[148:151], a[96:99], v[72:87]\n\t"
    "ds_read_b64_tr_b16 a[144:145], v18 offset:20480\n\t"
    "ds_read_b64_tr_b16 a[146:147], v18 offset:21504\n\t"
    "ds_read_b64_tr_b16 a[148:149], v19 offset:20480\n\t"
    "ds_read_b64_tr_b16 a[150:151], v19 offset:21504\n\t"
    "v_mfma_f32_32x32x16_bf16 v[56:71], a[152:155], a[100:103], v[56:71]\n\t"
    "v_rcp_f32 v33, v33\n\t"
    "v_rcp_f32 v34, v34\n\t"
    "v_rcp_f32 v35, v35\n\t"
    "v_mfma_f32_32x32x16_bf16 v[72:87], a[156:159], a[100:103], v[72:87]\n\t"
    "ds_read_b64_tr_b16 a[152:153], v18 offset:22528\n\t"
    "ds_read_b64_tr_b16 a[154:155], v18 offset:23552\n\t"
    "ds_read_b64_tr_b16 a[156:157], v19 offset:22528\n\t"
    "ds_read_b64_tr_b16 a[158:159], v19 offset:23552\n\t"
    "s_waitcnt lgkmcnt(8)\n\t"
    "v_mfma_f32_32x32x16_bf16 v[56:71], a[160:163], a[104:107], v[56:71]\n\t"
    "v_rcp_f32 v36, v36\n\t"
    "v_rcp_f32 v37, v37\n\t"
    "v_rcp_f32 v38, v38\n\t"
    "v_mfma_f32_32x32x16_bf16 v[72:87], a[164:167], a[104:107], v[72:87]\n\t"
    "ds_read_b64_tr_b16 a[160:161], v18 offset:24576\n\t"
    "ds_read_b64_tr_b16 a[162:163], v18 offset:25600\n\t"
    "ds_read_b64_tr_b16 a[164:165], v19 offset:24576\n\t"
    "ds_read_b64_tr_b16 a[166:167], v19 offset:25600\n\t"
    "v_mfma_f32_32x32x16_bf16 v[56:71], a[168:171], a[108:111], v[56:71]\n\t"
    "v_rcp_f32 v39, v39\n\t"
    "v_rcp_f32 v40, v40\n\t"
    "v_rcp_f32 v41, v41\n\t"
    "v_mfma_f32_32x32x16_bf16 v[72:87], a[172:175], a[108:111], v[72:87]\n\t"
    "ds_read_b64_tr_b16 a[168:169], v18 offset:26624\n\t"
    "ds_read_b64_tr_b16 a[170:171], v18 offset:27648\n\t"
    "ds_read_b64_tr_b16 a[172:173], v19 offset:26624\n\t"
    "ds_read_b64_tr_b16 a[174:175], v19 offset:27648\n\t"
    "s_waitcnt lgkmcnt(8)\n\t"
    "v_mfma_f32_32x32x16_bf16 v[56:71], a[144:147], a[112:115], v[56:71]\n\t"
    "v_rcp_f32 v42, v42\n\t"
    "v_rcp_f32 v43, v43\n\t"
    "v_rcp_f32 v44, v44\n\t"
    "v_mfma_f32_32x32x16_bf16 v[72:87], a[148:151], a[112:115], v[72:87]\n\t"
    "ds_read_b64_tr_b16 a[144:145], v18 offset:28672\n\t"
    "ds_read_b64_tr_b16 a[146:147], v18 offset:29696\n\t"
    "ds_read_b64_tr_b16 a[148:149], v19 offset:28672\n\t"
    "ds_read_b64_tr_b16 a[150:151], v19 offset:29696\n\t"
    "v_mfma_f32_32x32x16_bf16 v[56:71], a[152:155], a[116:119], v[56:71]\n\t"
    "v_rcp_f32 v45, v45\n\t"
    "v_rcp_f32 v46, v46\n\t"
    "v_rcp_f32 v47, v47\n\t"
    "v_mfma_f32_32x32x16_bf16 v[72:87], a[156:159], a[116:119], v[72:87]\n\t"
    "ds_read_b64_tr_b16 a[152:153], v18 offset:30720\n\t"
    "ds_read_b64_tr_b16 a[154:155], v18 offset:31744\n\t"
    "ds_read_b64_tr_b16 a[156:157], v19 offset:30720\n\t"
    "ds_read_b64_tr_b16 a[158:159], v19 offset:31744\n\t"
    "s_waitcnt lgkmcnt(8)\n\t"
    "v_mfma_f32_32x32x16_bf16 v[56:71], a[160:163], a[120:123], v[56:71]\n\t"
    "v_rcp_f32 v48, v48\n\t"
    "v_rcp_f32 v49, v49\n\t"
    "v_rcp_f32 v50, v50\n\t"
    "v_mfma_f32_32x32x16_bf16 v[72:87], a[164:167], a[120:123], v[72:87]\n\t"
    "ds_read_b64_tr_b16 a[160:161], v18 offset:32768\n\t"
    "ds_read_b64_tr_b16 a[162:163], v18 offset:33792\n\t"
    "ds_read_b64_tr_b16 a[164:165], v19 offset:32768\n\t"
    "ds_read_b64_tr_b16 a[166:167], v19 offset:33792\n\t"
    "v_mfma_f32_32x32x16_bf16 v[56:71], a[168:171], a[124:127], v[56:71]\n\t"
    "v_rcp_f32 v51, v51\n\t"
    "v_rcp_f32 v52, v52\n\t"
    "v_rcp_f32 v53, v53\n\t"
    "v_mfma_f32_32x32x16_bf16 v[72:87], a[172:175], a[124:127], v[72:87]\n\t"
    "ds_read_b64_tr_b16 a[168:169], v18 offset:34816\n\t"
    "ds_read_b64_tr_b16 a[170:171], v18 offset:35840\n\t"
    "ds_read_b64_tr_b16 a[172:173], v19 offset:34816\n\t"
    "ds_read_b64_tr_b16 a[174:175], v19 offset:35840\n\t"
    "s_waitcnt lgkmcnt(8)\n\t"
    "v_mfma_f32_32x32x16_bf16 v[56:71], a[144:147], a[128:131], v[56:71]\n\t"
    "s_waitcnt vmcnt(0) lgkmcnt(0)\n\t"
    "s_barrier\n\t"
    "v_mfma_f32_32x32x16_bf16 v[72:87], a[148:151], a[128:131], v[72:87]\n\t"
    "ds_read_b64_tr_b16 a[144:145], v20 offset:0\n\t"
    "ds_read_b64_tr_b16 a[146:147], v20 offset:1024\n\t"
    "ds_read_b64_tr_b16 a[148:149], v21 offset:0\n\t"
    "ds_read_b64_tr_b16 a[150:151], v21 offset:1024\n\t"
    "v_mfma_f32_32x32x16_bf16 v[56:71], a[152:155], a[132:135], v[56:71]\n\t"
    "s_add_u32 m0, %[m0b], 0\n\t"
    "s_add_u32 s64, s64, s65\n\t"
    "buffer_load_dwordx4 v13, s[60:63], s64 offen lds\n\t"
    "v_rcp_f32 v54, v54\n\t"
    "v_mfma_f32_32x32x16_bf16 v[72:87], a[156:159], a[132:135], v[72:87]\n\t"
    "ds_read_b64_tr_b16 a[152:153], v20 offset:2048\n\t"
    "ds_read_b64_tr_b16 a[154:155], v20 offset:3072\n\t"
    "ds_read_b64_tr_b16 a[156:157], v21 offset:2048\n\t"
    "ds_read_b64_tr_b16 a[158:159], v21 offset:3072\n\t"
    "s_waitcnt lgkmcnt(8)\n\t"
    "v_mfma_f32_32x32x16_bf16 v[56:71], a[160:163], a[136:139], v[56:71]\n\t"
    "s_add_u32 m0, %[m0b], 1024\n\t"
    "s_add_u32 s64, s64, s65\n\t"
    "buffer_load_dwordx4 v13, s[60:63], s64 offen lds\n\t"
    "v_rcp_f32 v55, v55\n\t"
    "v_mfma_f32_32x32x16_bf16 v[72:87], a[164:167], a[136:139], v[72:87]\n\t"
    "ds_read_b64_tr_b16 a[160:161], v20 offset:4096\n\t"
    "ds_read_b64_tr_b16 a[162:163], v20 offset:5120\n\t"
    "ds_read_b64_tr_b16 a[164:165], v21 offset:4096\n\t"
    "ds_read_b64_tr_b16 a[166:167], v21 offset:5120\n\t"
    "v_mfma_f32_32x32x16_bf16 v[56:71], a[168:171], a[140:143], v[56:71]\n\t"
    "s_add_u32 m0, %[m0b], 2048\n\t"
    "s_add_u32 s64, s64, s65\n\t"
    "buffer_load_dwordx4 v13, s[60:63], s64 offen lds\n\t"
    "v_mfma_f32_32x32x16_bf16 v[72:87], a[172:175], a[140:143], v[72:87]\n\t"
    "v_add_u32 v12, 0x100, v12\n\t"
    "v_pk_mul_f32 v[88:89], v[88:89], v[24:25]\n\t"
    "v_pk_mul_f32 v[90:91], v[90:91], v[26:27]\n\t"
    "ds_read_b128 v[24:27], v12 offset:0\n\t"
    "v_pk_mul_f32 v[92:93], v[92:93], v[28:29]\n\t"
    "v_pk_mul_f32 v[94:95], v[94:95], v[30:31]\n\t"
    "ds_read_b128 v[28:31], v12 offset:32\n\t"
    "v_pk_mul_f32 v[96:97], v[96:97], v[32:33]\n\t"
    "v_pk_mul_f32 v[98:99], v[98:99], v[34:35]\n\t"
    "ds_read_b128 v[32:35], v12 offset:64\n\t"
    "v_pk_mul_f32 v[100:101], v[100:101], v[36:37]\n\t"
    "v_pk_mul_f32 v[102:103], v[102:103], v[38:39]\n\t"
    "ds_read_b128 v[36:39], v12 offset:96\n\t"
    "v_pk_mul_f32 v[104:105], v[104:105], v[40:41]\n\t"
    "v_pk_mul_f32 v[106:107], v[106:107], v[42:43]\n\t"
    "ds_read_b128 v[40:43], v12 offset:128\n\t"
    "v_pk_mul_f32 v[108:109], v[108:109], v[44:45]\n\t"
    "v_pk_mul_f32 v[110:111], v[110:111], v[46:47]\n\t"
    "ds_read_b128 v[44:47], v12 offset:160\n\t"
    "v_pk_mul_f32 v[112:113], v[112:113], v[48:49]\n\t"
    "v_pk_mul_f32 v[114:115], v[114:115], v[50:51]\n\t"
    "ds_read_b128 v[48:51], v12 offset:192\n\t"
    "v_pk_mul_f32 v[116:117], v[116:117], v[52:53]\n\t"
    "v_pk_mul_f32 v[118:119], v[118:119], v[54:55]\n\t"
    "ds_read_b128 v[52:55], v12 offset:224\n\t"
    "s_set_gpr_idx_on s69, gpr_idx(DST)\n\t"
    "s_nop 0\n\t"
    "v_cvt_pk_bf16_f32 v128, v88, v89\n\t"
    "v_cvt_pk_bf16_f32 v129, v90, v91\n\t"
    "v_cvt_pk_bf16_f32 v130, v92, v93\n\t"
    "v_cvt_pk_bf16_f32 v131, v94, v95\n\t"
    "v_cvt_pk_bf16_f32 v132, v96, v97\n\t"
    "v_cvt_pk_bf16_f32 v133, v98, v99\n\t"
    "v_cvt_pk_bf16_f32 v134, v100, v101\n\t"
    "v_cvt_pk_bf16_f32 v135, v102, v103\n\t"
    "v_cvt_pk_bf16_f32 v136, v104, v105\n\t"
    "v_cvt_pk_bf16_f32 v137, v106, v107\n\t"
    "v_cvt_pk_bf16_f32 v138, v108, v109\n\t"
    "v_cvt_pk_bf16_f32 v139, v110, v111\n\t"
    "v_cvt_pk_bf16_f32 v140, v112, v113\n\t"
    "v_cvt_pk_bf16_f32 v141, v114, v115\n\t"
    "v_cvt_pk_bf16_f32 v142, v116, v117\n\t"
    "v_cvt_pk_bf16_f32 v143, v118, v119\n\t"
    "s_set_gpr_idx_off\n\t"
    "s_waitcnt lgkmcnt(0)\n\t"
    "v_pk_fma_f32 v[56:57], v[124:125], v[24:25], v[56:57] op_sel_hi:[0,1,1]\n\t"
    "v_pk_fma_f32 v[58:59], v[124:125], v[26:27], v[58:59] op_sel_hi:[0,1,1]\n\t"
    "v_pk_fma_f32 v[60:61], v[124:125], v[28:29], v[60:61] op_sel_hi:[0,1,1]\n\t"
    "v_pk_fma_f32 v[62:63], v[124:125], v[30:31], v[62:63] op_sel_hi:[0,1,1]\n\t"
    "v_pk_fma_f32 v[64:65], v[124:125], v[32:33], v[64:65] op_sel_hi:[0,1,1]\n\t"
    "v_pk_fma_f32 v[66:67], v[124:125], v[34:35], v[66:67] op_sel_hi:[0,1,1]\n\t"
    "v_pk_fma_f32 v[68:69], v[124:125], v[36:37], v[68:69] op_sel_hi:[0,1,1]\n\t"
    "v_pk_fma_f32 v[70:71], v[124:125], v[38:39], v[70:71] op_sel_hi:[0,1,1]\n\t"
    "v_pk_fma_f32 v[72:73], v[124:125], v[40:41], v[72:73] op_sel_hi:[0,1,1]\n\t"
    "v_pk_fma_f32 v[74:75], v[124:125], v[42:43], v[74:75] op_sel_hi:[0,1,1]\n\t"
    "v_pk_fma_f32 v[76:77], v[124:125], v[44:45], v[76:77] op_sel_hi:[0,1,1]\n\t"
    "v_pk_fma_f32 v[78:79], v[124:125], v[46:47], v[78:79] op_sel_hi:[0,1,1]\n\t"
    "v_pk_fma_f32 v[80:81], v[124:125], v[48:49], v[80:81] op_sel_hi:[0,1,1]\n\t"
    "v_pk_fma_f32 v[82:83], v[124:125], v[50:51], v[82:83] op_sel_hi:[0,1,1]\n\t"
    "v_pk_fma_f32 v[84:85], v[124:125], v[52:53], v[84:85] op_sel_hi:[0,1,1]\n\t"
    "v_pk_fma_f32 v[86:87], v[124:125], v[54:55], v[86:87] op_sel_hi:[0,1,1]\n\t"
    "v_pk_mul_f32 v[24:25], v[56:57], v[56:57]\n\t"
    "v_pk_mul_f32 v[26:27], v[58:59], v[58:59]\n\t"
    "v_pk_mul_f32 v[28:29], v[60:61], v[60:61]\n\t"
    "v_pk_mul_f32 v[30:31], v[62:63], v[62:63]\n\t"
    "v_pk_mul_f32 v[32:33], v[64:65], v[64:65]\n\t"
    "v_pk_mul_f32 v[34:35], v[66:67], v[66:67]\n\t"
    "v_pk_mul_f32 v[36:37], v[68:69], v[68:69]\n\t"
    "v_pk_mul_f32 v[38:39], v[70:71], v[70:71]\n\t"
    "v_pk_mul_f32 v[40:41], v[72:73], v[72:73]\n\t"
    "v_pk_mul_f32 v[42:43], v[74:75], v[74:75]\n\t"
    "v_pk_mul_f32 v[44:45], v[76:77], v[76:77]\n\t"
    "v_pk_mul_f32 v[46:47], v[78:79], v[78:79]\n\t"
    "v_pk_mul_f32 v[48:49], v[80:81], v[80:81]\n\t"
    "v_pk_mul_f32 v[50:51], v[82:83], v[82:83]\n\t"
    "v_pk_mul_f32 v[52:53], v[84:85], v[84:85]\n\t"
    "v_pk_mul_f32 v[54:55], v[86:87], v[86:87]\n\t"
    "v_pk_fma_f32 v[24:25], v[24:25], v[122:123], v[120:121] op_sel_hi:[1,0,0]\n\t"
    "v_pk_fma_f32 v[26:27], v[26:27], v[122:123], v[120:121] op_sel_hi:[1,0,0]\n\t"
    "v_pk_fma_f32 v[28:29], v[28:29], v[122:123], v[120:121] op_sel_hi:[1,0,0]\n\t"
    "v_pk_fma_f32 v[30:31], v[30:31], v[122:123], v[120:121] op_sel_hi:[1,0,0]\n\t"
    "v_pk_fma_f32 v[32:33], v[32:33], v[122:123], v[120:121] op_sel_hi:[1,0,0]\n\t"
    "v_pk_fma_f32 v[34:35], v[34:35], v[122:123], v[120:121] op_sel_hi:[1,0,0]\n\t"
    "v_pk_fma_f32 v[36:37], v[36:37], v[122:123], v[120:121] op_sel_hi:[1,0,0]\n\t"
    "v_pk_fma_f32 v[38:39], v[38:39], v[122:123], v[120:121] op_sel_hi:[1,0,0]\n\t"
    "v_pk_fma_f32 v[40:41], v[40:41], v[122:123], v[120:121] op_sel_hi:[1,0,0]\n\t"
    "v_pk_fma_f32 v[42:43], v[42:43], v[122:123], v[120:121] op_sel_hi:[1,0,0]\n\t"
    "v_pk_fma_f32 v[44:45], v[44:45], v[122:123], v[120:121] op_sel_hi:[1,0,0]\n\t"
    "v_pk_fma_f32 v[46:47], v[46:47], v[122:123], v[120:121] op_sel_hi:[1,0,0]\n\t"
    "v_pk_fma_f32 v[48:49], v[48:49], v[122:123], v[120:121] op_sel_hi:[1,0,0]\n\t"
    "v_pk_fma_f32 v[50:51], v[50:51], v[122:123], v[120:121] op_sel_hi:[1,0,0]\n\t"
    "v_pk_fma_f32 v[52:53], v[52:53], v[122:123], v[120:121] op_sel_hi:[1,0,0]\n\t"
    "v_pk_fma_f32 v[54:55], v[54:55], v[122:123], v[120:121] op_sel_hi:[1,0,0]\n\t"
    "v_pk_mul_f32 v[24:25], v[56:57], v[24:25]\n\t"
    "v_pk_mul_f32 v[26:27], v[58:59], v[26:27]\n\t"
    "v_pk_mul_f32 v[28:29], v[60:61], v[28:29]\n\t"
    "v_pk_mul_f32 v[30:31], v[62:63], v[30:31]\n\t"
    "v_pk_mul_f32 v[32:33], v[64:65], v[32:33]\n\t"
    "v_pk_mul_f32 v[34:35], v[66:67], v[34:35]\n\t"
    "v_pk_mul_f32 v[36:37], v[68:69], v[36:37]\n\t"
    "v_pk_mul_f32 v[38:39], v[70:71], v[38:39]\n\t"
    "v_pk_mul_f32 v[40:41], v[72:73], v[40:41]\n\t"
    "v_pk_mul_f32 v[42:43], v[74:75], v[42:43]\n\t"
    "v_pk_mul_f32 v[44:45], v[76:77], v[44:45]\n\t"
    "v_pk_mul_f32 v[46:47], v[78:79], v[46:47]\n\t"
    "v_pk_mul_f32 v[48:49], v[80:81], v[48:49]\n\t"
    "v_pk_mul_f32 v[50:51], v[82:83], v[50:51]\n\t"
    "v_pk_mul_f32 v[52:53], v[84:85], v[52:53]\n\t"
    "v_pk_mul_f32 v[54:55], v[86:87], v[54:55]\n\t"
    "ds_read_b64_tr_b16 a[168:169], v20 offset:6144\n\t"
    "ds_read_b64_tr_b16 a[170:171], v20 offset:7168\n\t"
    "ds_read_b64_tr_b16 a[172:173], v21 offset:6144\n\t"
    "ds_read_b64_tr_b16 a[174:175], v21 offset:7168\n\t"
    "s_waitcnt lgkmcnt(8)\n\t"
    "v_mfma_f32_32x32x16_bf16 v[88:103], a[144:147], v[128:131], 0\n\t"
    "s_add_u32 m0, %[m0b], 3072\n\t"
    "s_add_u32 s64, s64, s65\n\t"
    "buffer_load_dwordx4 v13, s[60:63], s64 offen lds\n\t"
    "v_exp_f32 v24, v24\n\t"
    "v_exp_f32 v25, v25\n\t"
    "v_mfma_f32_32x32x16_bf16 v[104:119], a[148:151], v[128:131], 0\n\t"
    "ds_read_b64_tr_b16 a[144:145], v20 offset:8192\n\t"
    "ds_read_b64_tr_b16 a[146:147], v20 offset:9216\n\t"
    "ds_read_b64_tr_b16 a[148:149], v21 offset:8192\n\t"
    "ds_read_b64_tr_b16 a[150:151], v21 offset:9216\n\t"
    "v_mfma_f32_32x32x16_bf16 v[88:103], a[152:155], v[132:135], v[88:103]\n\t"
    "s_add_u32 m0, %[m0b], 4096\n\t"
    "s_add_u32 s64, s64, s65\n\t"
    "buffer_load_dwordx4 v13, s[60:63], s64 offen lds\n\t"
    "v_exp_f32 v26, v26\n\t"
    "v_exp_f32 v27, v27\n\t"
    "v_mfma_f32_32x32x16_bf16 v[104:119], a[156:159], v[132:135], v[104:119]\n\t"
    "ds_read_b64_tr_b16 a[152:153], v20 offset:10240\n\t"
    "ds_read_b64_tr_b16 a[154:155], v20 offset:11264\n\t"
    "ds_read_b64_tr_b16 a[156:157], v21 offset:10240\n\t"
    "ds_read_b64_tr_b16 a[158:159], v21 offset:11264\n\t"
    "s_waitcnt lgkmcnt(8)\n\t"
    "v_mfma_f32_32x32x16_bf16 v[88:103], a[160:163], v[136:139], v[88:103]\n\t"
    "s_add_u32 m0, %[m0b], 5120\n\t"
    "s_add_u32 s64, s64, s65\n\t"
    "buffer_load_dwordx4 v13, s[60:63], s64 offen lds\n\t"
    "v_exp_f32 v28, v28\n\t"
    "v_exp_f32 v29, v29\n\t"
    "v_mfma_f32_32x32x16_bf16 v[104:119], a[164:167], v[136:139], v[104:119]\n\t"
    "ds_read_b64_tr_b16 a[160:161], v20 offset:12288\n\t"
    "ds_read_b64_tr_b16 a[162:163], v20 offset:13312\n\t"
    "ds_read_b64_tr_b16 a[164:165], v21 offset:12288\n\t"
    "ds_read_b64_tr_b16 a[166:167], v21 offset:13312\n\t"
    "v_mfma_f32_32x32x16_bf16 v[88:103], a[168:171], v[140:143], v[88:103]\n\t"
    "s_add_u32 m0, %[m0b], 6144\n\t"
    "s_add_u32 s64, s64, s65\n\t"
    "buffer_load_dwordx4 v13, s[60:63], s64 offen lds\n\t"
    "v_exp_f32 v30, v30\n\t"
    "v_exp_f32 v31, v31\n\t"
    "v_mfma_f32_32x32x16_bf16 v[104:119], a[172:175], v[140:143], v[104:119]\n\t"
    "ds_read_b64_tr_b16 a[168:169], v20 offset:14336\n\t"
    "ds_read_b64_tr_b16 a[170:171], v20 offset:15360\n\t"
    "ds_read_b64_tr_b16 a[172:173], v21 offset:14336\n\t"
    "ds_read_b64_tr_b16 a[174:175], v21 offset:15360\n\t"
    "s_waitcnt lgkmcnt(8)\n\t"
    "v_mfma_f32_32x32x16_bf16 v[88:103], a[144:147], v[144:147], v[88:103]\n\t"
    "s_add_u32 m0, %[m0b], 7168\n\t"
    "s_add_u32 s64, s64, s65\n\t"
    "buffer_load_dwordx4 v13, s[60:63], s64 offen lds\n\t"
    "v_exp_f32 v32, v32\n\t"
    "v_exp_f32 v33, v33\n\t"
    "v_mfma_f32_32x32x16_bf16 v[104:119], a[148:151], v[144:147], v[104:119]\n\t"
    "ds_read_b64_tr_b16 a[144:145], v20 offset:16384\n\t"
    "ds_read_b64_tr_b16 a[146:147], v20 offset:17408\n\t"
    "ds_read_b64_tr_b16 a[148:149], v21 offset:16384\n\t"
    "ds_read_b64_tr_b16 a[150:151], v21 offset:17408\n\t"
    "v_mfma_f32_32x32x16_bf16 v[88:103], a[152:155], v[148:151], v[88:103]\n\t"
    "s_add_u32 m0, %[m0b], 8192\n\t"
    "s_add_u32 s64, s64, s65\n\t"
    "buffer_load_dwordx4 v13, s[60:63], s64 offen lds\n\t"
    "s_add_u32 s64, s64, s66\n\t"
    "v_exp_f32 v34, v34\n\t"
    "v_mfma_f32_32x32x16_bf16 v[104:119], a[156:159], v[148:151], v[104:119]\n\t"
    "ds_read_b64_tr_b16 a[152:153], v20 offset:18432\n\t"
    "ds_read_b64_tr_b16 a[154:155], v20 offset:19456\n\t"
    "ds_read_b64_tr_b16 a[156:157], v21 offset:18432\n\t"
    "ds_read_b64_tr_b16 a[158:159], v21 offset:19456\n\t"
    "s_waitcnt lgkmcnt(8)\n\t"
    "v_mfma_f32_32x32x16_bf16 v[88:103], a[160:163], v[152:155], v[88:103]\n\t"
    "s_add_u32 m0, %[m0b], 36864\n\t"
    "s_add_u32 s64, s64, s65\n\t"
    "buffer_load_dwordx4 v13, s[60:63], s64 offen lds\n\t"
    "v_exp_f32 v35, v35\n\t"
    "v_exp_f32 v36, v36\n\t"
    "v_mfma_f32_32x32x16_bf16 v[104:119], a[164:167], v[152:155], v[104:119]\n\t"
    "ds_read_b64_tr_b16 a[160:161], v20 offset:20480\n\t"
    "ds_read_b64_tr_b16 a[162:163], v20 offset:21504\n\t"
    "ds_read_b64_tr_b16 a[164:165], v21 offset:20480\n\t"
    "ds_read_b64_tr_b16 a[166:167], v21 offset:21504\n\t"
    "v_mfma_f32_32x32x16_bf16 v[88:103], a[168:171], v[156:159], v[88:103]\n\t"
    "s_add_u32 m0, %[m0b], 37888\n\t"
    "s_add_u32 s64, s64, s65\n\t"
    "buffer_load_dwordx4 v13, s[60:63], s64 offen lds\n\t"
    "v_exp_f32 v37, v37\n\t"
    "v_exp_f32 v38, v38\n\t"
    "v_mfma_f32_32x32x16_bf16 v[104:119], a[172:175], v[156:159], v[104:119]\n\t"
    "ds_read_b64_tr_b16 a[168:169], v20 offset:22528\n\t"
    "ds_read_b64_tr_b16 a[170:171], v20 offset:23552\n\t"
    "ds_read_b64_tr_b16 a[172:173], v21 offset:22528\n\t"
    "ds_read_b64_tr_b16 a[174:175], v21 offset:23552\n\t"
    "s_waitcnt lgkmcnt(8)\n\t"
    "v_mfma_f32_32x32x16_bf16 v[88:103], a[144:147], v[160:163], v[88:103]\n\t"
    "s_add_u32 m0, %[m0b], 38912\n\t"
    "s_add_u32 s64, s64, s65\n\t"
    "buffer_load_dwordx4 v13, s[60:63], s64 offen lds\n\t"
    "v_exp_f32 v39, v39\n\t"
    "v_exp_f32 v40, v40\n\t"
    "v_mfma_f32_32x32x16_bf16 v[104:119], a[148:151], v[160:163], v[104:119]\n\t"
    "ds_read_b64_tr_b16 a[144:145], v20 offset:24576\n\t"
    "ds_read_b64_tr_b16 a[146:147], v20 offset:25600\n\t"
    "ds_read_b64_tr_b16 a[148:149], v21 offset:24576\n\t"
    "ds_read_b64_tr_b16 a[150:151], v21 offset:25600\n\t"
    "v_mfma_f32_32x32x16_bf16 v[88:103], a[152:155], v[164:167], v[88:103]\n\t"
    "s_add_u32 m0, %[m0b], 39936\n\t"
    "s_add_u32 s64, s64, s65\n\t"
    "buffer_load_dwordx4 v13, s[60:63], s64 offen lds\n\t"
    "v_exp_f32 v41, v41\n\t"
    "v_exp_f32 v42, v42\n\t"
    "v_mfma_f32_32x32x16_bf16 v[104:119], a[156:159], v[164:167], v[104:119]\n\t"
    "ds_read_b64_tr_b16 a[152:153], v20 offset:26624\n\t"
    "ds_read_b64_tr_b16 a[154:155], v20 offset:27648\n\t"
    "ds_read_b64_tr_b16 a[156:157], v21 offset:26624\n\t"
    "ds_read_b64_tr_b16 a[158:159], v21 offset:27648\n\t"
    "s_waitcnt lgkmcnt(8)\n\t"
    "v_mfma_f32_32x32x16_bf16 v[88:103], a[160:163], v[168:171], v[88:103]\n\t"
    "s_add_u32 m0, %[m0b], 40960\n\t"
    "s_add_u32 s64, s64, s65\n\t"
    "buffer_load_dwordx4 v13, s[60:63], s64 offen lds\n\t"
    "v_exp_f32 v43, v43\n\t"
    "v_exp_f32 v44, v44\n\t"
    "v_mfma_f32_32x32x16_bf16 v[104:119], a[164:167], v[168:171], v[104:119]\n\t"
    "ds_read_b64_tr_b16 a[160:161], v20 offset:28672\n\t"
    "ds_read_b64_tr_b16 a[162:163], v20 offset:29696\n\t"
    "ds_read_b64_tr_b16 a[164:165], v21 offset:28672\n\t"
    "ds_read_b64_tr_b16 a[166:167], v21 offset:29696\n\t"
    "v_mfma_f32_32x32x16_bf16 v[88:103], a[168:171], v[172:175], v[88:103]\n\t"
    "s_add_u32 m0, %[m0b], 41984\n\t"
    "s_add_u32 s64, s64, s65\n\t"
    "buffer_load_dwordx4 v13, s[60:63], s64 offen lds\n\t"
    "v_exp_f32 v45, v45\n\t"
    "v_exp_f32 v46, v46\n\t"
    "v_mfma_f32_32x32x16_bf16 v[104:119], a[172:175], v[172:175], v[104:119]\n\t"
    "ds_read_b64_tr_b16 a[168:169], v20 offset:30720\n\t"
    "ds_read_b64_tr_b16 a[170:171], v20 offset:31744\n\t"
    "ds_read_b64_tr_b16 a[172:173], v21 offset:30720\n\t"
    "ds_read_b64_tr_b16 a[174:175], v21 offset:31744\n\t"
    "s_waitcnt lgkmcnt(8)\n\t"
    "v_mfma_f32_32x32x16_bf16 v[88:103], a[144:147], v[176:179], v[88:103]\n\t"
    "s_add_u32 m0, %[m0b], 43008\n\t"
    "s_add_u32 s64, s64, s65\n\t"
    "buffer_load_dwordx4 v13, s[60:63], s64 offen lds\n\t"
    "v_exp_f32 v47, v47\n\t"
    "v_exp_f32 v48, v48\n\t"
    "v_mfma_f32_32x32x16_bf16 v[104:119], a[148:151], v[176:179], v[104:119]\n\t"
    "ds_read_b64_tr_b16 a[144:145], v20 offset:32768\n\t"
    "ds_read_b64_tr_b16 a[146:147], v20 offset:33792\n\t"
    "ds_read_b64_tr_b16 a[148:149], v21 offset:32768\n\t"
    "ds_read_b64_tr_b16 a[150:151], v21 offset:33792\n\t"
    "v_mfma_f32_32x32x16_bf16 v[88:103], a[152:155], v[180:183], v[88:103]\n\t"
    "s_add_u32 m0, %[m0b], 44032\n\t"
    "s_add_u32 s64, s64, s65\n\t"
    "buffer_load_dwordx4 v13, s[60:63], s64 offen lds\n\t"
    "v_exp_f32 v49, v49\n\t"
    "v_exp_f32 v50, v50\n\t"
    "v_mfma_f32_32x32x16_bf16 v[104:119], a[156:159], v[180:183], v[104:119]\n\t"
    "ds_read_b64_tr_b16 a[152:153], v20 offset:34816\n\t"
    "ds_read_b64_tr_b16 a[154:155], v20 offset:35840\n\t"
    "ds_read_b64_tr_b16 a[156:157], v21 offset:34816\n\t"
    "ds_read_b64_tr_b16 a[158:159], v21 offset:35840\n\t"
    "s_waitcnt lgkmcnt(8)\n\t"
    "v_mfma_f32_32x32x16_bf16 v[88:103], a[160:163], v[184:187], v[88:103]\n\t"
    "s_add_u32 m0, %[m0b], 45056\n\t"
    "s_add_u32 s64, s64, s65\n\t"
    "buffer_load_dwordx4 v13, s[60:63], s64 offen lds\n\t"
    "s_add_u32 s64, s64, s67\n\t"
    "v_exp_f32 v51, v51\n\t"
    "v_mfma_f32_32x32x16_bf16 v[104:119], a[164:167], v[184:187], v[104:119]\n\t"
    "ds_read_b64_tr_b16 a[160:161], v22 offset:0\n\t"
    "ds_read_b64_tr_b16 a[162:163], v22 offset:1024\n\t"
    "ds_read_b64_tr_b16 a[164:165], v23 offset:0\n\t"
    "ds_read_b64_tr_b16 a[166:167], v23 offset:1024\n\t"
    "v_mfma_f32_32x32x16_bf16 v[88:103], a[168:171], v[188:191], v[88:103]\n\t"
    "v_exp_f32 v52, v52\n\t"
    "v_exp_f32 v53, v53\n\t"
    "v_exp_f32 v54, v54\n\t"
    "v_exp_f32 v55, v55\n\t"
    "s_nop 0\n\t"
    "v_pk_add_f32 v[24:25], v[24:25], v[126:127] op_sel_hi:[1,0]\n\t"
    "v_pk_add_f32 v[26:27], v[26:27], v[126:127] op_sel_hi:[1,0]\n\t"
    "v_pk_add_f32 v[28:29], v[28:29], v[126:127] op_sel_hi:[1,0]\n\t"
    "v_pk_add_f32 v[30:31], v[30:31], v[126:127] op_sel_hi:[1,0]\n\t"
    "v_pk_add_f32 v[32:33], v[32:33], v[126:127] op_sel_hi:[1,0]\n\t"
    "v_pk_add_f32 v[34:35], v[34:35], v[126:127] op_sel_hi:[1,0]\n\t"
    "v_pk_add_f32 v[36:37], v[36:37], v[126:127] op_sel_hi:[1,0]\n\t"
    "v_pk_add_f32 v[38:39], v[38:39], v[126:127] op_sel_hi:[1,0]\n\t"
    "v_pk_add_f32 v[40:41], v[40:41], v[126:127] op_sel_hi:[1,0]\n\t"
    "v_pk_add_f32 v[42:43], v[42:43], v[126:127] op_sel_hi:[1,0]\n\t"
    "v_pk_add_f32 v[44:45], v[44:45], v[126:127] op_sel_hi:[1,0]\n\t"
    "v_pk_add_f32 v[46:47], v[46:47], v[126:127] op_sel_hi:[1,0]\n\t"
    "v_pk_add_f32 v[48:49], v[48:49], v[126:127] op_sel_hi:[1,0]\n\t"
    "v_pk_add_f32 v[50:51], v[50:51], v[126:127] op_sel_hi:[1,0]\n\t"
    "v_pk_add_f32 v[52:53], v[52:53], v[126:127] op_sel_hi:[1,0]\n\t"
    "v_pk_add_f32 v[54:55], v[54:55], v[126:127] op_sel_hi:[1,0]\n\t"
    "v_mfma_f32_32x32x16_bf16 v[104:119], a[172:175], v[188:191], v[104:119]\n\t"
    "ds_read_b64_tr_b16 a[168:169], v22 offset:2048\n\t"
    "ds_read_b64_tr_b16 a[170:171], v22 offset:3072\n\t"
    "ds_read_b64_tr_b16 a[172:173], v23 offset:2048\n\t"
    "ds_read_b64_tr_b16 a[174:175], v23 offset:3072\n\t"
    "s_waitcnt lgkmcnt(8)\n\t"
    "v_mfma_f32_32x32x16_bf16 v[88:103], a[144:147], v[192:195], v[88:103]\n\t"
    "v_rcp_f32 v24, v24\n\t"
    "v_rcp_f32 v25, v25\n\t"
    "v_rcp_f32 v26, v26\n\t"
    "v_rcp_f32 v27, v27\n\t"
    "v_mfma_f32_32x32x16_bf16 v[104:119], a[148:151], v[192:195], v[104:119]\n\t"
    "ds_read_b64_tr_b16 a[144:145], v22 offset:4096\n\t"
    "ds_read_b64_tr_b16 a[146:147], v22 offset:5120\n\t"
    "ds_read_b64_tr_b16 a[148:149], v23 offset:4096\n\t"
    "ds_read_b64_tr_b16 a[150:151], v23 offset:5120\n\t"
    "v_mfma_f32_32x32x16_bf16 v[88:103], a[152:155], v[196:199], v[88:103]\n\t"
    "v_rcp_f32 v28, v28\n\t"
    "v_rcp_f32 v29, v29\n\t"
    "v_rcp_f32 v30, v30\n\t"
    "v_rcp_f32 v31, v31\n\t"
    "v_mfma_f32_32x32x16_bf16 v[104:119], a[156:159], v[196:199], v[104:119]\n\t"
    "ds_read_b64_tr_b16 a[152:153], v22 offset:6144\n\t"
    "ds_read_b64_tr_b16 a[154:155], v22 offset:7168\n\t"
    "ds_read_b64_tr_b16 a[156:157], v23 offset:6144\n\t"
    "ds_read_b64_tr_b16 a[158:159], v23 offset:7168\n\t"
    "s_waitcnt lgkmcnt(8)\n\t"
    "v_mfma_f32_32x32x16_bf16 v[88:103], a[160:163], v[200:203], v[88:103]\n\t"
    "v_rcp_f32 v32, v32\n\t"
    "v_rcp_f32 v33, v33\n\t"
    "v_rcp_f32 v34, v34\n\t"
    "v_rcp_f32 v35, v35\n\t"
    "v_mfma_f32_32x32x16_bf16 v[104:119], a[164:167], v[200:203], v[104:119]\n\t"
    "ds_read_b64_tr_b16 a[160:161], v22 offset:8192\n\t"
    "ds_read_b64_tr_b16 a[162:163], v22 offset:9216\n\t"
    "ds_read_b64_tr_b16 a[164:165], v23 offset:8192\n\t"
    "ds_read_b64_tr_b16 a[166:167], v23 offset:9216\n\t"
    "v_mfma_f32_32x32x16_bf16 v[88:103], a[168:171], v[204:207], v[88:103]\n\t"
    "v_rcp_f32 v36, v36\n\t"
    "v_rcp_f32 v37, v37\n\t"
    "v_rcp_f32 v38, v38\n\t"
    "v_rcp_f32 v39, v39\n\t"
    "v_mfma_f32_32x32x16_bf16 v[104:119], a[172:175], v[204:207], v[104:119]\n\t"
    "ds_read_b64_tr_b16 a[168:169], v22 offset:10240\n\t"
    "ds_read_b64_tr_b16 a[170:171], v22 offset:11264\n\t"
    "ds_read_b64_tr_b16 a[172:173], v23 offset:10240\n\t"
    "ds_read_b64_tr_b16 a[174:175], v23 offset:11264\n\t"
    "s_waitcnt lgkmcnt(8)\n\t"
    "v_mfma_f32_32x32x16_bf16 v[88:103], a[144:147], v[208:211], v[88:103]\n\t"
    "v_rcp_f32 v40, v40\n\t"
    "v_rcp_f32 v41, v41\n\t"
    "v_rcp_f32 v42, v42\n\t"
    "v_rcp_f32 v43, v43\n\t"
    "v_mfma_f32_32x32x16_bf16 v[104:119], a[148:151], v[208:211], v[104:119]\n\t"
    "ds_read_b64_tr_b16 a[144:145], v22 offset:12288\n\t"
    "ds_read_b64_tr_b16 a[146:147], v22 offset:13312\n\t"
    "ds_read_b64_tr_b16 a[148:149], v23 offset:12288\n\t"
    "ds_read_b64_tr_b16 a[150:151], v23 offset:13312\n\t"
    "v_mfma_f32_32x32x16_bf16 v[88:103], a[152:155], v[212:215], v[88:103]\n\t"
    "v_rcp_f32 v44, v44\n\t"
    "v_rcp_f32 v45, v45\n\t"
    "v_rcp_f32 v46, v46\n\t"
    "v_rcp_f32 v47, v47\n\t"
    "v_mfma_f32_32x32x16_bf16 v[104:119], a[156:159], v[212:215], v[104:119]\n\t"
    "ds_read_b64_tr_b16 a[152:153], v22 offset:14336\n\t"
    "ds_read_b64_tr_b16 a[154:155], v22 offset:15360\n\t"
    "ds_read_b64_tr_b16 a[156:157], v23 offset:14336\n\t"
    "ds_read_b64_tr_b16 a[158:159], v23 offset:15360\n\t"
    "s_waitcnt lgkmcnt(8)\n\t"
    "v_mfma_f32_32x32x16_bf16 v[88:103], a[160:163], v[216:219], v[88:103]\n\t"
    "v_rcp_f32 v48, v48\n\t"
    "v_rcp_f32 v49, v49\n\t"
    "v_rcp_f32 v50, v50\n\t"
    "v_rcp_f32 v51, v51\n\t"
    "v_mfma_f32_32x32x16_bf16 v[104:119], a[164:167], v[216:219], v[104:119]\n\t"
    "ds_read_b64_tr_b16 a[160:161], v22 offset:16384\n\t"
    "ds_read_b64_tr_b16 a[162:163], v22 offset:17408\n\t"
    "ds_read_b64_tr_b16 a[164:165], v23 offset:16384\n\t"
    "ds_read_b64_tr_b16 a[166:167], v23 offset:17408\n\t"
    "v_mfma_f32_32x32x16_bf16 v[88:103], a[168:171], v[220:223], v[88:103]\n\t"
    "v_rcp_f32 v52, v52\n\t"
    "v_rcp_f32 v53, v53\n\t"
    "v_rcp_f32 v54, v54\n\t"
    "v_rcp_f32 v55, v55\n\t"
    "s_nop 0\n\t"
    "v_pk_mul_f32 v[56:57], v[56:57], v[24:25]\n\t"
    "v_pk_mul_f32 v[58:59], v[58:59], v[26:27]\n\t"
    "v_pk_mul_f32 v[60:61], v[60:61], v[28:29]\n\t"
    "v_pk_mul_f32 v[62:63], v[62:63], v[30:31]\n\t"
    "v_pk_mul_f32 v[64:65], v[64:65], v[32:33]\n\t"
    "v_pk_mul_f32 v[66:67], v[66:67], v[34:35]\n\t"
    "v_pk_mul_f32 v[68:69], v[68:69], v[36:37]\n\t"
    "v_pk_mul_f32 v[70:71], v[70:71], v[38:39]\n\t"
    "v_pk_mul_f32 v[72:73], v[72:73], v[40:41]\n\t"
    "v_pk_mul_f32 v[74:75], v[74:75], v[42:43]\n\t"
    "v_pk_mul_f32 v[76:77], v[76:77], v[44:45]\n\t"
    "v_pk_mul_f32 v[78:79], v[78:79], v[46:47]\n\t"
    "v_pk_mul_f32 v[80:81], v[80:81], v[48:49]\n\t"
    "v_pk_mul_f32 v[82:83], v[82:83], v[50:51]\n\t"
    "v_pk_mul_f32 v[84:85], v[84:85], v[52:53]\n\t"
    "v_pk_mul_f32 v[86:87], v[86:87], v[54:55]\n\t"
    "v_cvt_pk_bf16_f32 v56, v56, v57\n\t"
    "v_cvt_pk_bf16_f32 v57, v58, v59\n\t"
    "v_cvt_pk_bf16_f32 v58, v60, v61\n\t"
    "v_cvt_pk_bf16_f32 v59, v62, v63\n\t"
    "v_cvt_pk_bf16_f32 v60, v64, v65\n\t"
    "v_cvt_pk_bf16_f32 v61, v66, v67\n\t"
    "v_cvt_pk_bf16_f32 v62, v68, v69\n\t"
    "v_cvt_pk_bf16_f32 v63, v70, v71\n\t"
    "v_cvt_pk_bf16_f32 v64, v72, v73\n\t"
    "v_cvt_pk_bf16_f32 v65, v74, v75\n\t"
    "v_cvt_pk_bf16_f32 v66, v76, v77\n\t"
    "v_cvt_pk_bf16_f32 v67, v78, v79\n\t"
    "v_cvt_pk_bf16_f32 v68, v80, v81\n\t"
    "v_cvt_pk_bf16_f32 v69, v82, v83\n\t"
    "v_cvt_pk_bf16_f32 v70, v84, v85\n\t"
    "v_cvt_pk_bf16_f32 v71, v86, v87\n\t"
    "v_accvgpr_write_b32 a0, v56\n\t"
    "v_accvgpr_write_b32 a1, v57\n\t"
    "v_accvgpr_write_b32 a2, v58\n\t"
    "v_accvgpr_write_b32 a3, v59\n\t"
    "v_accvgpr_write_b32 a4, v60\n\t"
    "v_accvgpr_write_b32 a5, v61\n\t"
    "v_accvgpr_write_b32 a6, v62\n\t"
    "v_accvgpr_write_b32 a7, v63\n\t"
    "v_accvgpr_write_b32 a8, v64\n\t"
    "v_accvgpr_write_b32 a9, v65\n\t"
    "v_accvgpr_write_b32 a10, v66\n\t"
    "v_accvgpr_write_b32 a11, v67\n\t"
    "v_accvgpr_write_b32 a12, v68\n\t"
    "v_accvgpr_write_b32 a13, v69\n\t"
    "v_accvgpr_write_b32 a14, v70\n\t"
    "v_accvgpr_write_b32 a15, v71\n\t"
    "v_mfma_f32_32x32x16_bf16 v[104:119], a[172:175], v[220:223], v[104:119]\n\t"
    "ds_read_b64_tr_b16 a[168:169], v22 offset:18432\n\t"
    "ds_read_b64_tr_b16 a[170:171], v22 offset:19456\n\t"
    "ds_read_b64_tr_b16 a[172:173], v23 offset:18432\n\t"
    "ds_read_b64_tr_b16 a[174:175], v23 offset:19456\n\t"
    "s_waitcnt lgkmcnt(8)\n\t"
    "v_mfma_f32_32x32x16_bf16 v[88:103], a[144:147], v[224:227], v[88:103]\n\t"
    "v_mfma_f32_32x32x16_bf16 v[104:119], a[148:151], v[224:227], v[104:119]\n\t"
    "ds_read_b64_tr_b16 a[144:145], v22 offset:20480\n\t"
    "ds_read_b64_tr_b16 a[146:147], v22 offset:21504\n\t"
    "ds_read_b64_tr_b16 a[148:149], v23 offset:20480\n\t"
    "ds_read_b64_tr_b16 a[150:151], v23 offset:21504\n\t"
    "v_mfma_f32_32x32x16_bf16 v[88:103], a[152:155], v[228:231], v[88:103]\n\t"
    "v_mfma_f32_32x32x16_bf16 v[104:119], a[156:159], v[228:231], v[104:119]\n\t"
    "ds_read_b64_tr_b16 a[152:153], v22 offset:22528\n\t"
    "ds_read_b64_tr_b16 a[154:155], v22 offset:23552\n\t"
    "ds_read_b64_tr_b16 a[156:157], v23 offset:22528\n\t"
    "ds_read_b64_tr_b16 a[158:159], v23 offset:23552\n\t"
    "s_waitcnt lgkmcnt(8)\n\t"
    "v_mfma_f32_32x32x16_bf16 v[88:103], a[160:163], v[232:235], v[88:103]\n\t"
    "v_mfma_f32_32x32x16_bf16 v[104:119], a[164:167], v[232:235], v[104:119]\n\t"
    "ds_read_b64_tr_b16 a[160:161], v22 offset:24576\n\t"
    "ds_read_b64_tr_b16 a[162:163], v22 offset:25600\n\t"
    "ds_read_b64_tr_b16 a[164:165], v23 offset:24576\n\t"
    "ds_read_b64_tr_b16 a[166:167], v23 offset:25600\n\t"
    "v_mfma_f32_32x32x16_bf16 v[88:103], a[168:171], v[236:239], v[88:103]\n\t"
    "v_mfma_f32_32x32x16_bf16 v[104:119], a[172:175], v[236:239], v[104:119]\n\t"
    "ds_read_b64_tr_b16 a[168:169], v22 offset:26624\n\t"
    "ds_read_b64_tr_b16 a[170:171], v22 offset:27648\n\t"
    "ds_read_b64_tr_b16 a[172:173], v23 offset:26624\n\t"
    "ds_read_b64_tr_b16 a[174:175], v23 offset:27648\n\t"
    "s_waitcnt lgkmcnt(8)\n\t"
    "v_mfma_f32_32x32x16_bf16 v[88:103], a[144:147], v[240:243], v[88:103]\n\t"
    "v_mfma_f32_32x32x16_bf16 v[104:119], a[148:151], v[240:243], v[104:119]\n\t"
    "ds_read_b64_tr_b16 a[144:145], v22 offset:28672\n\t"
    "ds_read_b64_tr_b16 a[146:147], v22 offset:29696\n\t"
    "ds_read_b64_tr_b16 a[148:149], v23 offset:28672\n\t"
    "ds_read_b64_tr_b16 a[150:151], v23 offset:29696\n\t"
    "v_mfma_f32_32x32x16_bf16 v[88:103], a[152:155], v[244:247], v[88:103]\n\t"
    "v_mfma_f32_32x32x16_bf16 v[104:119], a[156:159], v[244:247], v[104:119]\n\t"
    "ds_read_b64_tr_b16 a[152:153], v22 offset:30720\n\t"
    "ds_read_b64_tr_b16 a[154:155], v22 offset:31744\n\t"
    "ds_read_b64_tr_b16 a[156:157], v23 offset:30720\n\t"
    "ds_read_b64_tr_b16 a[158:159], v23 offset:31744\n\t"
    "s_waitcnt lgkmcnt(8)\n\t"
    "v_mfma_f32_32x32x16_bf16 v[88:103], a[160:163], v[248:251], v[88:103]\n\t"
    "v_mfma_f32_32x32x16_bf16 v[104:119], a[164:167], v[248:251], v[104:119]\n\t"
    "ds_read_b64_tr_b16 a[160:161], v22 offset:32768\n\t"
    "ds_read_b64_tr_b16 a[162:163], v22 offset:33792\n\t"
    "ds_read_b64_tr_b16 a[164:165], v23 offset:32768\n\t"
    "ds_read_b64_tr_b16 a[166:167], v23 offset:33792\n\t"
    "v_mfma_f32_32x32x16_bf16 v[88:103], a[168:171], v[252:255], v[88:103]\n\t"
    "v_mfma_f32_32x32x16_bf16 v[104:119], a[172:175], v[252:255], v[104:119]\n\t"
    "ds_read_b64_tr_b16 a[168:169], v22 offset:34816\n\t"
    "ds_read_b64_tr_b16 a[170:171], v22 offset:35840\n\t"
    "ds_read_b64_tr_b16 a[172:173], v23 offset:34816\n\t"
    "ds_read_b64_tr_b16 a[174:175], v23 offset:35840\n\t"
    "s_waitcnt lgkmcnt(8)\n\t"
    "v_mfma_f32_32x32x16_bf16 v[88:103], a[144:147], a[0:3], v[88:103]\n\t"
    "s_waitcnt vmcnt(0) lgkmcnt(0)\n\t"
    "s_barrier\n\t"
    "v_mfma_f32_32x32x16_bf16 v[104:119], a[148:151], a[0:3], v[104:119]\n\t"
    "ds_read_b64_tr_b16 a[144:145], v16 offset:0\n\t"
    "ds_read_b64_tr_b16 a[146:147], v16 offset:1024\n\t"
    "ds_read_b64_tr_b16 a[148:149], v17 offset:0\n\t"
    "ds_read_b64_tr_b16 a[150:151], v17 offset:1024\n\t"
    "v_mfma_f32_32x32x16_bf16 v[88:103], a[152:155], a[4:7], v[88:103]\n\t"
    "s_add_u32 m0, %[m0b], 73728\n\t"
    "s_add_u32 s64, s64, s65\n\t"
    "buffer_load_dwordx4 v13, s[60:63], s64 offen lds\n\t"
    "v_mfma_f32_32x32x16_bf16 v[104:119], a[156:159], a[4:7], v[104:119]\n\t"
    "ds_read_b64_tr_b16 a[152:153], v16 offset:2048\n\t"
    "ds_read_b64_tr_b16 a[154:155], v16 offset:3072\n\t"
    "ds_read_b64_tr_b16 a[156:157], v17 offset:2048\n\t"
    "ds_read_b64_tr_b16 a[158:159], v17 offset:3072\n\t"
    "s_waitcnt lgkmcnt(8)\n\t"
    "v_mfma_f32_32x32x16_bf16 v[88:103], a[160:163], a[8:11], v[88:103]\n\t"
    "s_add_u32 m0, %[m0b], 74752\n\t"
    "s_add_u32 s64, s64, s65\n\t"
    "buffer_load_dwordx4 v13, s[60:63], s64 offen lds\n\t"
    "v_mfma_f32_32x32x16_bf16 v[104:119], a[164:167], a[8:11], v[104:119]\n\t"
    "ds_read_b64_tr_b16 a[160:161], v16 offset:4096\n\t"
    "ds_read_b64_tr_b16 a[162:163], v16 offset:5120\n\t"
    "ds_read_b64_tr_b16 a[164:165], v17 offset:4096\n\t"
    "ds_read_b64_tr_b16 a[166:167], v17 offset:5120\n\t"
    "v_mfma_f32_32x32x16_bf16 v[88:103], a[168:171], a[12:15], v[88:103]\n\t"
    "s_add_u32 m0, %[m0b], 75776\n\t"
    "s_add_u32 s64, s64, s65\n\t"
    "buffer_load_dwordx4 v13, s[60:63], s64 offen lds\n\t"
    "v_mfma_f32_32x32x16_bf16 v[104:119], a[172:175], a[12:15], v[104:119]\n\t"
    "s_mov_b32 s68, 4\n\t"
    ".Lml2_%=:\n"
    "ds_read_b64_tr_b16 a[168:169], v16 offset:6144\n\t"
    "ds_read_b64_tr_b16 a[170:171], v16 offset:7168\n\t"
    "ds_read_b64_tr_b16 a[172:173], v17 offset:6144\n\t"
    "ds_read_b64_tr_b16 a[174:175], v17 offset:7168\n\t"
    "s_waitcnt lgkmcnt(8)\n\t"
    "v_mfma_f32_32x32x16_bf16 v[56:71], a[144:147], v[128:131], 0\n\t"
    "s_add_u32 m0, %[m0b], 76800\n\t"
    "s_add_u32 s64, s64, s65\n\t"
    "buffer_load_dwordx4 v13, s[60:63], s64 offen lds\n\t"
    "v_mfma_f32_32x32x16_bf16 v[72:87], a[148:151], v[128:131], 0\n\t"
    "ds_read_b64_tr_b16 a[144:145], v16 offset:8192\n\t"
    "ds_read_b64_tr_b16 a[146:147], v16 offset:9216\n\t"
    "ds_read_b64_tr_b16 a[148:149], v17 offset:8192\n\t"
    "ds_read_b64_tr_b16 a[150:151], v17 offset:9216\n\t"
    "v_mfma_f32_32x32x16_bf16 v[56:71], a[152:155], v[132:135], v[56:71]\n\t"
    "s_add_u32 m0, %[m0b], 77824\n\t"
    "s_add_u32 s64, s64, s65\n\t"
    "buffer_load_dwordx4 v13, s[60:63], s64 offen lds\n\t"
    "v_mfma_f32_32x32x16_bf16 v[72:87], a[156:159], v[132:135], v[72:87]\n\t"
    "ds_read_b64_tr_b16 a[152:153], v16 offset:10240\n\t"
    "ds_read_b64_tr_b16 a[154:155], v16 offset:11264\n\t"
    "ds_read_b64_tr_b16 a[156:157], v17 offset:10240\n\t"
    "ds_read_b64_tr_b16 a[158:159], v17 offset:11264\n\t"
    "s_waitcnt lgkmcnt(8)\n\t"
    "v_mfma_f32_32x32x16_bf16 v[56:71], a[160:163], v[136:139], v[56:71]\n\t"
    "s_add_u32 m0, %[m0b], 78848\n\t"
    "s_add_u32 s64, s64, s65\n\t"
    "buffer_load_dwordx4 v13, s[60:63], s64 offen lds\n\t"
    "v_mfma_f32_32x32x16_bf16 v[72:87], a[164:167], v[136:139], v[72:87]\n\t"
    "ds_read_b64_tr_b16 a[160:161], v16 offset:12288\n\t"
    "ds_read_b64_tr_b16 a[162:163], v16 offset:13312\n\t"
    "ds_read_b64_tr_b16 a[164:165], v17 offset:12288\n\t"
    "ds_read_b64_tr_b16 a[166:167], v17 offset:13312\n\t"
    "v_mfma_f32_32x32x16_bf16 v[56:71], a[168:171], v[140:143], v[56:71]\n\t"
    "s_add_u32 m0, %[m0b], 79872\n\t"
    "s_add_u32 s64, s64, s65\n\t"
    "buffer_load_dwordx4 v13, s[60:63], s64 offen lds\n\t"
    "v_mfma_f32_32x32x16_bf16 v[72:87], a[172:175], v[140:143], v[72:87]\n\t"
    "ds_read_b64_tr_b16 a[168:169], v16 offset:14336\n\t"
    "ds_read_b64_tr_b16 a[170:171], v16 offset:15360\n\t"
    "ds_read_b64_tr_b16 a[172:173], v17 offset:14336\n\t"
    "ds_read_b64_tr_b16 a[174:175], v17 offset:15360\n\t"
    "s_waitcnt lgkmcnt(8)\n\t"
    "v_mfma_f32_32x32x16_bf16 v[56:71], a[144:147], v[144:147], v[56:71]\n\t"
    "s_add_u32 m0, %[m0b], 80896\n\t"
    "s_add_u32 s64, s64, s65\n\t"
    "buffer_load_dwordx4 v13, s[60:63], s64 offen lds\n\t"
    "v_mfma_f32_32x32x16_bf16 v[72:87], a[148:151], v[144:147], v[72:87]\n\t"
    "ds_read_b64_tr_b16 a[144:145], v16 offset:16384\n\t"
    "ds_read_b64_tr_b16 a[146:147], v16 offset:17408\n\t"
    "ds_read_b64_tr_b16 a[148:149], v17 offset:16384\n\t"
    "ds_read_b64_tr_b16 a[150:151], v17 offset:17408\n\t"
    "v_mfma_f32_32x32x16_bf16 v[56:71], a[152:155], v[148:151], v[56:71]\n\t"
    "s_add_u32 m0, %[m0b], 81920\n\t"
    "s_add_u32 s64, s64, s65\n\t"
    "buffer_load_dwordx4 v13, s[60:63], s64 offen lds\n\t"
    "s_add_u32 s64, s64, s66\n\t"
    "v_mfma_f32_32x32x16_bf16 v[72:87], a[156:159], v[148:151], v[72:87]\n\t"
    "ds_read_b64_tr_b16 a[152:153], v16 offset:18432\n\t"
    "ds_read_b64_tr_b16 a[154:155], v16 offset:19456\n\t"
    "ds_read_b64_tr_b16 a[156:157], v17 offset:18432\n\t"
    "ds_read_b64_tr_b16 a[158:159], v17 offset:19456\n\t"
    "s_waitcnt lgkmcnt(8)\n\t"
    "v_mfma_f32_32x32x16_bf16 v[56:71], a[160:163], v[152:155], v[56:71]\n\t"
    "s_add_u32 m0, %[m0b], 110592\n\t"
    "s_add_u32 s64, s64, s65\n\t"
    "buffer_load_dwordx4 v13, s[60:63], s64 offen lds\n\t"
    "v_mfma_f32_32x32x16_bf16 v[72:87], a[164:167], v[152:155], v[72:87]\n\t"
    "ds_read_b64_tr_b16 a[160:161], v16 offset:20480\n\t"
    "ds_read_b64_tr_b16 a[162:163], v16 offset:21504\n\t"
    "ds_read_b64_tr_b16 a[164:165], v17 offset:20480\n\t"
    "ds_read_b64_tr_b16 a[166:167], v17 offset:21504\n\t"
    "v_mfma_f32_32x32x16_bf16 v[56:71], a[168:171], v[156:159], v[56:71]\n\t"
    "s_add_u32 m0, %[m0b], 111616\n\t"
    "s_add_u32 s64, s64, s65\n\t"
    "buffer_load_dwordx4 v13, s[60:63], s64 offen lds\n\t"
    "v_mfma_f32_32x32x16_bf16 v[72:87], a[172:175], v[156:159], v[72:87]\n\t"
    "ds_read_b64_tr_b16 a[168:169], v16 offset:22528\n\t"
    "ds_read_b64_tr_b16 a[170:171], v16 offset:23552\n\t"
    "ds_read_b64_tr_b16 a[172:173], v17 offset:22528\n\t"
    "ds_read_b64_tr_b16 a[174:175], v17 offset:23552\n\t"
    "s_waitcnt lgkmcnt(8)\n\t"
    "v_mfma_f32_32x32x16_bf16 v[56:71], a[144:147], v[160:163], v[56:71]\n\t"
    "s_add_u32 m0, %[m0b], 112640\n\t"
    "s_add_u32 s64, s64, s65\n\t"
    "buffer_load_dwordx4 v13, s[60:63], s64 offen lds\n\t"
    "v_mfma_f32_32x32x16_bf16 v[72:87], a[148:151], v[160:163], v[72:87]\n\t"
    "ds_read_b64_tr_b16 a[144:145], v16 offset:24576\n\t"
    "ds_read_b64_tr_b16 a[146:147], v16 offset:25600\n\t"
    "ds_read_b64_tr_b16 a[148:149], v17 offset:24576\n\t"
    "ds_read_b64_tr_b16 a[150:151], v17 offset:25600\n\t"
    "v_mfma_f32_32x32x16_bf16 v[56:71], a[152:155], v[164:167], v[56:71]\n\t"
    "s_add_u32 m0, %[m0b], 113664\n\t"
    "s_add_u32 s64, s64, s65\n\t"
    "buffer_load_dwordx4 v13, s[60:63], s64 offen lds\n\t"
    "v_mfma_f32_32x32x16_bf16 v[72:87], a[156:159], v[164:167], v[72:87]\n\t"
    "ds_read_b64_tr_b16 a[152:153], v16 offset:26624\n\t"
    "ds_read_b64_tr_b16 a[154:155], v16 offset:27648\n\t"
    "ds_read_b64_tr_b16 a[156:157], v17 offset:26624\n\t"
    "ds_read_b64_tr_b16 a[158:159], v17 offset:27648\n\t"
    "s_waitcnt lgkmcnt(8)\n\t"
    "v_mfma_f32_32x32x16_bf16 v[56:71], a[160:163], v[168:171], v[56:71]\n\t"
    "s_add_u32 m0, %[m0b], 114688\n\t"
    "s_add_u32 s64, s64, s65\n\t"
    "buffer_load_dwordx4 v13, s[60:63], s64 offen lds\n\t"
    "v_mfma_f32_32x32x16_bf16 v[72:87], a[164:167], v[168:171], v[72:87]\n\t"
    "ds_read_b64_tr_b16 a[160:161], v16 offset:28672\n\t"
    "ds_read_b64_tr_b16 a[162:163], v16 offset:29696\n\t"
    "ds_read_b64_tr_b16 a[164:165], v17 offset:28672\n\t"
    "ds_read_b64_tr_b16 a[166:167], v17 offset:29696\n\t"
    "v_mfma_f32_32x32x16_bf16 v[56:71], a[168:171], v[172:175], v[56:71]\n\t"
    "s_add_u32 m0, %[m0b], 115712\n\t"
    "s_add_u32 s64, s64, s65\n\t"
    "buffer_load_dwordx4 v13, s[60:63], s64 offen lds\n\t"
    "v_mfma_f32_32x32x16_bf16 v[72:87], a[172:175], v[172:175], v[72:87]\n\t"
    "ds_read_b64_tr_b16 a[168:169], v16 offset:30720\n\t"
    "ds_read_b64_tr_b16 a[170:171], v16 offset:31744\n\t"
    "ds_read_b64_tr_b16 a[172:173], v17 offset:30720\n\t"
    "ds_read_b64_tr_b16 a[174:175], v17 offset:31744\n\t"
    "s_waitcnt lgkmcnt(8)\n\t"
    "v_mfma_f32_32x32x16_bf16 v[56:71], a[144:147], v[176:179], v[56:71]\n\t"
    "s_add_u32 m0, %[m0b], 116736\n\t"
    "s_add_u32 s64, s64, s65\n\t"
    "buffer_load_dwordx4 v13, s[60:63], s64 offen lds\n\t"
    "v_mfma_f32_32x32x16_bf16 v[72:87], a[148:151], v[176:179], v[72:87]\n\t"
    "ds_read_b64_tr_b16 a[144:145], v16 offset:32768\n\t"
    "ds_read_b64_tr_b16 a[146:147], v16 offset:33792\n\t"
    "ds_read_b64_tr_b16 a[148:149], v17 offset:32768\n\t"
    "ds_read_b64_tr_b16 a[150:151], v17 offset:33792\n\t"
    "v_mfma_f32_32x32x16_bf16 v[56:71], a[152:155], v[180:183], v[56:71]\n\t"
    "s_add_u32 m0, %[m0b], 117760\n\t"
    "s_add_u32 s64, s64, s65\n\t"
    "buffer_load_dwordx4 v13, s[60:63], s64 offen lds\n\t"
    "v_mfma_f32_32x32x16_bf16 v[72:87], a[156:159], v[180:183], v[72:87]\n\t"
    "ds_read_b64_tr_b16 a[152:153], v16 offset:34816\n\t"
    "ds_read_b64_tr_b16 a[154:155], v16 offset:35840\n\t"
    "ds_read_b64_tr_b16 a[156:157], v17 offset:34816\n\t"
    "ds_read_b64_tr_b16 a[158:159], v17 offset:35840\n\t"
    "s_waitcnt lgkmcnt(8)\n\t"
    "v_mfma_f32_32x32x16_bf16 v[56:71], a[160:163], v[184:187], v[56:71]\n\t"
    "s_add_u32 m0, %[m0b], 118784\n\t"
    "s_add_u32 s64, s64, s65\n\t"
    "buffer_load_dwordx4 v13, s[60:63], s64 offen lds\n\t"
    "s_add_u32 s64, s64, s67\n\t"
    "v_mfma_f32_32x32x16_bf16 v[72:87], a[164:167], v[184:187], v[72:87]\n\t"
    "ds_read_b64_tr_b16 a[160:161], v18 offset:0\n\t"
    "ds_read_b64_tr_b16 a[162:163], v18 offset:1024\n\t"
    "ds_read_b64_tr_b16 a[164:165], v19 offset:0\n\t"
    "ds_read_b64_tr_b16 a[166:167], v19 offset:1024\n\t"
    "v_mfma_f32_32x32x16_bf16 v[56:71], a[168:171], v[188:191], v[56:71]\n\t"
    "v_mfma_f32_32x32x16_bf16 v[72:87], a[172:175], v[188:191], v[72:87]\n\t"
    "ds_read_b64_tr_b16 a[168:169], v18 offset:2048\n\t"
    "ds_read_b64_tr_b16 a[170:171], v18 offset:3072\n\t"
    "ds_read_b64_tr_b16 a[172:173], v19 offset:2048\n\t"
    "ds_read_b64_tr_b16 a[174:175], v19 offset:3072\n\t"
    "s_waitcnt lgkmcnt(8)\n\t"
    "v_mfma_f32_32x32x16_bf16 v[56:71], a[144:147], v[192:195], v[56:71]\n\t"
    "v_cvt_pk_bf16_f32 v88, v88, v89\n\t"
    "v_cvt_pk_bf16_f32 v89, v90, v91\n\t"
    "v_cvt_pk_bf16_f32 v90, v92, v93\n\t"
    "v_cvt_pk_bf16_f32 v91, v94, v95\n\t"
    "buffer_store_dwordx4 v[88:91], %[pd], %[wsr], s70 offen offset:0 sc1\n\t"
    "v_mfma_f32_32x32x16_bf16 v[72:87], a[148:151], v[192:195], v[72:87]\n\t"
    "ds_read_b64_tr_b16 a[144:145], v18 offset:4096\n\t"
    "ds_read_b64_tr_b16 a[146:147], v18 offset:5120\n\t"
    "ds_read_b64_tr_b16 a[148:149], v19 offset:4096\n\t"
    "ds_read_b64_tr_b16 a[150:151], v19 offset:5120\n\t"
    "v_mfma_f32_32x32x16_bf16 v[56:71], a[152:155], v[196:199], v[56:71]\n\t"
    "v_cvt_pk_bf16_f32 v96, v96, v97\n\t"
    "v_cvt_pk_bf16_f32 v97, v98, v99\n\t"
    "v_cvt_pk_bf16_f32 v98, v100, v101\n\t"
    "v_cvt_pk_bf16_f32 v99, v102, v103\n\t"
    "buffer_store_dwordx4 v[96:99], %[pd], %[wsr], s70 offen offset:1024 sc1\n\t"
    "v_mfma_f32_32x32x16_bf16 v[72:87], a[156:159], v[196:199], v[72:87]\n\t"
    "ds_read_b64_tr_b16 a[152:153], v18 offset:6144\n\t"
    "ds_read_b64_tr_b16 a[154:155], v18 offset:7168\n\t"
    "ds_read_b64_tr_b16 a[156:157], v19 offset:6144\n\t"
    "ds_read_b64_tr_b16 a[158:159], v19 offset:7168\n\t"
    "s_waitcnt lgkmcnt(8)\n\t"
    "v_mfma_f32_32x32x16_bf16 v[56:71], a[160:163], v[200:203], v[56:71]\n\t"
    "v_cvt_pk_bf16_f32 v104, v104, v105\n\t"
    "v_cvt_pk_bf16_f32 v105, v106, v107\n\t"
    "v_cvt_pk_bf16_f32 v106, v108, v109\n\t"
    "v_cvt_pk_bf16_f32 v107, v110, v111\n\t"
    "buffer_store_dwordx4 v[104:107], %[pd], %[wsr], s70 offen offset:2048 sc1\n\t"
    "v_mfma_f32_32x32x16_bf16 v[72:87], a[164:167], v[200:203], v[72:87]\n\t"
    "ds_read_b64_tr_b16 a[160:161], v18 offset:8192\n\t"
    "ds_read_b64_tr_b16 a[162:163], v18 offset:9216\n\t"
    "ds_read_b64_tr_b16 a[164:165], v19 offset:8192\n\t"
    "ds_read_b64_tr_b16 a[166:167], v19 offset:9216\n\t"
    "v_mfma_f32_32x32x16_bf16 v[56:71], a[168:171], v[204:207], v[56:71]\n\t"
    "v_cvt_pk_bf16_f32 v112, v112, v113\n\t"
    "v_cvt_pk_bf16_f32 v113, v114, v115\n\t"
    "v_cvt_pk_bf16_f32 v114, v116, v117\n\t"
    "v_cvt_pk_bf16_f32 v115, v118, v119\n\t"
    "buffer_store_dwordx4 v[112:115], %[pd], %[wsr], s70 offen offset:3072 sc1\n\t"
    "v_mfma_f32_32x32x16_bf16 v[72:87], a[172:175], v[204:207], v[72:87]\n\t"
    "ds_read_b64_tr_b16 a[168:169], v18 offset:10240\n\t"
    "ds_read_b64_tr_b16 a[170:171], v18 offset:11264\n\t"
    "ds_read_b64_tr_b16 a[172:173], v19 offset:10240\n\t"
    "ds_read_b64_tr_b16 a[174:175], v19 offset:11264\n\t"
    "s_waitcnt lgkmcnt(8)\n\t"
    "v_mfma_f32_32x32x16_bf16 v[56:71], a[144:147], v[208:211], v[56:71]\n\t"
    "s_add_u32 s70, s70, 0x1000\n\t"
    "s_cmp_eq_u32 s70, 0x9000\n\t"
    "s_cselect_b32 s70, 0, s70\n\t"
    "v_mfma_f32_32x32x16_bf16 v[72:87], a[148:151], v[208:211], v[72:87]\n\t"
    "ds_read_b64_tr_b16 a[144:145], v18 offset:12288\n\t"
    "ds_read_b64_tr_b16 a[146:147], v18 offset:13312\n\t"
    "ds_read_b64_tr_b16 a[148:149], v19 offset:12288\n\t"
    "ds_read_b64_tr_b16 a[150:151], v19 offset:13312\n\t"
    "v_mfma_f32_32x32x16_bf16 v[56:71], a[152:155], v[212:215], v[56:71]\n\t"
    "v_mfma_f32_32x32x16_bf16 v[72:87], a[156:159], v[212:215], v[72:87]\n\t"
    "ds_read_b64_tr_b16 a[152:153], v18 offset:14336\n\t"
    "ds_read_b64_tr_b16 a[154:155], v18 offset:15360\n\t"
    "ds_read_b64_tr_b16 a[156:157], v19 offset:14336\n\t"
    "ds_read_b64_tr_b16 a[158:159], v19 offset:15360\n\t"
    "s_waitcnt lgkmcnt(8)\n\t"
    "v_mfma_f32_32x32x16_bf16 v[56:71], a[160:163], v[216:219], v[56:71]\n\t"
    "v_mfma_f32_32x32x16_bf16 v[72:87], a[164:167], v[216:219], v[72:87]\n\t"
    "ds_read_b64_tr_b16 a[160:161], v18 offset:16384\n\t"
    "ds_read_b64_tr_b16 a[162:163], v18 offset:17408\n\t"
    "ds_read_b64_tr_b16 a[164:165], v19 offset:16384\n\t"
    "ds_read_b64_tr_b16 a[166:167], v19 offset:17408\n\t"
    "v_mfma_f32_32x32x16_bf16 v[56:71], a[168:171], v[220:223], v[56:71]\n\t"
    "v_mfma_f32_32x32x16_bf16 v[72:87], a[172:175], v[220:223], v[72:87]\n\t"
    "ds_read_b64_tr_b16 a[168:169], v18 offset:18432\n\t"
    "ds_read_b64_tr_b16 a[170:171], v18 offset:19456\n\t"
    "ds_read_b64_tr_b16 a[172:173], v19 offset:18432\n\t"
    "ds_read_b64_tr_b16 a[174:175], v19 offset:19456\n\t"
    "s_waitcnt lgkmcnt(8)\n\t"
    "v_mfma_f32_32x32x16_bf16 v[56:71], a[144:147], v[224:227], v[56:71]\n\t"
    "v_mfma_f32_32x32x16_bf16 v[72:87], a[148:151], v[224:227], v[72:87]\n\t"
    "ds_read_b64_tr_b16 a[144:145], v18 offset:20480\n\t"
    "ds_read_b64_tr_b16 a[146:147], v18 offset:21504\n\t"
    "ds_read_b64_tr_b16 a[148:149], v19 offset:20480\n\t"
    "ds_read_b64_tr_b16 a[150:151], v19 offset:21504\n\t"
    "v_mfma_f32_32x32x16_bf16 v[56:71], a[152:155], v[228:231], v[56:71]\n\t"
    "v_mfma_f32_32x32x16_bf16 v[72:87], a[156:159], v[228:231], v[72:87]\n\t"
    "ds_read_b64_tr_b16 a[152:153], v18 offset:22528\n\t"
    "ds_read_b64_tr_b16 a[154:155], v18 offset:23552\n\t"
    "ds_read_b64_tr_b16 a[156:157], v19 offset:22528\n\t"
    "ds_read_b64_tr_b16 a[158:159], v19 offset:23552\n\t"
    "s_waitcnt lgkmcnt(8)\n\t"
    "v_mfma_f32_32x32x16_bf16 v[56:71], a[160:163], v[232:235], v[56:71]\n\t"
    "v_mfma_f32_32x32x16_bf16 v[72:87], a[164:167], v[232:235], v[72:87]\n\t"
    "ds_read_b64_tr_b16 a[160:161], v18 offset:24576\n\t"
    "ds_read_b64_tr_b16 a[162:163], v18 offset:25600\n\t"
    "ds_read_b64_tr_b16 a[164:165], v19 offset:24576\n\t"
    "ds_read_b64_tr_b16 a[166:167], v19 offset:25600\n\t"
    "v_mfma_f32_32x32x16_bf16 v[56:71], a[168:171], v[236:239], v[56:71]\n\t"
    "v_mfma_f32_32x32x16_bf16 v[72:87], a[172:175], v[236:239], v[72:87]\n\t"
    "ds_read_b64_tr_b16 a[168:169], v18 offset:26624\n\t"
    "ds_read_b64_tr_b16 a[170:171], v18 offset:27648\n\t"
    "ds_read_b64_tr_b16 a[172:173], v19 offset:26624\n\t"
    "ds_read_b64_tr_b16 a[174:175], v19 offset:27648\n\t"
    "s_waitcnt lgkmcnt(8)\n\t"
    "v_mfma_f32_32x32x16_bf16 v[56:71], a[144:147], v[240:243], v[56:71]\n\t"
    "v_mfma_f32_32x32x16_bf16 v[72:87], a[148:151], v[240:243], v[72:87]\n\t"
    "ds_read_b64_tr_b16 a[144:145], v18 offset:28672\n\t"
    "ds_read_b64_tr_b16 a[146:147], v18 offset:29696\n\t"
    "ds_read_b64_tr_b16 a[148:149], v19 offset:28672\n\t"
    "ds_read_b64_tr_b16 a[150:151], v19 offset:29696\n\t"
    "v_mfma_f32_32x32x16_bf16 v[56:71], a[152:155], v[244:247], v[56:71]\n\t"
    "v_mfma_f32_32x32x16_bf16 v[72:87], a[156:159], v[244:247], v[72:87]\n\t"
    "ds_read_b64_tr_b16 a[152:153], v18 offset:30720\n\t"
    "ds_read_b64_tr_b16 a[154:155], v18 offset:31744\n\t"
    "ds_read_b64_tr_b16 a[156:157], v19 offset:30720\n\t"
    "ds_read_b64_tr_b16 a[158:159], v19 offset:31744\n\t"
    "s_waitcnt lgkmcnt(8)\n\t"
    "v_mfma_f32_32x32x16_bf16 v[56:71], a[160:163], v[248:251], v[56:71]\n\t"
    "v_mfma_f32_32x32x16_bf16 v[72:87], a[164:167], v[248:251], v[72:87]\n\t"
    "ds_read_b64_tr_b16 a[160:161], v18 offset:32768\n\t"
    "ds_read_b64_tr_b16 a[162:163], v18 offset:33792\n\t"
    "ds_read_b64_tr_b16 a[164:165], v19 offset:32768\n\t"
    "ds_read_b64_tr_b16 a[166:167], v19 offset:33792\n\t"
    "v_mfma_f32_32x32x16_bf16 v[56:71], a[168:171], v[252:255], v[56:71]\n\t"
    "v_mfma_f32_32x32x16_bf16 v[72:87], a[172:175], v[252:255], v[72:87]\n\t"
    "ds_read_b64_tr_b16 a[168:169], v18 offset:34816\n\t"
    "ds_read_b64_tr_b16 a[170:171], v18 offset:35840\n\t"
    "ds_read_b64_tr_b16 a[172:173], v19 offset:34816\n\t"
    "ds_read_b64_tr_b16 a[174:175], v19 offset:35840\n\t"
    "s_waitcnt lgkmcnt(8)\n\t"
    "v_mfma_f32_32x32x16_bf16 v[56:71], a[144:147], a[0:3], v[56:71]\n\t"
    "s_waitcnt vmcnt(0) lgkmcnt(0)\n\t"
    "s_barrier\n\t"
    "buffer_store_dword %[vtag], %[vfl], %[wsr], s71 offen sc1\n\t"
    "s_add_u32 s71, s71, 4\n\t"
    "v_mfma_f32_32x32x16_bf16 v[72:87], a[148:151], a[0:3], v[72:87]\n\t"
    "ds_read_b64_tr_b16 a[144:145], v20 offset:0\n\t"
    "ds_read_b64_tr_b16 a[146:147], v20 offset:1024\n\t"
    "ds_read_b64_tr_b16 a[148:149], v21 offset:0\n\t"
    "ds_read_b64_tr_b16 a[150:151], v21 offset:1024\n\t"
    "v_mfma_f32_32x32x16_bf16 v[56:71], a[152:155], a[4:7], v[56:71]\n\t"
    "s_add_u32 m0, %[m0b], 0\n\t"
    "s_add_u32 s64, s64, s65\n\t"
    "buffer_load_dwordx4 v13, s[60:63], s64 offen lds\n\t"
    "v_mfma_f32_32x32x16_bf16 v[72:87], a[156:159], a[4:7], v[72:87]\n\t"
    "ds_read_b64_tr_b16 a[152:153], v20 offset:2048\n\t"
    "ds_read_b64_tr_b16 a[154:155], v20 offset:3072\n\t"
    "ds_read_b64_tr_b16 a[156:157], v21 offset:2048\n\t"
    "ds_read_b64_tr_b16 a[158:159], v21 offset:3072\n\t"
    "s_waitcnt lgkmcnt(8)\n\t"
    "v_mfma_f32_32x32x16_bf16 v[56:71], a[160:163], a[8:11], v[56:71]\n\t"
    "s_add_u32 m0, %[m0b], 1024\n\t"
    "s_add_u32 s64, s64, s65\n\t"
    "buffer_load_dwordx4 v13, s[60:63], s64 offen lds\n\t"
    "v_mfma_f32_32x32x16_bf16 v[72:87], a[164:167], a[8:11], v[72:87]\n\t"
    "ds_read_b64_tr_b16 a[160:161], v20 offset:4096\n\t"
    "ds_read_b64_tr_b16 a[162:163], v20 offset:5120\n\t"
    "ds_read_b64_tr_b16 a[164:165], v21 offset:4096\n\t"
    "ds_read_b64_tr_b16 a[166:167], v21 offset:5120\n\t"
    "v_mfma_f32_32x32x16_bf16 v[56:71], a[168:171], a[12:15], v[56:71]\n\t"
    "s_add_u32 m0, %[m0b], 2048\n\t"
    "s_add_u32 s64, s64, s65\n\t"
    "buffer_load_dwordx4 v13, s[60:63], s64 offen lds\n\t"
    "v_mfma_f32_32x32x16_bf16 v[72:87], a[172:175], a[12:15], v[72:87]\n\t"
    "ds_read_b64_tr_b16 a[168:169], v20 offset:6144\n\t"
    "ds_read_b64_tr_b16 a[170:171], v20 offset:7168\n\t"
    "ds_read_b64_tr_b16 a[172:173], v21 offset:6144\n\t"
    "ds_read_b64_tr_b16 a[174:175], v21 offset:7168\n\t"
    "s_waitcnt lgkmcnt(8)\n\t"
    "v_mfma_f32_32x32x16_bf16 v[88:103], a[144:147], v[128:131], 0\n\t"
    "s_add_u32 m0, %[m0b], 3072\n\t"
    "s_add_u32 s64, s64, s65\n\t"
    "buffer_load_dwordx4 v13, s[60:63], s64 offen lds\n\t"
    "v_mfma_f32_32x32x16_bf16 v[104:119], a[148:151], v[128:131], 0\n\t"
    "ds_read_b64_tr_b16 a[144:145], v20 offset:8192\n\t"
    "ds_read_b64_tr_b16 a[146:147], v20 offset:9216\n\t"
    "ds_read_b64_tr_b16 a[148:149], v21 offset:8192\n\t"
    "ds_read_b64_tr_b16 a[150:151], v21 offset:9216\n\t"
    "v_mfma_f32_32x32x16_bf16 v[88:103], a[152:155], v[132:135], v[88:103]\n\t"
    "s_add_u32 m0, %[m0b], 4096\n\t"
    "s_add_u32 s64, s64, s65\n\t"
    "buffer_load_dwordx4 v13, s[60:63], s64 offen lds\n\t"
    "v_mfma_f32_32x32x16_bf16 v[104:119], a[156:159], v[132:135], v[104:119]\n\t"
    "ds_read_b64_tr_b16 a[152:153], v20 offset:10240\n\t"
    "ds_read_b64_tr_b16 a[154:155], v20 offset:11264\n\t"
    "ds_read_b64_tr_b16 a[156:157], v21 offset:10240\n\t"
    "ds_read_b64_tr_b16 a[158:159], v21 offset:11264\n\t"
    "s_waitcnt lgkmcnt(8)\n\t"
    "v_mfma_f32_32x32x16_bf16 v[88:103], a[160:163], v[136:139], v[88:103]\n\t"
    "s_add_u32 m0, %[m0b], 5120\n\t"
    "s_add_u32 s64, s64, s65\n\t"
    "buffer_load_dwordx4 v13, s[60:63], s64 offen lds\n\t"
    "v_mfma_f32_32x32x16_bf16 v[104:119], a[164:167], v[136:139], v[104:119]\n\t"
    "ds_read_b64_tr_b16 a[160:161], v20 offset:12288\n\t"
    "ds_read_b64_tr_b16 a[162:163], v20 offset:13312\n\t"
    "ds_read_b64_tr_b16 a[164:165], v21 offset:12288\n\t"
    "ds_read_b64_tr_b16 a[166:167], v21 offset:13312\n\t"
    "v_mfma_f32_32x32x16_bf16 v[88:103], a[168:171], v[140:143], v[88:103]\n\t"
    "s_add_u32 m0, %[m0b], 6144\n\t"
    "s_add_u32 s64, s64, s65\n\t"
    "buffer_load_dwordx4 v13, s[60:63], s64 offen lds\n\t"
    "v_mfma_f32_32x32x16_bf16 v[104:119], a[172:175], v[140:143], v[104:119]\n\t"
    "ds_read_b64_tr_b16 a[168:169], v20 offset:14336\n\t"
    "ds_read_b64_tr_b16 a[170:171], v20 offset:15360\n\t"
    "ds_read_b64_tr_b16 a[172:173], v21 offset:14336\n\t"
    "ds_read_b64_tr_b16 a[174:175], v21 offset:15360\n\t"
    "s_waitcnt lgkmcnt(8)\n\t"
    "v_mfma_f32_32x32x16_bf16 v[88:103], a[144:147], v[144:147], v[88:103]\n\t"
    "s_add_u32 m0, %[m0b], 7168\n\t"
    "s_add_u32 s64, s64, s65\n\t"
    "buffer_load_dwordx4 v13, s[60:63], s64 offen lds\n\t"
    "v_mfma_f32_32x32x16_bf16 v[104:119], a[148:151], v[144:147], v[104:119]\n\t"
    "ds_read_b64_tr_b16 a[144:145], v20 offset:16384\n\t"
    "ds_read_b64_tr_b16 a[146:147], v20 offset:17408\n\t"
    "ds_read_b64_tr_b16 a[148:149], v21 offset:16384\n\t"
    "ds_read_b64_tr_b16 a[150:151], v21 offset:17408\n\t"
    "v_mfma_f32_32x32x16_bf16 v[88:103], a[152:155], v[148:151], v[88:103]\n\t"
    "s_add_u32 m0, %[m0b], 8192\n\t"
    "s_add_u32 s64, s64, s65\n\t"
    "buffer_load_dwordx4 v13, s[60:63], s64 offen lds\n\t"
    "s_add_u32 s64, s64, s66\n\t"
    "v_mfma_f32_32x32x16_bf16 v[104:119], a[156:159], v[148:151], v[104:119]\n\t"
    "ds_read_b64_tr_b16 a[152:153], v20 offset:18432\n\t"
    "ds_read_b64_tr_b16 a[154:155], v20 offset:19456\n\t"
    "ds_read_b64_tr_b16 a[156:157], v21 offset:18432\n\t"
    "ds_read_b64_tr_b16 a[158:159], v21 offset:19456\n\t"
    "s_waitcnt lgkmcnt(8)\n\t"
    "v_mfma_f32_32x32x16_bf16 v[88:103], a[160:163], v[152:155], v[88:103]\n\t"
    "s_add_u32 m0, %[m0b], 36864\n\t"
    "s_add_u32 s64, s64, s65\n\t"
    "buffer_load_dwordx4 v13, s[60:63], s64 offen lds\n\t"
    "v_mfma_f32_32x32x16_bf16 v[104:119], a[164:167], v[152:155], v[104:119]\n\t"
    "ds_read_b64_tr_b16 a[160:161], v20 offset:20480\n\t"
    "ds_read_b64_tr_b16 a[162:163], v20 offset:21504\n\t"
    "ds_read_b64_tr_b16 a[164:165], v21 offset:20480\n\t"
    "ds_read_b64_tr_b16 a[166:167], v21 offset:21504\n\t"
    "v_mfma_f32_32x32x16_bf16 v[88:103], a[168:171], v[156:159], v[88:103]\n\t"
    "s_add_u32 m0, %[m0b], 37888\n\t"
    "s_add_u32 s64, s64, s65\n\t"
    "buffer_load_dwordx4 v13, s[60:63], s64 offen lds\n\t"
    "v_mfma_f32_32x32x16_bf16 v[104:119], a[172:175], v[156:159], v[104:119]\n\t"
    "ds_read_b64_tr_b16 a[168:169], v20 offset:22528\n\t"
    "ds_read_b64_tr_b16 a[170:171], v20 offset:23552\n\t"
    "ds_read_b64_tr_b16 a[172:173], v21 offset:22528\n\t"
    "ds_read_b64_tr_b16 a[174:175], v21 offset:23552\n\t"
    "s_waitcnt lgkmcnt(8)\n\t"
    "v_mfma_f32_32x32x16_bf16 v[88:103], a[144:147], v[160:163], v[88:103]\n\t"
    "s_add_u32 m0, %[m0b], 38912\n\t"
    "s_add_u32 s64, s64, s65\n\t"
    "buffer_load_dwordx4 v13, s[60:63], s64 offen lds\n\t"
    "v_mfma_f32_32x32x16_bf16 v[104:119], a[148:151], v[160:163], v[104:119]\n\t"
    "ds_read_b64_tr_b16 a[144:145], v20 offset:24576\n\t"
    "ds_read_b64_tr_b16 a[146:147], v20 offset:25600\n\t"
    "ds_read_b64_tr_b16 a[148:149], v21 offset:24576\n\t"
    "ds_read_b64_tr_b16 a[150:151], v21 offset:25600\n\t"
    "v_mfma_f32_32x32x16_bf16 v[88:103], a[152:155], v[164:167], v[88:103]\n\t"
    "s_add_u32 m0, %[m0b], 39936\n\t"
    "s_add_u32 s64, s64, s65\n\t"
    "buffer_load_dwordx4 v13, s[60:63], s64 offen lds\n\t"
    "v_mfma_f32_32x32x16_bf16 v[104:119], a[156:159], v[164:167], v[104:119]\n\t"
    "ds_read_b64_tr_b16 a[152:153], v20 offset:26624\n\t"
    "ds_read_b64_tr_b16 a[154:155], v20 offset:27648\n\t"
    "ds_read_b64_tr_b16 a[156:157], v21 offset:26624\n\t"
    "ds_read_b64_tr_b16 a[158:159], v21 offset:27648\n\t"
    "s_waitcnt lgkmcnt(8)\n\t"
    "v_mfma_f32_32x32x16_bf16 v[88:103], a[160:163], v[168:171], v[88:103]\n\t"
    "s_add_u32 m0, %[m0b], 40960\n\t"
    "s_add_u32 s64, s64, s65\n\t"
    "buffer_load_dwordx4 v13, s[60:63], s64 offen lds\n\t"
    "v_mfma_f32_32x32x16_bf16 v[104:119], a[164:167], v[168:171], v[104:119]\n\t"
    "ds_read_b64_tr_b16 a[160:161], v20 offset:28672\n\t"
    "ds_read_b64_tr_b16 a[162:163], v20 offset:29696\n\t"
    "ds_read_b64_tr_b16 a[164:165], v21 offset:28672\n\t"
    "ds_read_b64_tr_b16 a[166:167], v21 offset:29696\n\t"
    "v_mfma_f32_32x32x16_bf16 v[88:103], a[168:171], v[172:175], v[88:103]\n\t"
    "s_add_u32 m0, %[m0b], 41984\n\t"
    "s_add_u32 s64, s64, s65\n\t"
    "buffer_load_dwordx4 v13, s[60:63], s64 offen lds\n\t"
    "v_mfma_f32_32x32x16_bf16 v[104:119], a[172:175], v[172:175], v[104:119]\n\t"
    "ds_read_b64_tr_b16 a[168:169], v20 offset:30720\n\t"
    "ds_read_b64_tr_b16 a[170:171], v20 offset:31744\n\t"
    "ds_read_b64_tr_b16 a[172:173], v21 offset:30720\n\t"
    "ds_read_b64_tr_b16 a[174:175], v21 offset:31744\n\t"
    "s_waitcnt lgkmcnt(8)\n\t"
    "v_mfma_f32_32x32x16_bf16 v[88:103], a[144:147], v[176:179], v[88:103]\n\t"
    "s_add_u32 m0, %[m0b], 43008\n\t"
    "s_add_u32 s64, s64, s65\n\t"
    "buffer_load_dwordx4 v13, s[60:63], s64 offen lds\n\t"
    "v_mfma_f32_32x32x16_bf16 v[104:119], a[148:151], v[176:179], v[104:119]\n\t"
    "ds_read_b64_tr_b16 a[144:145], v20 offset:32768\n\t"
    "ds_read_b64_tr_b16 a[146:147], v20 offset:33792\n\t"
    "ds_read_b64_tr_b16 a[148:149], v21 offset:32768\n\t"
    "ds_read_b64_tr_b16 a[150:151], v21 offset:33792\n\t"
    "v_mfma_f32_32x32x16_bf16 v[88:103], a[152:155], v[180:183], v[88:103]\n\t"
    "s_add_u32 m0, %[m0b], 44032\n\t"
    "s_add_u32 s64, s64, s65\n\t"
    "buffer_load_dwordx4 v13, s[60:63], s64 offen lds\n\t"
    "v_mfma_f32_32x32x16_bf16 v[104:119], a[156:159], v[180:183], v[104:119]\n\t"
    "ds_read_b64_tr_b16 a[152:153], v20 offset:34816\n\t"
    "ds_read_b64_tr_b16 a[154:155], v20 offset:35840\n\t"
    "ds_read_b64_tr_b16 a[156:157], v21 offset:34816\n\t"
    "ds_read_b64_tr_b16 a[158:159], v21 offset:35840\n\t"
    "s_waitcnt lgkmcnt(8)\n\t"
    "v_mfma_f32_32x32x16_bf16 v[88:103], a[160:163], v[184:187], v[88:103]\n\t"
    "s_add_u32 m0, %[m0b], 45056\n\t"
    "s_add_u32 s64, s64, s65\n\t"
    "buffer_load_dwordx4 v13, s[60:63], s64 offen lds\n\t"
    "s_add_u32 s64, s64, s67\n\t"
    "v_mfma_f32_32x32x16_bf16 v[104:119], a[164:167], v[184:187], v[104:119]\n\t"
    "ds_read_b64_tr_b16 a[160:161], v22 offset:0\n\t"
    "ds_read_b64_tr_b16 a[162:163], v22 offset:1024\n\t"
    "ds_read_b64_tr_b16 a[164:165], v23 offset:0\n\t"
    "ds_read_b64_tr_b16 a[166:167], v23 offset:1024\n\t"
    "v_mfma_f32_32x32x16_bf16 v[88:103], a[168:171], v[188:191], v[88:103]\n\t"
    "v_mfma_f32_32x32x16_bf16 v[104:119], a[172:175], v[188:191], v[104:119]\n\t"
    "ds_read_b64_tr_b16 a[168:169], v22 offset:2048\n\t"
    "ds_read_b64_tr_b16 a[170:171], v22 offset:3072\n\t"
    "ds_read_b64_tr_b16 a[172:173], v23 offset:2048\n\t"
    "ds_read_b64_tr_b16 a[174:175], v23 offset:3072\n\t"
    "s_waitcnt lgkmcnt(8)\n\t"
    "v_mfma_f32_32x32x16_bf16 v[88:103], a[144:147], v[192:195], v[88:103]\n\t"
    "v_cvt_pk_bf16_f32 v56, v56, v57\n\t"
    "v_cvt_pk_bf16_f32 v57, v58, v59\n\t"
    "v_cvt_pk_bf16_f32 v58, v60, v61\n\t"
    "v_cvt_pk_bf16_f32 v59, v62, v63\n\t"
    "buffer_store_dwordx4 v[56:59], %[pd], %[wsr], s70 offen offset:0 sc1\n\t"
    "v_mfma_f32_32x32x16_bf16 v[104:119], a[148:151], v[192:195], v[104:119]\n\t"
    "ds_read_b64_tr_b16 a[144:145], v22 offset:4096\n\t"
    "ds_read_b64_tr_b16 a[146:147], v22 offset:5120\n\t"
    "ds_read_b64_tr_b16 a[148:149], v23 offset:4096\n\t"
    "ds_read_b64_tr_b16 a[150:151], v23 offset:5120\n\t"
    "v_mfma_f32_32x32x16_bf16 v[88:103], a[152:155], v[196:199], v[88:103]\n\t"
    "v_cvt_pk_bf16_f32 v64, v64, v65\n\t"
    "v_cvt_pk_bf16_f32 v65, v66, v67\n\t"
    "v_cvt_pk_bf16_f32 v66, v68, v69\n\t"
    "v_cvt_pk_bf16_f32 v67, v70, v71\n\t"
    "buffer_store_dwordx4 v[64:67], %[pd], %[wsr], s70 offen offset:1024 sc1\n\t"
    "v_mfma_f32_32x32x16_bf16 v[104:119], a[156:159], v[196:199], v[104:119]\n\t"
    "ds_read_b64_tr_b16 a[152:153], v22 offset:6144\n\t"
    "ds_read_b64_tr_b16 a[154:155], v22 offset:7168\n\t"
    "ds_read_b64_tr_b16 a[156:157], v23 offset:6144\n\t"
    "ds_read_b64_tr_b16 a[158:159], v23 offset:7168\n\t"
    "s_waitcnt lgkmcnt(8)\n\t"
    "v_mfma_f32_32x32x16_bf16 v[88:103], a[160:163], v[200:203], v[88:103]\n\t"
    "v_cvt_pk_bf16_f32 v72, v72, v73\n\t"
    "v_cvt_pk_bf16_f32 v73, v74, v75\n\t"
    "v_cvt_pk_bf16_f32 v74, v76, v77\n\t"
    "v_cvt_pk_bf16_f32 v75, v78, v79\n\t"
    "buffer_store_dwordx4 v[72:75], %[pd], %[wsr], s70 offen offset:2048 sc1\n\t"
    "v_mfma_f32_32x32x16_bf16 v[104:119], a[164:167], v[200:203], v[104:119]\n\t"
    "ds_read_b64_tr_b16 a[160:161], v22 offset:8192\n\t"
    "ds_read_b64_tr_b16 a[162:163], v22 offset:9216\n\t"
    "ds_read_b64_tr_b16 a[164:165], v23 offset:8192\n\t"
    "ds_read_b64_tr_b16 a[166:167], v23 offset:9216\n\t"
    "v_mfma_f32_32x32x16_bf16 v[88:103], a[168:171], v[204:207], v[88:103]\n\t"
    "v_cvt_pk_bf16_f32 v80, v80, v81\n\t"
    "v_cvt_pk_bf16_f32 v81, v82, v83\n\t"
    "v_cvt_pk_bf16_f32 v82, v84, v85\n\t"
    "v_cvt_pk_bf16_f32 v83, v86, v87\n\t"
    "buffer_store_dwordx4 v[80:83], %[pd], %[wsr], s70 offen offset:3072 sc1\n\t"
    "v_mfma_f32_32x32x16_bf16 v[104:119], a[172:175], v[204:207], v[104:119]\n\t"
    "ds_read_b64_tr_b16 a[168:169], v22 offset:10240\n\t"
    "ds_read_b64_tr_b16 a[170:171], v22 offset:11264\n\t"
    "ds_read_b64_tr_b16 a[172:173], v23 offset:10240\n\t"
    "ds_read_b64_tr_b16 a[174:175], v23 offset:11264\n\t"
    "s_waitcnt lgkmcnt(8)\n\t"
    "v_mfma_f32_32x32x16_bf16 v[88:103], a[144:147], v[208:211], v[88:103]\n\t"
    "s_add_u32 s70, s70, 0x1000\n\t"
    "s_cmp_eq_u32 s70, 0x9000\n\t"
    "s_cselect_b32 s70, 0, s70\n\t"
    "v_mfma_f32_32x32x16_bf16 v[104:119], a[148:151], v[208:211], v[104:119]\n\t"
    "ds_read_b64_tr_b16 a[144:145], v22 offset:12288\n\t"
    "ds_read_b64_tr_b16 a[146:147], v22 offset:13312\n\t"
    "ds_read_b64_tr_b16 a[148:149], v23 offset:12288\n\t"
    "ds_read_b64_tr_b16 a[150:151], v23 offset:13312\n\t"
    "v_mfma_f32_32x32x16_bf16 v[88:103], a[152:155], v[212:215], v[88:103]\n\t"
    "v_mfma_f32_32x32x16_bf16 v[104:119], a[156:159], v[212:215], v[104:119]\n\t"
    "ds_read_b64_tr_b16 a[152:153], v22 offset:14336\n\t"
    "ds_read_b64_tr_b16 a[154:155], v22 offset:15360\n\t"
    "ds_read_b64_tr_b16 a[156:157], v23 offset:14336\n\t"
    "ds_read_b64_tr_b16 a[158:159], v23 offset:15360\n\t"
    "s_waitcnt lgkmcnt(8)\n\t"
    "v_mfma_f32_32x32x16_bf16 v[88:103], a[160:163], v[216:219], v[88:103]\n\t"
    "v_mfma_f32_32x32x16_bf16 v[104:119], a[164:167], v[216:219], v[104:119]\n\t"
    "ds_read_b64_tr_b16 a[160:161], v22 offset:16384\n\t"
    "ds_read_b64_tr_b16 a[162:163], v22 offset:17408\n\t"
    "ds_read_b64_tr_b16 a[164:165], v23 offset:16384\n\t"
    "ds_read_b64_tr_b16 a[166:167], v23 offset:17408\n\t"
    "v_mfma_f32_32x32x16_bf16 v[88:103], a[168:171], v[220:223], v[88:103]\n\t"
    "v_mfma_f32_32x32x16_bf16 v[104:119], a[172:175], v[220:223], v[104:119]\n\t"
    "ds_read_b64_tr_b16 a[168:169], v22 offset:18432\n\t"
    "ds_read_b64_tr_b16 a[170:171], v22 offset:19456\n\t"
    "ds_read_b64_tr_b16 a[172:173], v23 offset:18432\n\t"
    "ds_read_b64_tr_b16 a[174:175], v23 offset:19456\n\t"
    "s_waitcnt lgkmcnt(8)\n\t"
    "v_mfma_f32_32x32x16_bf16 v[88:103], a[144:147], v[224:227], v[88:103]\n\t"
    "v_mfma_f32_32x32x16_bf16 v[104:119], a[148:151], v[224:227], v[104:119]\n\t"
    "ds_read_b64_tr_b16 a[144:145], v22 offset:20480\n\t"
    "ds_read_b64_tr_b16 a[146:147], v22 offset:21504\n\t"
    "ds_read_b64_tr_b16 a[148:149], v23 offset:20480\n\t"
    "ds_read_b64_tr_b16 a[150:151], v23 offset:21504\n\t"
    "v_mfma_f32_32x32x16_bf16 v[88:103], a[152:155], v[228:231], v[88:103]\n\t"
    "v_mfma_f32_32x32x16_bf16 v[104:119], a[156:159], v[228:231], v[104:119]\n\t"
    "ds_read_b64_tr_b16 a[152:153], v22 offset:22528\n\t"
    "ds_read_b64_tr_b16 a[154:155], v22 offset:23552\n\t"
    "ds_read_b64_tr_b16 a[156:157], v23 offset:22528\n\t"
    "ds_read_b64_tr_b16 a[158:159], v23 offset:23552\n\t"
    "s_waitcnt lgkmcnt(8)\n\t"
    "v_mfma_f32_32x32x16_bf16 v[88:103], a[160:163], v[232:235], v[88:103]\n\t"
    "v_mfma_f32_32x32x16_bf16 v[104:119], a[164:167], v[232:235], v[104:119]\n\t"
    "ds_read_b64_tr_b16 a[160:161], v22 offset:24576\n\t"
    "ds_read_b64_tr_b16 a[162:163], v22 offset:25600\n\t"
    "ds_read_b64_tr_b16 a[164:165], v23 offset:24576\n\t"
    "ds_read_b64_tr_b16 a[166:167], v23 offset:25600\n\t"
    "v_mfma_f32_32x32x16_bf16 v[88:103], a[168:171], v[236:239], v[88:103]\n\t"
    "v_mfma_f32_32x32x16_bf16 v[104:119], a[172:175], v[236:239], v[104:119]\n\t"
    "ds_read_b64_tr_b16 a[168:169], v22 offset:26624\n\t"
    "ds_read_b64_tr_b16 a[170:171], v22 offset:27648\n\t"
    "ds_read_b64_tr_b16 a[172:173], v23 offset:26624\n\t"
    "ds_read_b64_tr_b16 a[174:175], v23 offset:27648\n\t"
    "s_waitcnt lgkmcnt(8)\n\t"
    "v_mfma_f32_32x32x16_bf16 v[88:103], a[144:147], v[240:243], v[88:103]\n\t"
    "v_mfma_f32_32x32x16_bf16 v[104:119], a[148:151], v[240:243], v[104:119]\n\t"
    "ds_read_b64_tr_b16 a[144:145], v22 offset:28672\n\t"
    "ds_read_b64_tr_b16 a[146:147], v22 offset:29696\n\t"
    "ds_read_b64_tr_b16 a[148:149], v23 offset:28672\n\t"
    "ds_read_b64_tr_b16 a[150:151], v23 offset:29696\n\t"
    "v_mfma_f32_32x32x16_bf16 v[88:103], a[152:155], v[244:247], v[88:103]\n\t"
    "v_mfma_f32_32x32x16_bf16 v[104:119], a[156:159], v[244:247], v[104:119]\n\t"
    "ds_read_b64_tr_b16 a[152:153], v22 offset:30720\n\t"
    "ds_read_b64_tr_b16 a[154:155], v22 offset:31744\n\t"
    "ds_read_b64_tr_b16 a[156:157], v23 offset:30720\n\t"
    "ds_read_b64_tr_b16 a[158:159], v23 offset:31744\n\t"
    "s_waitcnt lgkmcnt(8)\n\t"
    "v_mfma_f32_32x32x16_bf16 v[88:103], a[160:163], v[248:251], v[88:103]\n\t"
    "v_mfma_f32_32x32x16_bf16 v[104:119], a[164:167], v[248:251], v[104:119]\n\t"
    "ds_read_b64_tr_b16 a[160:161], v22 offset:32768\n\t"
    "ds_read_b64_tr_b16 a[162:163], v22 offset:33792\n\t"
    "ds_read_b64_tr_b16 a[164:165], v23 offset:32768\n\t"
    "ds_read_b64_tr_b16 a[166:167], v23 offset:33792\n\t"
    "v_mfma_f32_32x32x16_bf16 v[88:103], a[168:171], v[252:255], v[88:103]\n\t"
    "v_mfma_f32_32x32x16_bf16 v[104:119], a[172:175], v[252:255], v[104:119]\n\t"
    "ds_read_b64_tr_b16 a[168:169], v22 offset:34816\n\t"
    "ds_read_b64_tr_b16 a[170:171], v22 offset:35840\n\t"
    "ds_read_b64_tr_b16 a[172:173], v23 offset:34816\n\t"
    "ds_read_b64_tr_b16 a[174:175], v23 offset:35840\n\t"
    "s_waitcnt lgkmcnt(8)\n\t"
    "v_mfma_f32_32x32x16_bf16 v[88:103], a[144:147], a[0:3], v[88:103]\n\t"
    "s_waitcnt vmcnt(0) lgkmcnt(0)\n\t"
    "s_barrier\n\t"
    "buffer_store_dword %[vtag], %[vfl], %[wsr], s71 offen sc1\n\t"
    "s_add_u32 s71, s71, 4\n\t"
    "v_mfma_f32_32x32x16_bf16 v[104:119], a[148:151], a[0:3], v[104:119]\n\t"
    "ds_read_b64_tr_b16 a[144:145], v16 offset:0\n\t"
    "ds_read_b64_tr_b16 a[146:147], v16 offset:1024\n\t"
    "ds_read_b64_tr_b16 a[148:149], v17 offset:0\n\t"
    "ds_read_b64_tr_b16 a[150:151], v17 offset:1024\n\t"
    "v_mfma_f32_32x32x16_bf16 v[88:103], a[152:155], a[4:7], v[88:103]\n\t"
    "s_add_u32 m0, %[m0b], 73728\n\t"
    "s_add_u32 s64, s64, s65\n\t"
    "buffer_load_dwordx4 v13, s[60:63], s64 offen lds\n\t"
    "v_mfma_f32_32x32x16_bf16 v[104:119], a[156:159], a[4:7], v[104:119]\n\t"
    "ds_read_b64_tr_b16 a[152:153], v16 offset:2048\n\t"
    "ds_read_b64_tr_b16 a[154:155], v16 offset:3072\n\t"
    "ds_read_b64_tr_b16 a[156:157], v17 offset:2048\n\t"
    "ds_read_b64_tr_b16 a[158:159], v17 offset:3072\n\t"
    "s_waitcnt lgkmcnt(8)\n\t"
    "v_mfma_f32_32x32x16_bf16 v[88:103], a[160:163], a[8:11], v[88:103]\n\t"
    "s_add_u32 m0, %[m0b], 74752\n\t"
    "s_add_u32 s64, s64, s65\n\t"
    "buffer_load_dwordx4 v13, s[60:63], s64 offen lds\n\t"
    "v_mfma_f32_32x32x16_bf16 v[104:119], a[164:167], a[8:11], v[104:119]\n\t"
    "ds_read_b64_tr_b16 a[160:161], v16 offset:4096\n\t"
    "ds_read_b64_tr_b16 a[162:163], v16 offset:5120\n\t"
    "ds_read_b64_tr_b16 a[164:165], v17 offset:4096\n\t"
    "ds_read_b64_tr_b16 a[166:167], v17 offset:5120\n\t"
    "v_mfma_f32_32x32x16_bf16 v[88:103], a[168:171], a[12:15], v[88:103]\n\t"
    "s_add_u32 m0, %[m0b], 75776\n\t"
    "s_add_u32 s64, s64, s65\n\t"
    "buffer_load_dwordx4 v13, s[60:63], s64 offen lds\n\t"
    "v_mfma_f32_32x32x16_bf16 v[104:119], a[172:175], a[12:15], v[104:119]\n\t"
    "s_sub_u32 s68, s68, 1\n\t"
    "s_cmp_lg_u32 s68, 0\n\t"
    "s_cbranch_scc1 .Lml2_%=\n\t"
    "s_waitcnt lgkmcnt(0)\n\t"
    "s_nop 15\n\t"
    "s_nop 3\n\t"
    "v_cvt_pk_bf16_f32 v88, v88, v89\n\t"
    "v_cvt_pk_bf16_f32 v89, v90, v91\n\t"
    "v_cvt_pk_bf16_f32 v90, v92, v93\n\t"
    "v_cvt_pk_bf16_f32 v91, v94, v95\n\t"
    "buffer_store_dwordx4 v[88:91], %[pd], %[wsr], s70 offen offset:0 sc1\n\t"
    "v_cvt_pk_bf16_f32 v96, v96, v97\n\t"
    "v_cvt_pk_bf16_f32 v97, v98, v99\n\t"
    "v_cvt_pk_bf16_f32 v98, v100, v101\n\t"
    "v_cvt_pk_bf16_f32 v99, v102, v103\n\t"
    "buffer_store_dwordx4 v[96:99], %[pd], %[wsr], s70 offen offset:1024 sc1\n\t"
    "v_cvt_pk_bf16_f32 v104, v104, v105\n\t"
    "v_cvt_pk_bf16_f32 v105, v106, v107\n\t"
    "v_cvt_pk_bf16_f32 v106, v108, v109\n\t"
    "v_cvt_pk_bf16_f32 v107, v110, v111\n\t"
    "buffer_store_dwordx4 v[104:107], %[pd], %[wsr], s70 offen offset:2048 sc1\n\t"
    "v_cvt_pk_bf16_f32 v112, v112, v113\n\t"
    "v_cvt_pk_bf16_f32 v113, v114, v115\n\t"
    "v_cvt_pk_bf16_f32 v114, v116, v117\n\t"
    "v_cvt_pk_bf16_f32 v115, v118, v119\n\t"
    "buffer_store_dwordx4 v[112:115], %[pd], %[wsr], s70 offen offset:3072 sc1\n\t"
        : X_OUT(X), "+{v16}"(ab[0]), "+{v17}"(ab[1]), "+{v12}"(csa), "+{v13}"(vo3) P1_DBG_OUT
        : [vo4] "v"(vo576), [vo16] "v"(voff16), [vst] "v"(c * 8), [vrs] "v"(c8 == 0 && hh == 0 ? WS_RS1 + (tb * 32 + c) * 4 : OOB),
          [vtag] "v"(tag), [vfl] "v"(lane == 0 ? WS_FLAG + (FL_PART + (tt * 8 + c8) * 16) * 4 : OOB),
          [pd] "v"(WS_PART + ((c8 * 64 + tb) * 18) * 2048 + voff16), [wsr] "s"(ws), [m0b] "s"(ldsb + w * 9216), [so3] "s"(so3), [so4] "s"(so4),
          [sst] "s"(WS_ST1 + tb * 18 * 256), [sx1] "s"(WS_X1F + tb * 36864),
          [w3a] "s"((unsigned)w3p), [w3b] "s"((unsigned)(w3p >> 32) & 0xffffu), [w4a] "s"((unsigned)w4p), [w4b] "s"((unsigned)(w4p >> 32) & 0xffffu)
        : "memory", "scc", "vcc", "m0", "s60", "s61", "s62", "s63", "s64", "s65", "s66", "s67", "s68", "s69", "s70", "s71", "v14", "v15", "v18", "v19", "v20", "v21", "v22", "v23", "v24", "v25", "v26", "v27", "v28", "v29", "v30", "v31", "v32", "v33", "v34", "v35", "v36", "v37", "v38", "v39", "v40", "v41", "v42", "v43", "v44", "v45", "v46", "v47", "v48", "v49", "v50", "v51", "v52", "v53", "v54", "v55", "v56", "v57", "v58", "v59", "v60", "v61", "v62", "v63", "v64", "v65", "v66", "v67", "v68", "v69", "v70", "v71", "v72", "v73", "v74", "v75", "v76", "v77", "v78", "v79", "v80", "v81", "v82", "v83", "v84", "v85", "v86", "v87", "v88", "v89", "v90", "v91", "v92", "v93", "v94", "v95", "v96", "v97", "v98", "v99", "v100", "v101", "v102", "v103", "v104", "v105", "v106", "v107", "v108", "v109", "v110", "v111", "v112", "v113", "v114", "v115", "v116", "v117", "v118", "v119", "v120", "v121", "v122", "v123", "v124", "v125", "v126", "v127", "v128", "v129", "v130", "v131", "v132", "v133", "v134", "v135", "v136", "v137", "v138", "v139", "v140", "v141", "v142", "v143", "v144", "v145", "v146", "v147", "v148", "v149", "v150", "v151", "v152", "v153", "v154", "v155", "v156", "v157", "v158", "v159", "v160", "v161", "v162", "v163", "v164", "v165", "v166", "v167", "v168", "v169", "v170", "v171", "v172", "v173", "v174", "v175", "v176", "v177", "v178", "v179", "v180", "v181", "v182", "v183", "v184", "v185", "v186", "v187", "v188", "v189", "v190", "v191", "v192", "v193", "v194", "v195", "v196", "v197", "v198", "v199", "v200", "v201", "v202", "v203", "v204", "v205", "v206", "v207", "v208", "v209", "v210", "v211", "v212", "v213", "v214", "v215", "v216", "v217", "v218", "v219", "v220", "v221", "v222", "v223", "v224", "v225", "v226", "v227", "v228", "v229", "v230", "v231", "v232", "v233", "v234", "v235", "v236", "v237", "v238", "v239", "v240", "v241", "v242", "v243", "v244", "v245", "v246", "v247", "v248", "v249", "v250", "v251", "v252", "v253", "v254", "v255", "a144", "a145", "a146", "a147", "a148", "a149", "a150", "a151", "a152", "a153", "a154", "a155", "a156", "a157", "a158", "a159", "a160", "a161", "a162", "a163", "a164", "a165", "a166", "a167", "a168", "a169", "a170", "a171", "a172", "a173", "a174", "a175");
  }
  STAMP(8);
  publish(flags + FL_PART + (tt * 8 + c8) * 16 + 8, tag, w);
}

DEV void fin_phase(const Params& P, char* lds, unsigned ldsb, srd_t ws, unsigned tag, int tt, int c8, int w) {
  const int lane = lane_id(), c = lane & 31, hh = lane >> 5;
  unsigned* flags = (unsigned*)(P.ws + WS_FLAG);
  const int tb = tt * 4 + w;
  const unsigned voff16 = lane * 16;
  const bool extra = w == (c8 & 3);
  const int db[3] = {2 * c8, 2 * c8 + 1, 16 + (c8 >> 2)};
  u32x4 i0, i1;
#pragma unroll
  for (int d = 0; d < 4; d++) {
    const int k = 8 * (d >> 1) + 4 * hh + 2 * (d & 1);
    i0[d] = (c == k ? 0x3f80u : 0u) | (c == k + 1 ? 0x3f800000u : 0u);
    i1[d] = (c == k + 16 ? 0x3f80u : 0u) | (c == k + 17 ? 0x3f800000u : 0u);
  }
  const bf16x8 id0 = as_bf(i0), id1 = as_bf(i1);
  const srd_t xs = mk_srd(P.x, S * D * 2), outs = mk_srd(P.out, S * D * 2);
  const unsigned xo = (tb * 32 + c) * (D * 2) + 8 * hh;
  const unsigned vo3[3] = {voff16, voff16, extra ? voff16 : OOB}, xo3[3] = {xo, xo, extra ? xo : OOB};
  u32x2 xv[3][4];
  f32x16 y[3] = {f32x16{}, f32x16{}, f32x16{}};
  const int pidx = FL_PART + (tt * 8 + (lane & 7)) * 16 + (lane < 8 ? 0 : 1 + c8);
  volatile unsigned* ord = (volatile unsigned*)(lds + LDS_CTRL);
  if (w == 0) { const bool r = __all(lane >= 16 || __hip_atomic_load(flags + pidx, __ATOMIC_RELAXED, __HIP_MEMORY_SCOPE_AGENT) == tag); if (lane == 0) *ord = r; }
  BARRIER();
  const bool b_first = __builtin_amdgcn_readfirstlane(*ord) != 0;
  f32x16 acc[3] = {f32x16{}, f32x16{}, f32x16{}};
#pragma unroll
  for (int u = 0; u < 3; u++)
#pragma unroll
    for (int q = 0; q < 4; q++) xv[u][q] = __builtin_amdgcn_raw_buffer_load_b64(xs, xo3[u] + 16 * q, db[u] * 64, 0);
#pragma unroll 1
  for (int step = 0; step < 2; step++) {
    if ((step == 0) == b_first) {
      await_wg(ws, ldsb, pidx, lane < 16, tag, w);
      STAMP(6);
      const float rs1 = __uint_as_float(ld32(ws, WS_RS1 + (tb * 32 + c) * 4));
      f32x16 yp[3];
      u32x4 xa[3], xb[3];
#pragma unroll
      for (int u = 0; u < 3; u++) { xa[u] = ld128s(ws, vo3[u], WS_X1F + (tb * 36 + 2 * db[u]) * 1024); xb[u] = ld128s(ws, vo3[u] + 1024, WS_X1F + (tb * 36 + 2 * db[u]) * 1024); }
      const unsigned sp = WS_PART + tb * 18 * 2048;
      asm volatile(
    "s_mov_b32 s64, %[sp0]\n\t"
    "buffer_load_dwordx4 a[0:3], %[vo], %[ws], s64 offen sc1\n\t"
    "buffer_load_dwordx4 a[4:7], %[vo], %[ws], s64 offen offset:1024 sc1\n\t"
    "s_add_u32 s64, s64, 2359296\n\t"
    "buffer_load_dwordx4 a[8:11], %[vo], %[ws], s64 offen sc1\n\t"
    "buffer_load_dwordx4 a[12:15], %[vo], %[ws], s64 offen offset:1024 sc1\n\t"
    "s_add_u32 s64, s64, 2359296\n\t"
    "buffer_load_dwordx4 a[16:19], %[vo], %[ws], s64 offen sc1\n\t"
    "buffer_load_dwordx4 a[20:23], %[vo], %[ws], s64 offen offset:1024 sc1\n\t"
    "s_add_u32 s64, s64, 2359296\n\t"
    "buffer_load_dwordx4 a[24:27], %[vo], %[ws], s64 offen sc1\n\t"
    "buffer_load_dwordx4 a[28:31], %[vo], %[ws], s64 offen offset:1024 sc1\n\t"
    "s_add_u32 s64, s64, 2359296\n\t"
    "buffer_load_dwordx4 a[32:35], %[vo], %[ws], s64 offen sc1\n\t"
    "buffer_load_dwordx4 a[36:39], %[vo], %[ws], s64 offen offset:1024 sc1\n\t"
    "s_add_u32 s64, s64, 2359296\n\t"
    "buffer_load_dwordx4 a[40:43], %[vo], %[ws], s64 offen sc1\n\t"
    "buffer_load_dwordx4 a[44:47], %[vo], %[ws], s64 offen offset:1024 sc1\n\t"
    "s_add_u32 s64, s64, 2359296\n\t"
    "buffer_load_dwordx4 a[48:51], %[vo], %[ws], s64 offen sc1\n\t"
    "buffer_load_dwordx4 a[52:55], %[vo], %[ws], s64 offen offset:1024 sc1\n\t"
    "s_add_u32 s64, s64, 2359296\n\t"
    "buffer_load_dwordx4 a[56:59], %[vo], %[ws], s64 offen sc1\n\t"
    "buffer_load_dwordx4 a[60:63], %[vo], %[ws], s64 offen offset:1024 sc1\n\t"
    "s_mov_b32 s64, %[sp1]\n\t"
    "buffer_load_dwordx4 a[64:67], %[vo], %[ws], s64 offen sc1\n\t"
    "buffer_load_dwordx4 a[68:71], %[vo], %[ws], s64 offen offset:1024 sc1\n\t"
    "s_add_u32 s64, s64, 2359296\n\t"
    "buffer_load_dwordx4 a[72:75], %[vo], %[ws], s64 offen sc1\n\t"
    "buffer_load_dwordx4 a[76:79], %[vo], %[ws], s64 offen offset:1024 sc1\n\t"
    "s_add_u32 s64, s64, 2359296\n\t"
    "buffer_load_dwordx4 a[80:83], %[vo], %[ws], s64 offen sc1\n\t"
    "buffer_load_dwordx4 a[84:87], %[vo], %[ws], s64 offen offset:1024 sc1\n\t"
    "s_add_u32 s64, s64, 2359296\n\t"
    "buffer_load_dwordx4 a[88:91], %[vo], %[ws], s64 offen sc1\n\t"
    "buffer_load_dwordx4 a[92:95], %[vo], %[ws], s64 offen offset:1024 sc1\n\t"
    "s_add_u32 s64, s64, 2359296\n\t"
    "buffer_load_dwordx4 a[96:99], %[vo], %[ws], s64 offen sc1\n\t"
    "buffer_load_dwordx4 a[100:103], %[vo], %[ws], s64 offen offset:1024 sc1\n\t"
    "s_add_u32 s64, s64, 2359296\n\t"
    "buffer_load_dwordx4 a[104:107], %[vo], %[ws], s64 offen sc1\n\t"
    "buffer_load_dwordx4 a[108:111], %[vo], %[ws], s64 offen offset:1024 sc1\n\t"
    "s_add_u32 s64, s64, 2359296\n\t"
    "buffer_load_dwordx4 a[112:115], %[vo], %[ws], s64 offen sc1\n\t"
    "buffer_load_dwordx4 a[116:119], %[vo], %[ws], s64 offen offset:1024 sc1\n\t"
    "s_add_u32 s64, s64, 2359296\n\t"
    "buffer_load_dwordx4 a[120:123], %[vo], %[ws], s64 offen sc1\n\t"
    "buffer_load_dwordx4 a[124:127], %[vo], %[ws], s64 offen offset:1024 sc1\n\t"
    "s_mov_b32 s64, %[sp2]\n\t"
    "buffer_load_dwordx4 a[128:131], %[vo2], %[ws], s64 offen sc1\n\t"
    "buffer_load_dwordx4 a[132:135], %[vo2], %[ws], s64 offen offset:1024 sc1\n\t"
    "s_add_u32 s64, s64, 2359296\n\t"
    "buffer_load_dwordx4 a[136:139], %[vo2], %[ws], s64 offen sc1\n\t"
    "buffer_load_dwordx4 a[140:143], %[vo2], %[ws], s64 offen offset:1024 sc1\n\t"
    "s_add_u32 s64, s64, 2359296\n\t"
    "buffer_load_dwordx4 a[144:147], %[vo2], %[ws], s64 offen sc1\n\t"
    "buffer_load_dwordx4 a[148:151], %[vo2], %[ws], s64 offen offset:1024 sc1\n\t"
    "s_add_u32 s64, s64, 2359296\n\t"
    "buffer_load_dwordx4 a[152:155], %[vo2], %[ws], s64 offen sc1\n\t"
    "buffer_load_dwordx4 a[156:159], %[vo2], %[ws], s64 offen offset:1024 sc1\n\t"
    "s_add_u32 s64, s64, 2359296\n\t"
    "buffer_load_dwordx4 a[160:163], %[vo2], %[ws], s64 offen sc1\n\t"
    "buffer_load_dwordx4 a[164:167], %[vo2], %[ws], s64 offen offset:1024 sc1\n\t"
    "s_add_u32 s64, s64, 2359296\n\t"
    "buffer_load_dwordx4 a[168:171], %[vo2], %[ws], s64 offen sc1\n\t"
    "buffer_load_dwordx4 a[172:175], %[vo2], %[ws], s64 offen offset:1024 sc1\n\t"
    "s_add_u32 s64, s64, 2359296\n\t"
    "buffer_load_dwordx4 a[176:179], %[vo2], %[ws], s64 offen sc1\n\t"
    "buffer_load_dwordx4 a[180:183], %[vo2], %[ws], s64 offen offset:1024 sc1\n\t"
    "s_add_u32 s64, s64, 2359296\n\t"
    "buffer_load_dwordx4 a[184:187], %[vo2], %[ws], s64 offen sc1\n\t"
    "buffer_load_dwordx4 a[188:191], %[vo2], %[ws], s64 offen offset:1024 sc1\n\t"
    "s_waitcnt vmcnt(46)\n\t"
    "v_mfma_f32_32x32x16_bf16 v[128:143], %[id0], a[0:3], 0\n\t"
    "v_mfma_f32_32x32x16_bf16 v[128:143], %[id1], a[4:7], v[128:143]\n\t"
    "s_waitcnt vmcnt(44)\n\t"
    "v_mfma_f32_32x32x16_bf16 v[128:143], %[id0], a[8:11], v[128:143]\n\t"
    "v_mfma_f32_32x32x16_bf16 v[128:143], %[id1], a[12:15], v[128:143]\n\t"
    "s_waitcnt vmcnt(42)\n\t"
    "v_mfma_f32_32x32x16_bf16 v[128:143], %[id0], a[16:19], v[128:143]\n\t"
    "v_mfma_f32_32x32x16_bf16 v[128:143], %[id1], a[20:23], v[128:143]\n\t"
    "s_waitcnt vmcnt(40)\n\t"
    "v_mfma_f32_32x32x16_bf16 v[128:143], %[id0], a[24:27], v[128:143]\n\t"
    "v_mfma_f32_32x32x16_bf16 v[128:143], %[id1], a[28:31], v[128:143]\n\t"
    "s_waitcnt vmcnt(38)\n\t"
    "v_mfma_f32_32x32x16_bf16 v[128:143], %[id0], a[32:35], v[128:143]\n\t"
    "v_mfma_f32_32x32x16_bf16 v[128:143], %[id1], a[36:39], v[128:143]\n\t"
    "s_waitcnt vmcnt(36)\n\t"
    "v_mfma_f32_32x32x16_bf16 v[128:143], %[id0], a[40:43], v[128:143]\n\t"
    "v_mfma_f32_32x32x16_bf16 v[128:143], %[id1], a[44:47], v[128:143]\n\t"
    "s_waitcnt vmcnt(34)\n\t"
    "v_mfma_f32_32x32x16_bf16 v[128:143], %[id0], a[48:51], v[128:143]\n\t"
    "v_mfma_f32_32x32x16_bf16 v[128:143], %[id1], a[52:55], v[128:143]\n\t"
    "s_waitcnt vmcnt(32)\n\t"
    "v_mfma_f32_32x32x16_bf16 v[128:143], %[id0], a[56:59], v[128:143]\n\t"
    "v_mfma_f32_32x32x16_bf16 v[128:143], %[id1], a[60:63], v[128:143]\n\t"
    "s_waitcnt vmcnt(30)\n\t"
    "v_mfma_f32_32x32x16_bf16 v[144:159], %[id0], a[64:67], 0\n\t"
    "v_mfma_f32_32x32x16_bf16 v[144:159], %[id1], a[68:71], v[144:159]\n\t"
    "s_waitcnt vmcnt(28)\n\t"
    "v_mfma_f32_32x32x16_bf16 v[144:159], %[id0], a[72:75], v[144:159]\n\t"
    "v_mfma_f32_32x32x16_bf16 v[144:159], %[id1], a[76:79], v[144:159]\n\t"
    "s_waitcnt vmcnt(26)\n\t"
    "v_mfma_f32_32x32x16_bf16 v[144:159], %[id0], a[80:83], v[144:159]\n\t"
    "v_mfma_f32_32x32x16_bf16 v[144:159], %[id1], a[84:87], v[144:159]\n\t"
    "s_waitcnt vmcnt(24)\n\t"
    "v_mfma_f32_32x32x16_bf16 v[144:159], %[id0], a[88:91], v[144:159]\n\t"
    "v_mfma_f32_32x32x16_bf16 v[144:159], %[id1], a[92:95], v[144:159]\n\t"
    "s_waitcnt vmcnt(22)\n\t"
    "v_mfma_f32_32x32x16_bf16 v[144:159], %[id0], a[96:99], v[144:159]\n\t"
    "v_mfma_f32_32x32x16_bf16 v[144:159], %[id1], a[100:103], v[144:159]\n\t"
    "s_waitcnt vmcnt(20)\n\t"
    "v_mfma_f32_32x32x16_bf16 v[144:159], %[id0], a[104:107], v[144:159]\n\t"
    "v_mfma_f32_32x32x16_bf16 v[144:159], %[id1], a[108:111], v[144:159]\n\t"
    "s_waitcnt vmcnt(18)\n\t"
    "v_mfma_f32_32x32x16_bf16 v[144:159], %[id0], a[112:115], v[144:159]\n\t"
    "v_mfma_f32_32x32x16_bf16 v[144:159], %[id1], a[116:119], v[144:159]\n\t"
    "s_waitcnt vmcnt(16)\n\t"
    "v_mfma_f32_32x32x16_bf16 v[144:159], %[id0], a[120:123], v[144:159]\n\t"
    "v_mfma_f32_32x32x16_bf16 v[144:159], %[id1], a[124:127], v[144:159]\n\t"
    "s_waitcnt vmcnt(14)\n\t"
    "v_mfma_f32_32x32x16_bf16 v[160:175], %[id0], a[128:131], 0\n\t"
    "v_mfma_f32_32x32x16_bf16 v[160:175], %[id1], a[132:135], v[160:175]\n\t"
    "s_waitcnt vmcnt(12)\n\t"
    "v_mfma_f32_32x32x16_bf16 v[160:175], %[id0], a[136:139], v[160:175]\n\t"
    "v_mfma_f32_32x32x16_bf16 v[160:175], %[id1], a[140:143], v[160:175]\n\t"
    "s_waitcnt vmcnt(10)\n\t"
    "v_mfma_f32_32x32x16_bf16 v[160:175], %[id0], a[144:147], v[160:175]\n\t"
    "v_mfma_f32_32x32x16_bf16 v[160:175], %[id1], a[148:151], v[160:175]\n\t"
    "s_waitcnt vmcnt(8)\n\t"
    "v_mfma_f32_32x32x16_bf16 v[160:175], %[id0], a[152:155], v[160:175]\n\t"
    "v_mfma_f32_32x32x16_bf16 v[160:175], %[id1], a[156:159], v[160:175]\n\t"
    "s_waitcnt vmcnt(6)\n\t"
    "v_mfma_f32_32x32x16_bf16 v[160:175], %[id0], a[160:163], v[160:175]\n\t"
    "v_mfma_f32_32x32x16_bf16 v[160:175], %[id1], a[164:167], v[160:175]\n\t"
    "s_waitcnt vmcnt(4)\n\t"
    "v_mfma_f32_32x32x16_bf16 v[160:175], %[id0], a[168:171], v[160:175]\n\t"
    "v_mfma_f32_32x32x16_bf16 v[160:175], %[id1], a[172:175], v[160:175]\n\t"
    "s_waitcnt vmcnt(2)\n\t"
    "v_mfma_f32_32x32x16_bf16 v[160:175], %[id0], a[176:179], v[160:175]\n\t"
    "v_mfma_f32_32x32x16_bf16 v[160:175], %[id1], a[180:183], v[160:175]\n\t"
    "s_waitcnt vmcnt(0)\n\t"
    "v_mfma_f32_32x32x16_bf16 v[160:175], %[id0], a[184:187], v[160:175]\n\t"
    "v_mfma_f32_32x32x16_bf16 v[160:175], %[id1], a[188:191], v[160:175]\n\t"
    "s_nop 15\n\t"
    "s_nop 3\n\t"
          : "={v[128:143]}"(yp[0]), "={v[144:159]}"(yp[1]), "={v[160:175]}"(yp[2])
          : [vo] "v"(voff16), [vo2] "v"(vo3[2]), [ws] "s"(ws), [id0] "v"(id0), [id1] "v"(id1), [sp0] "s"(sp + db[0] * 2048), [sp1] "s"(sp + db[1] * 2048), [sp2] "s"(sp + db[2] * 2048)
          : "memory", "scc", "s64", "a0", "a1", "a2", "a3", "a4", "a5", "a6", "a7", "a8", "a9", "a10", "a11", "a12", "a13", "a14", "a15", "a16", "a17", "a18", "a19", "a20", "a21", "a22", "a23", "a24", "a25", "a26", "a27", "a28", "a29", "a30", "a31", "a32", "a33", "a34", "a35", "a36", "a37", "a38", "a39", "a40", "a41", "a42", "a43", "a44", "a45", "a46", "a47", "a48", "a49", "a50", "a51", "a52", "a53", "a54", "a55", "a56", "a57", "a58", "a59", "a60", "a61", "a62", "a63", "a64", "a65", "a66", "a67", "a68", "a69", "a70", "a71", "a72", "a73", "a74", "a75", "a76", "a77", "a78", "a79", "a80", "a81", "a82", "a83", "a84", "a85", "a86", "a87", "a88", "a89", "a90", "a91", "a92", "a93", "a94", "a95", "a96", "a97", "a98", "a99", "a100", "a101", "a102", "a103", "a104", "a105", "a106", "a107", "a108", "a109", "a110", "a111", "a112", "a113", "a114", "a115", "a116", "a117", "a118", "a119", "a120", "a121", "a122", "a123", "a124", "a125", "a126", "a127", "a128", "a129", "a130", "a131", "a132", "a133", "a134", "a135", "a136", "a137", "a138", "a139", "a140", "a141", "a142", "a143", "a144", "a145", "a146", "a147", "a148", "a149", "a150", "a151", "a152", "a153", "a154", "a155", "a156", "a157", "a158", "a159", "a160", "a161", "a162", "a163", "a164", "a165", "a166", "a167", "a168", "a169", "a170", "a171", "a172", "a173", "a174", "a175", "a176", "a177", "a178", "a179", "a180", "a181", "a182", "a183", "a184", "a185", "a186", "a187", "a188", "a189", "a190", "a191");
#pragma unroll
      for (int u = 0; u < 3; u++)
#pragma unroll
        for (int e = 0; e < 16; e++) {
          const unsigned xw = e < 8 ? xa[u][e >> 1] : xb[u][(e - 8) >> 1];
          const float yy = __builtin_fmaf(yp[u][e], rs1, (e & 1) ? bfhi(xw) : bflo(xw));
          y[u][e] = yy * frcp(1.f + __builtin_amdgcn_exp2f(-1.4426950408889634f * yy));
        }
      STAMP(7);
    } else {
      await_wg(ws, ldsb, FL_O + (tt >> 2) * 32 + lane, lane < 32, tag, w);
      STAMP(10);
      XRegs X;
      unsigned ab[2];
      stage_bases(ldsb, lane, ab);
      mm01_load(X, ws, voff16, WS_OF + tb * 36864, ab[0], ab[1], acc[0], acc[1]);
      if (extra) { f32x16 t; mm0(X, ab[c8 >> 2] + 2 * ST_B, t); acc[2] = t; }
      STAMP(11);
    }
  }
#pragma unroll
  for (int u = 0; u < 3; u++)
#pragma unroll
    for (int q = 0; q < 4; q++) {
      const float xf[4] = {bflo(xv[u][q][0]), bfhi(xv[u][q][0]), bflo(xv[u][q][1]), bfhi(xv[u][q][1])};
      float r[4];
#pragma unroll
      for (int e = 0; e < 4; e++) r[e] = __builtin_fmaf(acc[u][4 * q + e], y[u][4 * q + e], xf[e]);
      __builtin_amdgcn_raw_buffer_store_b64(u32x2{cvtpk(r[0], r[1]), cvtpk(r[2], r[3])}, outs, xo3[u] + 16 * q, db[u] * 64, 16);
    }
}

__global__ __launch_bounds__(256, 1) void k_all(Params P) {
  extern __shared__ __attribute__((aligned(16))) char lds[];
  const int tid = threadIdx.x, lane = tid & 63, w = __builtin_amdgcn_readfirstlane(tid >> 6);
  const int b = blockIdx.x, xcd = b & 7, i = b >> 3;
  const unsigned ldsb = (unsigned)(uintptr_t)lds;
  unsigned* flags = (unsigned*)(P.ws + WS_FLAG);
  const srd_t ws = mk_srd(P.ws, WS_END);
  STAMP(0);
  {
    unsigned long long pc; unsigned len;
    asm volatile("s_getpc_b64 %0\n.Lk_begin:\n\ts_mov_b32 %1, .Lk_end-.Lk_begin" : "=s"(pc), "=s"(len));
    const srd_t cs = mk_srd((const void*)pc, len < 131072u ? len : 0u);
    const unsigned o = (w * 64 + lane) * 128;
    touch(o, cs, ldsb + LDS_CTRL); touch(o + 32768, cs, ldsb + LDS_CTRL); touch(o + 65536, cs, ldsb + LDS_CTRL);
    if (w == 3) {
      const unsigned L = 54 * i + lane, sel = L >= 1152 ? 2 : L >= 576 ? 1 : 0, row = L - 576 * sel;
      touch(lane < 54 ? row * 1152 + (sel == 2 ? 8 : 2 * (xcd >> 1) + sel) * 128 : OOB, mk_srd(P.w2, D * D * 2), ldsb + LDS_CTRL);
    }
    const unsigned po = (i + 32 * lane) * 65536u;
    if (w == 0) { touch(po, ws, ldsb + LDS_CTRL); touch(po, mk_srd(P.w2, D * D * 2), ldsb + LDS_CTRL); }
    else if (w == 1) { touch(po, mk_srd(P.w3, D * FF * 2), ldsb + LDS_CTRL); touch(po, mk_srd(P.wo, D * D * 2), ldsb + LDS_CTRL); }
    else if (w == 2) { touch(po, mk_srd(P.w4, D * FF * 2), ldsb + LDS_CTRL); touch(po, mk_srd(P.out, S * D * 2), ldsb + LDS_CTRL); }
    else { touch(po, mk_srd(P.wqkv, 3 * D * D * 2), ldsb + LDS_CTRL); touch(po, mk_srd(P.w1, D * D * 2), ldsb + LDS_CTRL); touch(po, mk_srd(P.x, S * D * 2), ldsb + LDS_CTRL); }
  }
  unsigned epoch;
  asm volatile("buffer_load_dword %0, %1, %2, 0 offen sc1" : "=v"(epoch) : "v"(WS_FLAG + FL_EPOCH * 4), "s"(ws));
  int f1idx;
  float mu_x, rs_x;
  p1_phase(P, lds, ldsb, ws, xcd, i, w, lane, f1idx, mu_x, rs_x);
  pin_v(epoch);
  const unsigned tag = __builtin_amdgcn_readfirstlane(epoch) + 1u;
  if (i >= 16) {
    const int j = i - 16, mtt = (xcd & 1) * 8 + (j & 7), mc8 = (xcd >> 1) * 2 + (j >> 3);
    publish(flags + FL_P1 + f1idx, tag, w);
    stage4<false>(mk_srd(P.w2, D * D * 2), ldsb, mc8, w, lane_id());
    STAMP(2);
    mlp_phase(P, lds, ldsb, ws, tag, mtt, mc8, w, mu_x, rs_x);
    STAMP(9);
  } else {
    publish(flags + FL_P1 + f1idx, tag, w);
    STAMP(2);
    const int head = xcd * 4 + (i >> 2), qr = i & 3;
    attn_phase(P, lds, ldsb, ws, tag, head, qr, w);
    publish(flags + FL_O + qr * 32 + head, tag, w);
    stage4<true>(mk_srd(P.wo, D * D * 2), ldsb, head & 7, w, lane_id());
    STAMP(9);
    fin_phase(P, lds, ldsb, ws, tag, qr * 4 + (head >> 3), head & 7, w);
  }
  STAMP(12);
  if (b == 0 && w == 0) {
    const int lane = lane_id();
#pragma unroll
    for (int k = 0; k < 4; k++) await(flags, FL_P1 + 64 * k + lane, true, tag);
    if (lane == 0) __hip_atomic_store(flags + FL_EPOCH, tag, __ATOMIC_RELAXED, __HIP_MEMORY_SCOPE_AGENT);
  }
  asm volatile(".Lk_end:");
}

extern "C" void kernel_launch(void* const* d_in, const int* in_sizes, int n_in, void* d_out, int out_size, void* d_ws, size_t ws_size, hipStream_t stream) {
  Params p;
  p.x = (const unsigned short*)d_in[0];
  p.wqkv = (const unsigned short*)d_in[1];
  p.wo = (const unsigned short*)d_in[2];
  p.w1 = (const unsigned short*)d_in[4];
  p.w2 = (const unsigned short*)d_in[5];
  p.w3 = (const unsigned short*)d_in[6];
  p.w4 = (const unsigned short*)d_in[7];
  p.ws = (char*)d_ws;
  p.out = (unsigned short*)d_out;
  (void)hipFuncSetAttribute((const void*)k_all, hipFuncAttributeMaxDynamicSharedMemorySize, LDS_TOTAL);
  k_all<<<256, 256, LDS_TOTAL, stream>>>(p);
}
```
